```python
import torch

def _softmax(x, axis=-1):
    return torch.softmax(x, dim=axis)
def _gelu(x):
    return torch.nn.functional.gelu(x, approximate="tanh")
def _silu(x):
    return torch.nn.functional.silu(x)
def _sigmoid(x):
    return torch.sigmoid(x)
def _layernorm(x, axis=-1, eps=1e-5):
    m = x.mean(dim=axis, keepdim=True)
    v = x.var(dim=axis, keepdim=True, unbiased=False)
    return (x - m) / torch.sqrt(v + eps)
def _scatter_add(tgt, idx, src, axis=0):
    return tgt.clone().index_add_(axis, idx, src)
def _segment_sum(src, seg, n):
    out = torch.zeros((n,) + src.shape[1:], dtype=src.dtype)
    return out.index_add_(0, seg, src)
def _pad(x, pw):
    flat = tuple(v for pair in reversed(pw) for v in pair)
    return torch.nn.functional.pad(x, flat)

kernel_name = 'recurrent_scan__mha_block_cached__mixer_channel_mlp__prompt'

def setup_inputs():

    _w = (lambda a: (lambda t: t.to(torch.bfloat16) if t.is_floating_point() else t)(torch.as_tensor(a)))
    def _A_setup():
        B, Sq, D = 1, 2048, 512
        H, Sk, Dh = 32, 2048, 128
        return {
            "x": _w((lambda *s: torch.randn(*s, dtype=torch.bfloat16) * 0.05)(B, Sq, D)),
            "k_cache": _w((lambda *s: torch.randn(*s, dtype=torch.bfloat16) * 1.6)(B, H, Sk, Dh)),
            "v_cache": _w((lambda *s: torch.randn(*s, dtype=torch.bfloat16) * 0.8)(B, H, Sk, Dh)),
            "wq": _w((lambda *s: torch.randn(*s, dtype=torch.bfloat16) * 1.6)(D, H * Dh) * (D ** -0.5)),
            "wo": _w((lambda *s: torch.randn(*s, dtype=torch.bfloat16) * 0.8)(H * Dh, D) * ((H * Dh) ** -0.5)),
        }
    _A_in = _A_setup()

    def _B_setup():
        B, S, D, F = 1, 2048, 512, 1792
        return {
            "x": _w((lambda *s: torch.randn(*s, dtype=torch.bfloat16) * 0.05)(B, S, D)),
            "w1": _w((lambda *s: torch.randn(*s, dtype=torch.bfloat16) * 1.6)(D, F) * (D ** -0.5)),
            "b1": _w((lambda *s: torch.randn(*s, dtype=torch.bfloat16) * 0.8)(F)),
            "w2": _w((lambda *s: torch.randn(*s, dtype=torch.bfloat16) * 0.2)(F, D) * (F ** -0.5)),
            "b2": _w((lambda *s: torch.randn(*s, dtype=torch.bfloat16) * 0.2)(D)),
        }
    _B_in = _B_setup()

    return {
        "A_x": _A_in["x"],
        "A_k_cache": _A_in["k_cache"],
        "A_v_cache": _A_in["v_cache"],
        "A_wq": _A_in["wq"],
        "A_wo": _A_in["wo"],
        "B_x": _B_in["x"],
        "B_w1": _B_in["w1"],
        "B_b1": _B_in["b1"],
        "B_w2": _B_in["w2"],
        "B_b2": _B_in["b2"],
    }

def reference(A_x, A_k_cache, A_v_cache, A_wq, A_wo, B_x, B_w1, B_b1, B_w2, B_b2):
    def _A_ref(x, k_cache, v_cache, wq, wo):
        B, Sq, D = x.shape
        H, Dh = 32, 128
        q = (lambda x, *d: x.permute(*d))((x @ wq).reshape(B, Sq, H, Dh), 0, 2, 1, 3)
        scores = torch.einsum("bhqd,bhkd->bhqk", q, k_cache) * (Dh ** -0.5)
        p = _softmax(scores, axis=-1)
        o = torch.einsum("bhqk,bhkd->bhqd", p, v_cache)
        return (lambda x, *d: x.permute(*d))(o, 0, 2, 1, 3).reshape(B, Sq, H * Dh) @ wo

    def _B_ref(x, w1, b1, w2, b2):
        y = _layernorm(x, axis=-1)
        h = _gelu(torch.einsum("bsd,df->bsf", y, w1) + b1)
        return torch.einsum("bsf,fd->bsd", h, w2) + b2

    x = A_x
    xn = _layernorm(x, axis=-1)
    s = _A_ref(xn, A_k_cache, A_v_cache, A_wq, A_wo)
    g = _silu(_B_ref(xn, B_w1, B_b1, B_w2, B_b2))
    return x + s * g
```

```cpp
#include <hip/hip_runtime.h>
#include <hip/hip_bf16.h>
#include <stdint.h>

typedef __attribute__((ext_vector_type(8))) short bf16x8;
typedef __attribute__((ext_vector_type(16))) float f32x16;
typedef __attribute__((ext_vector_type(4))) float f32x4;
typedef unsigned short u16;

__device__ __forceinline__ float bf2f(u16 v) { return __uint_as_float(((unsigned)v) << 16); }
__device__ __forceinline__ u16 f2bf(float f) { __hip_bfloat16 b = __float2bfloat16(f); return *reinterpret_cast<u16*>(&b); }

constexpr int S = 2048, DM = 512, H = 32, DH = 128, F = 1792, HD = H * DH;

__global__ __launch_bounds__(256) void ln_kernel(const u16* __restrict__ x, u16* __restrict__ xn, u16* __restrict__ y) {
  int row = blockIdx.x * 4 + (threadIdx.x >> 6), lane = threadIdx.x & 63;
  bf16x8 v = *reinterpret_cast<const bf16x8*>(x + row * DM + lane * 8);
  float f[8]; float s = 0;
  for (int i = 0; i < 8; ++i) { f[i] = bf2f((u16)v[i]); s += f[i]; }
  for (int o = 32; o; o >>= 1) s += __shfl_xor(s, o);
  float m = s * (1.f / DM), q = 0;
  for (int i = 0; i < 8; ++i) { float d = f[i] - m; q += d * d; }
  for (int o = 32; o; o >>= 1) q += __shfl_xor(q, o);
  float r = 1.f / sqrtf(q * (1.f / DM) + 1e-5f);
  bf16x8 o1; s = 0;
  for (int i = 0; i < 8; ++i) { u16 b = f2bf((f[i] - m) * r); o1[i] = (short)b; f[i] = bf2f(b); s += f[i]; }
  *reinterpret_cast<bf16x8*>(xn + row * DM + lane * 8) = o1;
  for (int o = 32; o; o >>= 1) s += __shfl_xor(s, o);
  m = s * (1.f / DM); q = 0;
  for (int i = 0; i < 8; ++i) { float d = f[i] - m; q += d * d; }
  for (int o = 32; o; o >>= 1) q += __shfl_xor(q, o);
  r = 1.f / sqrtf(q * (1.f / DM) + 1e-5f);
  for (int i = 0; i < 8; ++i) o1[i] = (short)f2bf((f[i] - m) * r);
  *reinterpret_cast<bf16x8*>(y + row * DM + lane * 8) = o1;
}

template <int MODE>
__global__ __launch_bounds__(256) void gemm_simple(const u16* __restrict__ A, const u16* __restrict__ B, const u16* __restrict__ bias,
                                                   void* __restrict__ C, int M, int N, int K) {
  int wid = threadIdx.x >> 6, lane = threadIdx.x & 63, r = lane & 31, h = lane >> 5;
  int m0 = blockIdx.y * 64 + (wid >> 1) * 32, n0 = blockIdx.x * 64 + (wid & 1) * 32;
  f32x16 acc = {};
  for (int k = 0; k < K; k += 16) {
    bf16x8 a = *reinterpret_cast<const bf16x8*>(A + (long)(m0 + r) * K + k + 8 * h);
    bf16x8 b;
    for (int j = 0; j < 8; ++j) b[j] = (short)B[(long)(k + 8 * h + j) * N + n0 + r];
    acc = __builtin_amdgcn_mfma_f32_32x32x16_bf16(a, b, acc, 0, 0, 0);
  }
  for (int i = 0; i < 16; ++i) {
    int row = m0 + (i & 3) + 8 * (i >> 2) + 4 * h, col = n0 + r;
    float v = acc[i];
    if (MODE == 0) ((u16*)C)[(long)row * N + col] = f2bf(v);
    if (MODE == 1) { v += bf2f(bias[col]); float t = 0.7978845608f * (v + 0.044715f * v * v * v);
      float g = 0.5f * v * (1.f + tanhf(t)); ((u16*)C)[(long)row * N + col] = f2bf(g); }
    if (MODE == 2) ((float*)C)[(long)row * N + col] = v;
  }
}

__global__ __launch_bounds__(256) void attn_simple(const u16* __restrict__ q, const u16* __restrict__ k, const u16* __restrict__ v, u16* __restrict__ o) {
  int wid = threadIdx.x >> 6, lane = threadIdx.x & 63, r = lane & 31, hi = lane >> 5;
  int h = blockIdx.y, q0 = blockIdx.x * 128 + wid * 32;
  bf16x8 qf[8];
  for (int d = 0; d < 8; ++d) qf[d] = *reinterpret_cast<const bf16x8*>(q + (long)(q0 + r) * HD + h * DH + d * 16 + hi * 8);
  const u16* K = k + (long)h * S * DH; const u16* V = v + (long)h * S * DH;
  f32x16 acc[4] = {}; float m = -1e30f, l = 0;
  const float C = 0.088388347648318440f * 1.4426950408889634f;
  for (int t = 0; t < S; t += 32) {
    f32x16 s = {};
    for (int d = 0; d < 8; ++d) {
      bf16x8 a = *reinterpret_cast<const bf16x8*>(K + (long)(t + r) * DH + d * 16 + hi * 8);
      s = __builtin_amdgcn_mfma_f32_32x32x16_bf16(a, qf[d], s, 0, 0, 0);
    }
    float mx = s[0]; for (int i = 1; i < 16; ++i) mx = fmaxf(mx, s[i]);
    mx = fmaxf(mx, __shfl_xor(mx, 32));
    float mn = fmaxf(m, mx), al = exp2f((m - mn) * C); m = mn;
    float ps = 0; float p[16];
    for (int i = 0; i < 16; ++i) { p[i] = exp2f((s[i] - mn) * C); ps += p[i]; }
    ps += __shfl_xor(ps, 32);
    l = l * al + ps;
    for (int d = 0; d < 4; ++d) for (int i = 0; i < 16; ++i) acc[d][i] *= al;
    for (int ks = 0; ks < 2; ++ks) {
      bf16x8 pb; for (int j = 0; j < 8; ++j) pb[j] = (short)f2bf(p[8 * ks + j]);
      for (int d = 0; d < 4; ++d) {
        bf16x8 a;
        for (int j = 0; j < 8; ++j) a[j] = (short)V[(long)(t + 16 * ks + 8 * (j >> 2) + 4 * hi + (j & 3)) * DH + d * 32 + r];
        acc[d] = __builtin_amdgcn_mfma_f32_32x32x16_bf16(a, pb, acc[d], 0, 0, 0);
      }
    }
  }
  float il = 1.f / l;
  for (int d = 0; d < 4; ++d) for (int i = 0; i < 16; ++i) {
    int dd = d * 32 + (i & 3) + 8 * (i >> 2) + 4 * hi;
    o[(long)(q0 + r) * HD + h * DH + dd] = f2bf(acc[d][i] * il);
  }
}

__global__ __launch_bounds__(256) void final_kernel(const u16* __restrict__ x, const float* __restrict__ s, const float* __restrict__ g, const u16* __restrict__ b2, u16* __restrict__ out) {
  int i = blockIdx.x * 256 + threadIdx.x;
  float z = g[i] + bf2f(b2[i & (DM - 1)]);
  float sg = z / (1.f + __expf(-z));
  out[i] = f2bf(bf2f(x[i]) + s[i] * sg);
}

extern "C" void kernel_launch(void* const* d_in, const int* in_sizes, int n_in, void* d_out, int out_size, void* d_ws, size_t ws_size, hipStream_t stream) {
  const u16* x = (const u16*)d_in[0]; const u16* kc = (const u16*)d_in[1]; const u16* vc = (const u16*)d_in[2];
  const u16* wq = (const u16*)d_in[3]; const u16* wo = (const u16*)d_in[4];
  const u16* w1 = (const u16*)d_in[6]; const u16* b1 = (const u16*)d_in[7]; const u16* w2 = (const u16*)d_in[8]; const u16* b2 = (const u16*)d_in[9];
  char* ws = (char*)d_ws;
  u16* xn = (u16*)ws; u16* y = (u16*)(ws + (2 << 20)); u16* q = (u16*)(ws + (4 << 20)); u16* o = (u16*)(ws + (24 << 20));
  u16* hbuf = (u16*)(ws + (44 << 20)); float* s = (float*)(ws + (56 << 20)); float* g = (float*)(ws + (64 << 20));
  ln_kernel<<<S / 4, 256, 0, stream>>>(x, xn, y);
  gemm_simple<0><<<dim3(HD / 64, S / 64), 256, 0, stream>>>(xn, wq, nullptr, q, S, HD, DM);
  gemm_simple<1><<<dim3(F / 64, S / 64), 256, 0, stream>>>(y, w1, b1, hbuf, S, F, DM);
  attn_simple<<<dim3(S / 128, H), 256, 0, stream>>>(q, kc, vc, o);
  gemm_simple<2><<<dim3(DM / 64, S / 64), 256, 0, stream>>>(o, wo, nullptr, s, S, DM, HD);
  gemm_simple<2><<<dim3(DM / 64, S / 64), 256, 0, stream>>>(hbuf, w2, nullptr, g, S, DM, F);
  final_kernel<<<S * DM / 256, 256, 0, stream>>>(x, s, g, b2, (u16*)d_out);
}
```

```cpp
#include <hip/hip_runtime.h>
#include <hip/hip_bf16.h>
#include <stdint.h>

typedef __attribute__((ext_vector_type(8))) short bf16x8;
typedef __attribute__((ext_vector_type(16))) float f32x16;
typedef __attribute__((ext_vector_type(4))) float f32x4;
typedef unsigned short u16;

__device__ __forceinline__ float bf2f(u16 v) { return __uint_as_float(((unsigned)v) << 16); }
__device__ __forceinline__ u16 f2bf(float f) { __hip_bfloat16 b = __float2bfloat16(f); return *reinterpret_cast<u16*>(&b); }

constexpr int S = 2048, DM = 512, H = 32, DH = 128, F = 1792, HD = H * DH;

__global__ __launch_bounds__(256) void ln_kernel(const u16* __restrict__ x, u16* __restrict__ xn, u16* __restrict__ y) {
  int row = blockIdx.x * 4 + (threadIdx.x >> 6), lane = threadIdx.x & 63;
  bf16x8 v = *reinterpret_cast<const bf16x8*>(x + row * DM + lane * 8);
  float f[8]; float s = 0;
  for (int i = 0; i < 8; ++i) { f[i] = bf2f((u16)v[i]); s += f[i]; }
  for (int o = 32; o; o >>= 1) s += __shfl_xor(s, o);
  float m = s * (1.f / DM), q = 0;
  for (int i = 0; i < 8; ++i) { float d = f[i] - m; q += d * d; }
  for (int o = 32; o; o >>= 1) q += __shfl_xor(q, o);
  float r = 1.f / sqrtf(q * (1.f / DM) + 1e-5f);
  bf16x8 o1; s = 0;
  for (int i = 0; i < 8; ++i) { u16 b = f2bf((f[i] - m) * r); o1[i] = (short)b; f[i] = bf2f(b); s += f[i]; }
  *reinterpret_cast<bf16x8*>(xn + row * DM + lane * 8) = o1;
  for (int o = 32; o; o >>= 1) s += __shfl_xor(s, o);
  m = s * (1.f / DM); q = 0;
  for (int i = 0; i < 8; ++i) { float d = f[i] - m; q += d * d; }
  for (int o = 32; o; o >>= 1) q += __shfl_xor(q, o);
  r = 1.f / sqrtf(q * (1.f / DM) + 1e-5f);
  for (int i = 0; i < 8; ++i) o1[i] = (short)f2bf((f[i] - m) * r);
  *reinterpret_cast<bf16x8*>(y + row * DM + lane * 8) = o1;
}

template <int MODE>
__global__ __launch_bounds__(256) void gemm_simple(const u16* __restrict__ A, const u16* __restrict__ B, const u16* __restrict__ bias,
                                                   void* __restrict__ C, int M, int N, int K) {
  int wid = threadIdx.x >> 6, lane = threadIdx.x & 63, r = lane & 31, h = lane >> 5;
  int m0 = blockIdx.y * 64 + (wid >> 1) * 32, n0 = blockIdx.x * 64 + (wid & 1) * 32;
  f32x16 acc = {};
  for (int k = 0; k < K; k += 16) {
    bf16x8 a = *reinterpret_cast<const bf16x8*>(A + (long)(m0 + r) * K + k + 8 * h);
    bf16x8 b;
    for (int j = 0; j < 8; ++j) b[j] = (short)B[(long)(k + 8 * h + j) * N + n0 + r];
    acc = __builtin_amdgcn_mfma_f32_32x32x16_bf16(a, b, acc, 0, 0, 0);
  }
  for (int i = 0; i < 16; ++i) {
    int row = m0 + (i & 3) + 8 * (i >> 2) + 4 * h, col = n0 + r;
    float v = acc[i];
    if (MODE == 0) ((u16*)C)[(long)row * N + col] = f2bf(v);
    if (MODE == 1) { v += bf2f(bias[col]); float t = 0.7978845608f * (v + 0.044715f * v * v * v);
      float g = 0.5f * v * (1.f + tanhf(t)); ((u16*)C)[(long)row * N + col] = f2bf(g); }
    if (MODE == 2) ((float*)C)[(long)row * N + col] = v;
  }
}

__global__ __launch_bounds__(256) void attn_simple(const u16* __restrict__ q, const u16* __restrict__ k, const u16* __restrict__ v, u16* __restrict__ o) {
  int wid = threadIdx.x >> 6, lane = threadIdx.x & 63, r = lane & 31, hi = lane >> 5;
  int h = blockIdx.y, q0 = blockIdx.x * 128 + wid * 32;
  bf16x8 qf[8];
  for (int d = 0; d < 8; ++d) qf[d] = *reinterpret_cast<const bf16x8*>(q + (long)(q0 + r) * HD + h * DH + d * 16 + hi * 8);
  const u16* K = k + (long)h * S * DH; const u16* V = v + (long)h * S * DH;
  f32x16 acc[4] = {}; float m = -1e30f, l = 0;
  const float C = 0.088388347648318440f * 1.4426950408889634f;
  for (int t = 0; t < S; t += 32) {
    f32x16 s = {};
    for (int d = 0; d < 8; ++d) {
      bf16x8 a = *reinterpret_cast<const bf16x8*>(K + (long)(t + r) * DH + d * 16 + hi * 8);
      s = __builtin_amdgcn_mfma_f32_32x32x16_bf16(a, qf[d], s, 0, 0, 0);
    }
    float mx = s[0]; for (int i = 1; i < 16; ++i) mx = fmaxf(mx, s[i]);
    mx = fmaxf(mx, __shfl_xor(mx, 32));
    float mn = fmaxf(m, mx), al = exp2f((m - mn) * C); m = mn;
    float ps = 0; float p[16];
    for (int i = 0; i < 16; ++i) { p[i] = exp2f((s[i] - mn) * C); ps += p[i]; }
    ps += __shfl_xor(ps, 32);
    l = l * al + ps;
    for (int d = 0; d < 4; ++d) for (int i = 0; i < 16; ++i) acc[d][i] *= al;
    for (int ks = 0; ks < 2; ++ks) {
      bf16x8 pb; for (int j = 0; j < 8; ++j) pb[j] = (short)f2bf(p[8 * ks + j]);
      for (int d = 0; d < 4; ++d) {
        bf16x8 a;
        for (int j = 0; j < 8; ++j) a[j] = (short)V[(long)(t + 16 * ks + 8 * (j >> 2) + 4 * hi + (j & 3)) * DH + d * 32 + r];
        acc[d] = __builtin_amdgcn_mfma_f32_32x32x16_bf16(a, pb, acc[d], 0, 0, 0);
      }
    }
  }
  float il = 1.f / l;
  for (int d = 0; d < 4; ++d) for (int i = 0; i < 16; ++i) {
    int dd = d * 32 + (i & 3) + 8 * (i >> 2) + 4 * hi;
    o[(long)(q0 + r) * HD + h * DH + dd] = f2bf(acc[d][i] * il);
  }
}

__global__ __launch_bounds__(256) void final_kernel(const u16* __restrict__ x, const float* __restrict__ s, const float* __restrict__ g, const u16* __restrict__ b2, u16* __restrict__ out) {
  int i = blockIdx.x * 256 + threadIdx.x;
  float z = g[i] + bf2f(b2[i & (DM - 1)]);
  float sg = z / (1.f + __expf(-z));
  out[i] = f2bf(bf2f(x[i]) + s[i] * sg);
}


__global__ __launch_bounds__(256) void dummy_kernel0(float* p) {
  asm volatile("s_mov_b32 s20, 64\n\tL_d_%=:\n\t.rept 1\n\tv_add_f32_e64 v2, v40, v50\n\tv_add_f32_e64 v3, v41, v51\n\tv_add_f32_e64 v4, v42, v52\n\tv_add_f32_e64 v5, v43, v53\n\tv_add_f32_e64 v6, v44, v54\n\tv_add_f32_e64 v7, v45, v55\n\tv_add_f32_e64 v8, v46, v56\n\tv_add_f32_e64 v9, v47, v57\n\tv_add_f32_e64 v10, v40, v50\n\tv_add_f32_e64 v11, v41, v51\n\tv_add_f32_e64 v12, v42, v52\n\tv_add_f32_e64 v13, v43, v53\n\tv_add_f32_e64 v14, v44, v54\n\tv_add_f32_e64 v15, v45, v55\n\tv_add_f32_e64 v16, v46, v56\n\tv_add_f32_e64 v17, v47, v57\n\tv_add_f32_e64 v18, v40, v50\n\tv_add_f32_e64 v19, v41, v51\n\tv_add_f32_e64 v20, v42, v52\n\tv_add_f32_e64 v21, v43, v53\n\tv_add_f32_e64 v22, v44, v54\n\tv_add_f32_e64 v23, v45, v55\n\tv_add_f32_e64 v24, v46, v56\n\tv_add_f32_e64 v25, v47, v57\n\tv_add_f32_e64 v26, v40, v50\n\tv_add_f32_e64 v27, v41, v51\n\tv_add_f32_e64 v28, v42, v52\n\tv_add_f32_e64 v29, v43, v53\n\tv_add_f32_e64 v30, v44, v54\n\tv_add_f32_e64 v31, v45, v55\n\tv_add_f32_e64 v32, v46, v56\n\tv_add_f32_e64 v33, v47, v57\n\tv_add_f32_e64 v2, v40, v50\n\tv_add_f32_e64 v3, v41, v51\n\tv_add_f32_e64 v4, v42, v52\n\tv_add_f32_e64 v5, v43, v53\n\tv_add_f32_e64 v6, v44, v54\n\tv_add_f32_e64 v7, v45, v55\n\tv_add_f32_e64 v8, v46, v56\n\tv_add_f32_e64 v9, v47, v57\n\tv_add_f32_e64 v10, v40, v50\n\tv_add_f32_e64 v11, v41, v51\n\tv_add_f32_e64 v12, v42, v52\n\tv_add_f32_e64 v13, v43, v53\n\tv_add_f32_e64 v14, v44, v54\n\tv_add_f32_e64 v15, v45, v55\n\tv_add_f32_e64 v16, v46, v56\n\tv_add_f32_e64 v17, v47, v57\n\tv_add_f32_e64 v18, v40, v50\n\tv_add_f32_e64 v19, v41, v51\n\tv_add_f32_e64 v20, v42, v52\n\tv_add_f32_e64 v21, v43, v53\n\tv_add_f32_e64 v22, v44, v54\n\tv_add_f32_e64 v23, v45, v55\n\tv_add_f32_e64 v24, v46, v56\n\tv_add_f32_e64 v25, v47, v57\n\tv_add_f32_e64 v26, v40, v50\n\tv_add_f32_e64 v27, v41, v51\n\tv_add_f32_e64 v28, v42, v52\n\tv_add_f32_e64 v29, v43, v53\n\tv_add_f32_e64 v30, v44, v54\n\tv_add_f32_e64 v31, v45, v55\n\tv_add_f32_e64 v32, v46, v56\n\tv_add_f32_e64 v33, v47, v57\n\t.endr\n\ts_sub_u32 s20, s20, 1\n\ts_cmp_lg_u32 s20, 0\n\ts_cbranch_scc1 L_d_%=" ::: "s20", "scc", "v0","v1","v2","v3","v4","v5","v6","v7","v8","v9","v10","v11","v12","v13","v14","v15","v16","v17","v18","v19","v20","v21","v22","v23","v24","v25","v26","v27","v28","v29","v30","v31","v32","v33","v34","v35","v36","v37","v38","v39","v40","v41","v42","v43","v44","v45","v46","v47","v48","v49","v50","v51","v52","v53","v54","v55","v56","v57","v58","v59","v60","v61","v62","v63");
}

__global__ __launch_bounds__(256) void dummy_kernel1(float* p) {
  asm volatile("s_mov_b32 s20, 64\n\tL_d_%=:\n\t.rept 1\n\tv_add_f32_e64 v3, v40, v51\n\tv_add_f32_e64 v4, v41, v52\n\tv_add_f32_e64 v5, v42, v53\n\tv_add_f32_e64 v6, v43, v54\n\tv_add_f32_e64 v7, v44, v55\n\tv_add_f32_e64 v8, v45, v56\n\tv_add_f32_e64 v9, v46, v57\n\tv_add_f32_e64 v10, v47, v50\n\tv_add_f32_e64 v11, v40, v51\n\tv_add_f32_e64 v12, v41, v52\n\tv_add_f32_e64 v13, v42, v53\n\tv_add_f32_e64 v14, v43, v54\n\tv_add_f32_e64 v15, v44, v55\n\tv_add_f32_e64 v16, v45, v56\n\tv_add_f32_e64 v17, v46, v57\n\tv_add_f32_e64 v18, v47, v50\n\tv_add_f32_e64 v19, v40, v51\n\tv_add_f32_e64 v20, v41, v52\n\tv_add_f32_e64 v21, v42, v53\n\tv_add_f32_e64 v22, v43, v54\n\tv_add_f32_e64 v23, v44, v55\n\tv_add_f32_e64 v24, v45, v56\n\tv_add_f32_e64 v25, v46, v57\n\tv_add_f32_e64 v26, v47, v50\n\tv_add_f32_e64 v27, v40, v51\n\tv_add_f32_e64 v28, v41, v52\n\tv_add_f32_e64 v29, v42, v53\n\tv_add_f32_e64 v30, v43, v54\n\tv_add_f32_e64 v31, v44, v55\n\tv_add_f32_e64 v32, v45, v56\n\tv_add_f32_e64 v33, v46, v57\n\tv_add_f32_e64 v2, v47, v50\n\tv_add_f32_e64 v3, v40, v51\n\tv_add_f32_e64 v4, v41, v52\n\tv_add_f32_e64 v5, v42, v53\n\tv_add_f32_e64 v6, v43, v54\n\tv_add_f32_e64 v7, v44, v55\n\tv_add_f32_e64 v8, v45, v56\n\tv_add_f32_e64 v9, v46, v57\n\tv_add_f32_e64 v10, v47, v50\n\tv_add_f32_e64 v11, v40, v51\n\tv_add_f32_e64 v12, v41, v52\n\tv_add_f32_e64 v13, v42, v53\n\tv_add_f32_e64 v14, v43, v54\n\tv_add_f32_e64 v15, v44, v55\n\tv_add_f32_e64 v16, v45, v56\n\tv_add_f32_e64 v17, v46, v57\n\tv_add_f32_e64 v18, v47, v50\n\tv_add_f32_e64 v19, v40, v51\n\tv_add_f32_e64 v20, v41, v52\n\tv_add_f32_e64 v21, v42, v53\n\tv_add_f32_e64 v22, v43, v54\n\tv_add_f32_e64 v23, v44, v55\n\tv_add_f32_e64 v24, v45, v56\n\tv_add_f32_e64 v25, v46, v57\n\tv_add_f32_e64 v26, v47, v50\n\tv_add_f32_e64 v27, v40, v51\n\tv_add_f32_e64 v28, v41, v52\n\tv_add_f32_e64 v29, v42, v53\n\tv_add_f32_e64 v30, v43, v54\n\tv_add_f32_e64 v31, v44, v55\n\tv_add_f32_e64 v32, v45, v56\n\tv_add_f32_e64 v33, v46, v57\n\tv_add_f32_e64 v2, v47, v50\n\t.endr\n\ts_sub_u32 s20, s20, 1\n\ts_cmp_lg_u32 s20, 0\n\ts_cbranch_scc1 L_d_%=" ::: "s20", "scc", "v0","v1","v2","v3","v4","v5","v6","v7","v8","v9","v10","v11","v12","v13","v14","v15","v16","v17","v18","v19","v20","v21","v22","v23","v24","v25","v26","v27","v28","v29","v30","v31","v32","v33","v34","v35","v36","v37","v38","v39","v40","v41","v42","v43","v44","v45","v46","v47","v48","v49","v50","v51","v52","v53","v54","v55","v56","v57","v58","v59","v60","v61","v62","v63");
}

__global__ __launch_bounds__(256) void dummy_kernel2(float* p) {
  asm volatile("s_mov_b32 s20, 64\n\tL_d_%=:\n\t.rept 1\n\tv_add_f32_e64 v4, v40, v52\n\tv_add_f32_e64 v5, v41, v53\n\tv_add_f32_e64 v6, v42, v54\n\tv_add_f32_e64 v7, v43, v55\n\tv_add_f32_e64 v8, v44, v56\n\tv_add_f32_e64 v9, v45, v57\n\tv_add_f32_e64 v10, v46, v50\n\tv_add_f32_e64 v11, v47, v51\n\tv_add_f32_e64 v12, v40, v52\n\tv_add_f32_e64 v13, v41, v53\n\tv_add_f32_e64 v14, v42, v54\n\tv_add_f32_e64 v15, v43, v55\n\tv_add_f32_e64 v16, v44, v56\n\tv_add_f32_e64 v17, v45, v57\n\tv_add_f32_e64 v18, v46, v50\n\tv_add_f32_e64 v19, v47, v51\n\tv_add_f32_e64 v20, v40, v52\n\tv_add_f32_e64 v21, v41, v53\n\tv_add_f32_e64 v22, v42, v54\n\tv_add_f32_e64 v23, v43, v55\n\tv_add_f32_e64 v24, v44, v56\n\tv_add_f32_e64 v25, v45, v57\n\tv_add_f32_e64 v26, v46, v50\n\tv_add_f32_e64 v27, v47, v51\n\tv_add_f32_e64 v28, v40, v52\n\tv_add_f32_e64 v29, v41, v53\n\tv_add_f32_e64 v30, v42, v54\n\tv_add_f32_e64 v31, v43, v55\n\tv_add_f32_e64 v32, v44, v56\n\tv_add_f32_e64 v33, v45, v57\n\tv_add_f32_e64 v2, v46, v50\n\tv_add_f32_e64 v3, v47, v51\n\tv_add_f32_e64 v4, v40, v52\n\tv_add_f32_e64 v5, v41, v53\n\tv_add_f32_e64 v6, v42, v54\n\tv_add_f32_e64 v7, v43, v55\n\tv_add_f32_e64 v8, v44, v56\n\tv_add_f32_e64 v9, v45, v57\n\tv_add_f32_e64 v10, v46, v50\n\tv_add_f32_e64 v11, v47, v51\n\tv_add_f32_e64 v12, v40, v52\n\tv_add_f32_e64 v13, v41, v53\n\tv_add_f32_e64 v14, v42, v54\n\tv_add_f32_e64 v15, v43, v55\n\tv_add_f32_e64 v16, v44, v56\n\tv_add_f32_e64 v17, v45, v57\n\tv_add_f32_e64 v18, v46, v50\n\tv_add_f32_e64 v19, v47, v51\n\tv_add_f32_e64 v20, v40, v52\n\tv_add_f32_e64 v21, v41, v53\n\tv_add_f32_e64 v22, v42, v54\n\tv_add_f32_e64 v23, v43, v55\n\tv_add_f32_e64 v24, v44, v56\n\tv_add_f32_e64 v25, v45, v57\n\tv_add_f32_e64 v26, v46, v50\n\tv_add_f32_e64 v27, v47, v51\n\tv_add_f32_e64 v28, v40, v52\n\tv_add_f32_e64 v29, v41, v53\n\tv_add_f32_e64 v30, v42, v54\n\tv_add_f32_e64 v31, v43, v55\n\tv_add_f32_e64 v32, v44, v56\n\tv_add_f32_e64 v33, v45, v57\n\tv_add_f32_e64 v2, v46, v50\n\tv_add_f32_e64 v3, v47, v51\n\t.endr\n\ts_sub_u32 s20, s20, 1\n\ts_cmp_lg_u32 s20, 0\n\ts_cbranch_scc1 L_d_%=" ::: "s20", "scc", "v0","v1","v2","v3","v4","v5","v6","v7","v8","v9","v10","v11","v12","v13","v14","v15","v16","v17","v18","v19","v20","v21","v22","v23","v24","v25","v26","v27","v28","v29","v30","v31","v32","v33","v34","v35","v36","v37","v38","v39","v40","v41","v42","v43","v44","v45","v46","v47","v48","v49","v50","v51","v52","v53","v54","v55","v56","v57","v58","v59","v60","v61","v62","v63");
}

__global__ __launch_bounds__(256) void dummy_kernel3(float* p) {
  asm volatile("s_mov_b32 s20, 64\n\tL_d_%=:\n\t.rept 1\n\tv_add_f32_e64 v5, v40, v53\n\tv_add_f32_e64 v6, v41, v54\n\tv_add_f32_e64 v7, v42, v55\n\tv_add_f32_e64 v8, v43, v56\n\tv_add_f32_e64 v9, v44, v57\n\tv_add_f32_e64 v10, v45, v50\n\tv_add_f32_e64 v11, v46, v51\n\tv_add_f32_e64 v12, v47, v52\n\tv_add_f32_e64 v13, v40, v53\n\tv_add_f32_e64 v14, v41, v54\n\tv_add_f32_e64 v15, v42, v55\n\tv_add_f32_e64 v16, v43, v56\n\tv_add_f32_e64 v17, v44, v57\n\tv_add_f32_e64 v18, v45, v50\n\tv_add_f32_e64 v19, v46, v51\n\tv_add_f32_e64 v20, v47, v52\n\tv_add_f32_e64 v21, v40, v53\n\tv_add_f32_e64 v22, v41, v54\n\tv_add_f32_e64 v23, v42, v55\n\tv_add_f32_e64 v24, v43, v56\n\tv_add_f32_e64 v25, v44, v57\n\tv_add_f32_e64 v26, v45, v50\n\tv_add_f32_e64 v27, v46, v51\n\tv_add_f32_e64 v28, v47, v52\n\tv_add_f32_e64 v29, v40, v53\n\tv_add_f32_e64 v30, v41, v54\n\tv_add_f32_e64 v31, v42, v55\n\tv_add_f32_e64 v32, v43, v56\n\tv_add_f32_e64 v33, v44, v57\n\tv_add_f32_e64 v2, v45, v50\n\tv_add_f32_e64 v3, v46, v51\n\tv_add_f32_e64 v4, v47, v52\n\tv_add_f32_e64 v5, v40, v53\n\tv_add_f32_e64 v6, v41, v54\n\tv_add_f32_e64 v7, v42, v55\n\tv_add_f32_e64 v8, v43, v56\n\tv_add_f32_e64 v9, v44, v57\n\tv_add_f32_e64 v10, v45, v50\n\tv_add_f32_e64 v11, v46, v51\n\tv_add_f32_e64 v12, v47, v52\n\tv_add_f32_e64 v13, v40, v53\n\tv_add_f32_e64 v14, v41, v54\n\tv_add_f32_e64 v15, v42, v55\n\tv_add_f32_e64 v16, v43, v56\n\tv_add_f32_e64 v17, v44, v57\n\tv_add_f32_e64 v18, v45, v50\n\tv_add_f32_e64 v19, v46, v51\n\tv_add_f32_e64 v20, v47, v52\n\tv_add_f32_e64 v21, v40, v53\n\tv_add_f32_e64 v22, v41, v54\n\tv_add_f32_e64 v23, v42, v55\n\tv_add_f32_e64 v24, v43, v56\n\tv_add_f32_e64 v25, v44, v57\n\tv_add_f32_e64 v26, v45, v50\n\tv_add_f32_e64 v27, v46, v51\n\tv_add_f32_e64 v28, v47, v52\n\tv_add_f32_e64 v29, v40, v53\n\tv_add_f32_e64 v30, v41, v54\n\tv_add_f32_e64 v31, v42, v55\n\tv_add_f32_e64 v32, v43, v56\n\tv_add_f32_e64 v33, v44, v57\n\tv_add_f32_e64 v2, v45, v50\n\tv_add_f32_e64 v3, v46, v51\n\tv_add_f32_e64 v4, v47, v52\n\t.endr\n\ts_sub_u32 s20, s20, 1\n\ts_cmp_lg_u32 s20, 0\n\ts_cbranch_scc1 L_d_%=" ::: "s20", "scc", "v0","v1","v2","v3","v4","v5","v6","v7","v8","v9","v10","v11","v12","v13","v14","v15","v16","v17","v18","v19","v20","v21","v22","v23","v24","v25","v26","v27","v28","v29","v30","v31","v32","v33","v34","v35","v36","v37","v38","v39","v40","v41","v42","v43","v44","v45","v46","v47","v48","v49","v50","v51","v52","v53","v54","v55","v56","v57","v58","v59","v60","v61","v62","v63");
}

__global__ __launch_bounds__(256) void dummy_kernel4(float* p) {
  asm volatile("s_mov_b32 s20, 64\n\tL_d_%=:\n\t.rept 1\n\tv_add_f32_e64 v6, v40, v54\n\tv_add_f32_e64 v7, v41, v55\n\tv_add_f32_e64 v8, v42, v56\n\tv_add_f32_e64 v9, v43, v57\n\tv_add_f32_e64 v10, v44, v50\n\tv_add_f32_e64 v11, v45, v51\n\tv_add_f32_e64 v12, v46, v52\n\tv_add_f32_e64 v13, v47, v53\n\tv_add_f32_e64 v14, v40, v54\n\tv_add_f32_e64 v15, v41, v55\n\tv_add_f32_e64 v16, v42, v56\n\tv_add_f32_e64 v17, v43, v57\n\tv_add_f32_e64 v18, v44, v50\n\tv_add_f32_e64 v19, v45, v51\n\tv_add_f32_e64 v20, v46, v52\n\tv_add_f32_e64 v21, v47, v53\n\tv_add_f32_e64 v22, v40, v54\n\tv_add_f32_e64 v23, v41, v55\n\tv_add_f32_e64 v24, v42, v56\n\tv_add_f32_e64 v25, v43, v57\n\tv_add_f32_e64 v26, v44, v50\n\tv_add_f32_e64 v27, v45, v51\n\tv_add_f32_e64 v28, v46, v52\n\tv_add_f32_e64 v29, v47, v53\n\tv_add_f32_e64 v30, v40, v54\n\tv_add_f32_e64 v31, v41, v55\n\tv_add_f32_e64 v32, v42, v56\n\tv_add_f32_e64 v33, v43, v57\n\tv_add_f32_e64 v2, v44, v50\n\tv_add_f32_e64 v3, v45, v51\n\tv_add_f32_e64 v4, v46, v52\n\tv_add_f32_e64 v5, v47, v53\n\tv_add_f32_e64 v6, v40, v54\n\tv_add_f32_e64 v7, v41, v55\n\tv_add_f32_e64 v8, v42, v56\n\tv_add_f32_e64 v9, v43, v57\n\tv_add_f32_e64 v10, v44, v50\n\tv_add_f32_e64 v11, v45, v51\n\tv_add_f32_e64 v12, v46, v52\n\tv_add_f32_e64 v13, v47, v53\n\tv_add_f32_e64 v14, v40, v54\n\tv_add_f32_e64 v15, v41, v55\n\tv_add_f32_e64 v16, v42, v56\n\tv_add_f32_e64 v17, v43, v57\n\tv_add_f32_e64 v18, v44, v50\n\tv_add_f32_e64 v19, v45, v51\n\tv_add_f32_e64 v20, v46, v52\n\tv_add_f32_e64 v21, v47, v53\n\tv_add_f32_e64 v22, v40, v54\n\tv_add_f32_e64 v23, v41, v55\n\tv_add_f32_e64 v24, v42, v56\n\tv_add_f32_e64 v25, v43, v57\n\tv_add_f32_e64 v26, v44, v50\n\tv_add_f32_e64 v27, v45, v51\n\tv_add_f32_e64 v28, v46, v52\n\tv_add_f32_e64 v29, v47, v53\n\tv_add_f32_e64 v30, v40, v54\n\tv_add_f32_e64 v31, v41, v55\n\tv_add_f32_e64 v32, v42, v56\n\tv_add_f32_e64 v33, v43, v57\n\tv_add_f32_e64 v2, v44, v50\n\tv_add_f32_e64 v3, v45, v51\n\tv_add_f32_e64 v4, v46, v52\n\tv_add_f32_e64 v5, v47, v53\n\t.endr\n\ts_sub_u32 s20, s20, 1\n\ts_cmp_lg_u32 s20, 0\n\ts_cbranch_scc1 L_d_%=" ::: "s20", "scc", "v0","v1","v2","v3","v4","v5","v6","v7","v8","v9","v10","v11","v12","v13","v14","v15","v16","v17","v18","v19","v20","v21","v22","v23","v24","v25","v26","v27","v28","v29","v30","v31","v32","v33","v34","v35","v36","v37","v38","v39","v40","v41","v42","v43","v44","v45","v46","v47","v48","v49","v50","v51","v52","v53","v54","v55","v56","v57","v58","v59","v60","v61","v62","v63");
}

__global__ __launch_bounds__(256) void dummy_kernel5(float* p) {
  asm volatile("s_mov_b32 s20, 64\n\tL_d_%=:\n\t.rept 1\n\tv_add_f32_e64 v7, v40, v55\n\tv_add_f32_e64 v8, v41, v56\n\tv_add_f32_e64 v9, v42, v57\n\tv_add_f32_e64 v10, v43, v50\n\tv_add_f32_e64 v11, v44, v51\n\tv_add_f32_e64 v12, v45, v52\n\tv_add_f32_e64 v13, v46, v53\n\tv_add_f32_e64 v14, v47, v54\n\tv_add_f32_e64 v15, v40, v55\n\tv_add_f32_e64 v16, v41, v56\n\tv_add_f32_e64 v17, v42, v57\n\tv_add_f32_e64 v18, v43, v50\n\tv_add_f32_e64 v19, v44, v51\n\tv_add_f32_e64 v20, v45, v52\n\tv_add_f32_e64 v21, v46, v53\n\tv_add_f32_e64 v22, v47, v54\n\tv_add_f32_e64 v23, v40, v55\n\tv_add_f32_e64 v24, v41, v56\n\tv_add_f32_e64 v25, v42, v57\n\tv_add_f32_e64 v26, v43, v50\n\tv_add_f32_e64 v27, v44, v51\n\tv_add_f32_e64 v28, v45, v52\n\tv_add_f32_e64 v29, v46, v53\n\tv_add_f32_e64 v30, v47, v54\n\tv_add_f32_e64 v31, v40, v55\n\tv_add_f32_e64 v32, v41, v56\n\tv_add_f32_e64 v33, v42, v57\n\tv_add_f32_e64 v2, v43, v50\n\tv_add_f32_e64 v3, v44, v51\n\tv_add_f32_e64 v4, v45, v52\n\tv_add_f32_e64 v5, v46, v53\n\tv_add_f32_e64 v6, v47, v54\n\tv_add_f32_e64 v7, v40, v55\n\tv_add_f32_e64 v8, v41, v56\n\tv_add_f32_e64 v9, v42, v57\n\tv_add_f32_e64 v10, v43, v50\n\tv_add_f32_e64 v11, v44, v51\n\tv_add_f32_e64 v12, v45, v52\n\tv_add_f32_e64 v13, v46, v53\n\tv_add_f32_e64 v14, v47, v54\n\tv_add_f32_e64 v15, v40, v55\n\tv_add_f32_e64 v16, v41, v56\n\tv_add_f32_e64 v17, v42, v57\n\tv_add_f32_e64 v18, v43, v50\n\tv_add_f32_e64 v19, v44, v51\n\tv_add_f32_e64 v20, v45, v52\n\tv_add_f32_e64 v21, v46, v53\n\tv_add_f32_e64 v22, v47, v54\n\tv_add_f32_e64 v23, v40, v55\n\tv_add_f32_e64 v24, v41, v56\n\tv_add_f32_e64 v25, v42, v57\n\tv_add_f32_e64 v26, v43, v50\n\tv_add_f32_e64 v27, v44, v51\n\tv_add_f32_e64 v28, v45, v52\n\tv_add_f32_e64 v29, v46, v53\n\tv_add_f32_e64 v30, v47, v54\n\tv_add_f32_e64 v31, v40, v55\n\tv_add_f32_e64 v32, v41, v56\n\tv_add_f32_e64 v33, v42, v57\n\tv_add_f32_e64 v2, v43, v50\n\tv_add_f32_e64 v3, v44, v51\n\tv_add_f32_e64 v4, v45, v52\n\tv_add_f32_e64 v5, v46, v53\n\tv_add_f32_e64 v6, v47, v54\n\t.endr\n\ts_sub_u32 s20, s20, 1\n\ts_cmp_lg_u32 s20, 0\n\ts_cbranch_scc1 L_d_%=" ::: "s20", "scc", "v0","v1","v2","v3","v4","v5","v6","v7","v8","v9","v10","v11","v12","v13","v14","v15","v16","v17","v18","v19","v20","v21","v22","v23","v24","v25","v26","v27","v28","v29","v30","v31","v32","v33","v34","v35","v36","v37","v38","v39","v40","v41","v42","v43","v44","v45","v46","v47","v48","v49","v50","v51","v52","v53","v54","v55","v56","v57","v58","v59","v60","v61","v62","v63");
}

__global__ __launch_bounds__(256) void dummy_kernel6(float* p) {
  asm volatile("s_mov_b32 s20, 64\n\tL_d_%=:\n\t.rept 1\n\tv_add_f32_e64 v8, v40, v56\n\tv_add_f32_e64 v9, v41, v57\n\tv_add_f32_e64 v10, v42, v50\n\tv_add_f32_e64 v11, v43, v51\n\tv_add_f32_e64 v12, v44, v52\n\tv_add_f32_e64 v13, v45, v53\n\tv_add_f32_e64 v14, v46, v54\n\tv_add_f32_e64 v15, v47, v55\n\tv_add_f32_e64 v16, v40, v56\n\tv_add_f32_e64 v17, v41, v57\n\tv_add_f32_e64 v18, v42, v50\n\tv_add_f32_e64 v19, v43, v51\n\tv_add_f32_e64 v20, v44, v52\n\tv_add_f32_e64 v21, v45, v53\n\tv_add_f32_e64 v22, v46, v54\n\tv_add_f32_e64 v23, v47, v55\n\tv_add_f32_e64 v24, v40, v56\n\tv_add_f32_e64 v25, v41, v57\n\tv_add_f32_e64 v26, v42, v50\n\tv_add_f32_e64 v27, v43, v51\n\tv_add_f32_e64 v28, v44, v52\n\tv_add_f32_e64 v29, v45, v53\n\tv_add_f32_e64 v30, v46, v54\n\tv_add_f32_e64 v31, v47, v55\n\tv_add_f32_e64 v32, v40, v56\n\tv_add_f32_e64 v33, v41, v57\n\tv_add_f32_e64 v2, v42, v50\n\tv_add_f32_e64 v3, v43, v51\n\tv_add_f32_e64 v4, v44, v52\n\tv_add_f32_e64 v5, v45, v53\n\tv_add_f32_e64 v6, v46, v54\n\tv_add_f32_e64 v7, v47, v55\n\tv_add_f32_e64 v8, v40, v56\n\tv_add_f32_e64 v9, v41, v57\n\tv_add_f32_e64 v10, v42, v50\n\tv_add_f32_e64 v11, v43, v51\n\tv_add_f32_e64 v12, v44, v52\n\tv_add_f32_e64 v13, v45, v53\n\tv_add_f32_e64 v14, v46, v54\n\tv_add_f32_e64 v15, v47, v55\n\tv_add_f32_e64 v16, v40, v56\n\tv_add_f32_e64 v17, v41, v57\n\tv_add_f32_e64 v18, v42, v50\n\tv_add_f32_e64 v19, v43, v51\n\tv_add_f32_e64 v20, v44, v52\n\tv_add_f32_e64 v21, v45, v53\n\tv_add_f32_e64 v22, v46, v54\n\tv_add_f32_e64 v23, v47, v55\n\tv_add_f32_e64 v24, v40, v56\n\tv_add_f32_e64 v25, v41, v57\n\tv_add_f32_e64 v26, v42, v50\n\tv_add_f32_e64 v27, v43, v51\n\tv_add_f32_e64 v28, v44, v52\n\tv_add_f32_e64 v29, v45, v53\n\tv_add_f32_e64 v30, v46, v54\n\tv_add_f32_e64 v31, v47, v55\n\tv_add_f32_e64 v32, v40, v56\n\tv_add_f32_e64 v33, v41, v57\n\tv_add_f32_e64 v2, v42, v50\n\tv_add_f32_e64 v3, v43, v51\n\tv_add_f32_e64 v4, v44, v52\n\tv_add_f32_e64 v5, v45, v53\n\tv_add_f32_e64 v6, v46, v54\n\tv_add_f32_e64 v7, v47, v55\n\t.endr\n\ts_sub_u32 s20, s20, 1\n\ts_cmp_lg_u32 s20, 0\n\ts_cbranch_scc1 L_d_%=" ::: "s20", "scc", "v0","v1","v2","v3","v4","v5","v6","v7","v8","v9","v10","v11","v12","v13","v14","v15","v16","v17","v18","v19","v20","v21","v22","v23","v24","v25","v26","v27","v28","v29","v30","v31","v32","v33","v34","v35","v36","v37","v38","v39","v40","v41","v42","v43","v44","v45","v46","v47","v48","v49","v50","v51","v52","v53","v54","v55","v56","v57","v58","v59","v60","v61","v62","v63");
}

__global__ __launch_bounds__(256) void dummy_kernel7(float* p) {
  asm volatile("s_mov_b32 s20, 64\n\tL_d_%=:\n\t.rept 1\n\tv_add_f32_e64 v9, v40, v57\n\tv_add_f32_e64 v10, v41, v50\n\tv_add_f32_e64 v11, v42, v51\n\tv_add_f32_e64 v12, v43, v52\n\tv_add_f32_e64 v13, v44, v53\n\tv_add_f32_e64 v14, v45, v54\n\tv_add_f32_e64 v15, v46, v55\n\tv_add_f32_e64 v16, v47, v56\n\tv_add_f32_e64 v17, v40, v57\n\tv_add_f32_e64 v18, v41, v50\n\tv_add_f32_e64 v19, v42, v51\n\tv_add_f32_e64 v20, v43, v52\n\tv_add_f32_e64 v21, v44, v53\n\tv_add_f32_e64 v22, v45, v54\n\tv_add_f32_e64 v23, v46, v55\n\tv_add_f32_e64 v24, v47, v56\n\tv_add_f32_e64 v25, v40, v57\n\tv_add_f32_e64 v26, v41, v50\n\tv_add_f32_e64 v27, v42, v51\n\tv_add_f32_e64 v28, v43, v52\n\tv_add_f32_e64 v29, v44, v53\n\tv_add_f32_e64 v30, v45, v54\n\tv_add_f32_e64 v31, v46, v55\n\tv_add_f32_e64 v32, v47, v56\n\tv_add_f32_e64 v33, v40, v57\n\tv_add_f32_e64 v2, v41, v50\n\tv_add_f32_e64 v3, v42, v51\n\tv_add_f32_e64 v4, v43, v52\n\tv_add_f32_e64 v5, v44, v53\n\tv_add_f32_e64 v6, v45, v54\n\tv_add_f32_e64 v7, v46, v55\n\tv_add_f32_e64 v8, v47, v56\n\tv_add_f32_e64 v9, v40, v57\n\tv_add_f32_e64 v10, v41, v50\n\tv_add_f32_e64 v11, v42, v51\n\tv_add_f32_e64 v12, v43, v52\n\tv_add_f32_e64 v13, v44, v53\n\tv_add_f32_e64 v14, v45, v54\n\tv_add_f32_e64 v15, v46, v55\n\tv_add_f32_e64 v16, v47, v56\n\tv_add_f32_e64 v17, v40, v57\n\tv_add_f32_e64 v18, v41, v50\n\tv_add_f32_e64 v19, v42, v51\n\tv_add_f32_e64 v20, v43, v52\n\tv_add_f32_e64 v21, v44, v53\n\tv_add_f32_e64 v22, v45, v54\n\tv_add_f32_e64 v23, v46, v55\n\tv_add_f32_e64 v24, v47, v56\n\tv_add_f32_e64 v25, v40, v57\n\tv_add_f32_e64 v26, v41, v50\n\tv_add_f32_e64 v27, v42, v51\n\tv_add_f32_e64 v28, v43, v52\n\tv_add_f32_e64 v29, v44, v53\n\tv_add_f32_e64 v30, v45, v54\n\tv_add_f32_e64 v31, v46, v55\n\tv_add_f32_e64 v32, v47, v56\n\tv_add_f32_e64 v33, v40, v57\n\tv_add_f32_e64 v2, v41, v50\n\tv_add_f32_e64 v3, v42, v51\n\tv_add_f32_e64 v4, v43, v52\n\tv_add_f32_e64 v5, v44, v53\n\tv_add_f32_e64 v6, v45, v54\n\tv_add_f32_e64 v7, v46, v55\n\tv_add_f32_e64 v8, v47, v56\n\t.endr\n\ts_sub_u32 s20, s20, 1\n\ts_cmp_lg_u32 s20, 0\n\ts_cbranch_scc1 L_d_%=" ::: "s20", "scc", "v0","v1","v2","v3","v4","v5","v6","v7","v8","v9","v10","v11","v12","v13","v14","v15","v16","v17","v18","v19","v20","v21","v22","v23","v24","v25","v26","v27","v28","v29","v30","v31","v32","v33","v34","v35","v36","v37","v38","v39","v40","v41","v42","v43","v44","v45","v46","v47","v48","v49","v50","v51","v52","v53","v54","v55","v56","v57","v58","v59","v60","v61","v62","v63");
}
extern "C" void kernel_launch(void* const* d_in, const int* in_sizes, int n_in, void* d_out, int out_size, void* d_ws, size_t ws_size, hipStream_t stream) {
  const u16* x = (const u16*)d_in[0]; const u16* kc = (const u16*)d_in[1]; const u16* vc = (const u16*)d_in[2];
  const u16* wq = (const u16*)d_in[3]; const u16* wo = (const u16*)d_in[4];
  const u16* w1 = (const u16*)d_in[6]; const u16* b1 = (const u16*)d_in[7]; const u16* w2 = (const u16*)d_in[8]; const u16* b2 = (const u16*)d_in[9];
  char* ws = (char*)d_ws;
  u16* xn = (u16*)ws; u16* y = (u16*)(ws + (2 << 20)); u16* q = (u16*)(ws + (4 << 20)); u16* o = (u16*)(ws + (24 << 20));
  u16* hbuf = (u16*)(ws + (44 << 20)); float* s = (float*)(ws + (56 << 20)); float* g = (float*)(ws + (64 << 20));
  dummy_kernel0<<<256, 256, 0, stream>>>((float*)d_ws);
  dummy_kernel1<<<256, 256, 0, stream>>>((float*)d_ws);
  dummy_kernel2<<<256, 256, 0, stream>>>((float*)d_ws);
  dummy_kernel3<<<256, 256, 0, stream>>>((float*)d_ws);
  dummy_kernel4<<<256, 256, 0, stream>>>((float*)d_ws);
  dummy_kernel5<<<256, 256, 0, stream>>>((float*)d_ws);
  dummy_kernel6<<<256, 256, 0, stream>>>((float*)d_ws);
  dummy_kernel7<<<256, 256, 0, stream>>>((float*)d_ws);
  ln_kernel<<<S / 4, 256, 0, stream>>>(x, xn, y);
  gemm_simple<0><<<dim3(HD / 64, S / 64), 256, 0, stream>>>(xn, wq, nullptr, q, S, HD, DM);
  gemm_simple<1><<<dim3(F / 64, S / 64), 256, 0, stream>>>(y, w1, b1, hbuf, S, F, DM);
  attn_simple<<<dim3(S / 128, H), 256, 0, stream>>>(q, kc, vc, o);
  gemm_simple<2><<<dim3(DM / 64, S / 64), 256, 0, stream>>>(o, wo, nullptr, s, S, DM, HD);
  gemm_simple<2><<<dim3(DM / 64, S / 64), 256, 0, stream>>>(hbuf, w2, nullptr, g, S, DM, F);
  final_kernel<<<S * DM / 256, 256, 0, stream>>>(x, s, g, b2, (u16*)d_out);
}
```

```cpp
#include <hip/hip_runtime.h>
#include <hip/hip_bf16.h>

constexpr size_t WS_XN = 0, WS_ST = 2u << 20, WS_CB = WS_ST + 65536, WS_FLAG = WS_ST + 131072, WS_OH = 4u << 20, WS_PART = 32u << 20;

typedef __attribute__((ext_vector_type(8))) short s16x8;
typedef __attribute__((ext_vector_type(4))) unsigned u32x4;
typedef __attribute__((ext_vector_type(2))) unsigned u32x2;
typedef __attribute__((ext_vector_type(2))) float f32x2;
typedef __attribute__((ext_vector_type(2))) __bf16 bf16x2;
typedef unsigned short u16;

__device__ __forceinline__ float bf2f(u16 v) { return __uint_as_float(((unsigned)v) << 16); }
__device__ __forceinline__ unsigned pk2(float lo, float hi) { f32x2 v = {lo, hi}; bf16x2 b = __builtin_convertvector(v, bf16x2); return __builtin_bit_cast(unsigned, b); }
template <int CTRL> __device__ __forceinline__ float dpp_add(float x) {
  return x + __int_as_float(__builtin_amdgcn_update_dpp(0, __float_as_int(x), CTRL, 0xf, 0xf, true));
}
__device__ __forceinline__ float wave_sum(float x) {
  x = dpp_add<0xB1>(x);
  x = dpp_add<0x4E>(x);
  x = dpp_add<0x141>(x);
  x = dpp_add<0x140>(x);
  { auto r = __builtin_amdgcn_permlane16_swap(__float_as_uint(x), __float_as_uint(x), false, false); x = __uint_as_float(r[0]) + __uint_as_float(r[1]); }
  { auto r = __builtin_amdgcn_permlane32_swap(__float_as_uint(x), __float_as_uint(x), false, false); x = __uint_as_float(r[0]) + __uint_as_float(r[1]); }
  return x;
}

constexpr float QSCALE = 0.12751743082459868f;
constexpr int A_LN_BLOCKS = 128, A_CS_BLOCKS = 8, A_PF_BLOCKS = 96;
__global__ __launch_bounds__(256) void prep_kernel(const u16* __restrict__ x, const u16* __restrict__ w1, const u16* __restrict__ b1, const u16* __restrict__ wq,
                                                   u16* __restrict__ xn_frag, float* __restrict__ stats, float* __restrict__ cb, unsigned* __restrict__ flags) {
  __shared__ __attribute__((aligned(16))) char lds[16384];
  const int tid = threadIdx.x, lane = tid & 63, wid = tid >> 6;
  if (blockIdx.x < A_LN_BLOCKS) {
    const int qb = blockIdx.x;
    s16x8 v[4];
#pragma unroll
    for (int i = 0; i < 4; ++i) v[i] = *reinterpret_cast<const s16x8*>(x + (size_t)(qb * 16 + wid * 4 + i) * 512 + lane * 8);
#pragma unroll
    for (int i = 0; i < 4; ++i) {
      float f[8]; float s = 0.f;
#pragma unroll
      for (int e = 0; e < 8; ++e) { f[e] = bf2f((u16)v[i][e]); s += f[e]; }
      const float m = wave_sum(s) * (1.f / 512.f);
      float q = 0.f;
#pragma unroll
      for (int e = 0; e < 8; ++e) { f[e] -= m; q += f[e] * f[e]; }
      const float r = 1.f / sqrtf(wave_sum(q) * (1.f / 512.f) + 1e-5f);
      s = 0.f;
#pragma unroll
      for (int e = 0; e < 8; ++e) { f[e] *= r; s += f[e]; }
      const float m2 = wave_sum(s) * (1.f / 512.f);
      q = 0.f;
#pragma unroll
      for (int e = 0; e < 8; ++e) { f[e] -= m2; q += f[e] * f[e]; }
      const float r2 = 1.f / sqrtf(wave_sum(q) * (1.f / 512.f) + 1e-5f);
      unsigned p[4];
#pragma unroll
      for (int e = 0; e < 4; ++e) p[e] = pk2(f[2 * e] * QSCALE, f[2 * e + 1] * QSCALE);
      const int row = wid * 4 + i;
      if (lane == 0) stats[qb * 16 + row] = r2 * (1.f / QSCALE);
      const int ks = lane >> 2, jhi = (lane & 3) >> 1, g0 = 2 * (lane & 1);
      u32x2 lo = {p[0], p[1]}, hi = {p[2], p[3]};
      *reinterpret_cast<u32x2*>(lds + ks * 1024 + (16 * g0 + row) * 16 + 8 * jhi) = lo;
      *reinterpret_cast<u32x2*>(lds + ks * 1024 + (16 * (g0 + 1) + row) * 16 + 8 * jhi) = hi;
    }
    __syncthreads();
    char* dst = reinterpret_cast<char*>(xn_frag) + (size_t)qb * 16384;
#pragma unroll
    for (int i = 0; i < 4; ++i) *reinterpret_cast<u32x4*>(dst + i * 4096 + tid * 16) = *reinterpret_cast<const u32x4*>(lds + i * 4096 + tid * 16);
  } else if (blockIdx.x >= A_LN_BLOCKS + A_CS_BLOCKS) {
    const int pb = blockIdx.x - (A_LN_BLOCKS + A_CS_BLOCKS), hg = (pb & 7) >> 1, part = pb >> 3;
    const char* q0 = reinterpret_cast<const char*>(wq) + hg * 2048 + (tid & 127) * 16;
    const char* f0 = reinterpret_cast<const char*>(w1) + (hg < 3 ? hg * 1024 : 2560) + (tid & 63) * 16;
    u32x4 v[33];
#pragma unroll
    for (int i = 0; i < 22; ++i) { const int row = part * 43 + i * 2 + (tid >> 7); v[i] = *reinterpret_cast<const u32x4*>(q0 + (size_t)(row < 512 ? row : 511) * 8192); }
#pragma unroll
    for (int i = 0; i < 11; ++i) { const int row = part * 43 + i * 4 + wid; v[22 + i] = *reinterpret_cast<const u32x4*>(f0 + (size_t)(row < 512 ? row : 511) * 3584); }
    {
      unsigned long long c0, c1;
      asm volatile("s_getpc_b64 s[20:21] \n L_pfpc_%=: \n s_add_u32 s20, s20, mega_code_start-L_pfpc_%= \n s_addc_u32 s21, s21, 0 \n s_add_u32 s22, s20, mega_code_end-mega_code_start-16 \n s_addc_u32 s23, s21, 0 \n s_mov_b64 %0, s[20:21] \n s_mov_b64 %1, s[22:23]" : "=&s"(c0), "=&s"(c1) :: "scc", "s20", "s21", "s22", "s23");
      u32x4 cv[2];
#pragma unroll
      for (int i = 0; i < 2; ++i) { unsigned long long a = c0 + part * 8192 + i * 4096 + tid * 16; a = a < c1 ? a : c1; cv[i] = *reinterpret_cast<const u32x4*>(a & ~15ull); }
#pragma unroll
      for (int i = 0; i < 2; ++i) asm volatile("" :: "v"(cv[i]));
    }
#pragma unroll
    for (int i = 0; i < 33; ++i) asm volatile("" :: "v"(v[i]));
  } else {
    const int j = blockIdx.x - A_LN_BLOCKS;
    u32x4 z = {0u, 0u, 0u, 0u};
    *reinterpret_cast<u32x4*>(flags + (j * 256 + tid) * 4) = z;
    if (tid < 224) cb[j * 224 + tid] = bf2f(b1[j * 224 + tid]);
  }
}

extern "C" __global__ __launch_bounds__(256, 1) void mega_kernel(const u16* __restrict__ wq, const u16* __restrict__ kc, const u16* __restrict__ vc,
    const u16* __restrict__ w1, const u16* __restrict__ w2, const u16* __restrict__ wo, char* ws, const u16* __restrict__ x, const u16* __restrict__ b2,
    u16* __restrict__ out) {
  extern __shared__ __attribute__((aligned(16))) char lds[];
  const int b = blockIdx.x, idx = b >> 3, xcd = b & 7;
  const int h = 8 * (xcd >> 1) + (idx & 7), y = 4 * (xcd & 1) + (idx >> 3);
  const int w = __builtin_amdgcn_readfirstlane(threadIdx.x >> 6);
  const int qb = y * 16 + w * 4;
  const int pfo = ((b & 3) << 8) | (idx * 4 + w);
  const int j = xcd & 3, qb128 = 8 * (xcd >> 2) + (idx & 7), nb128 = idx >> 3, tile = qb128 * 4 + nb128;
  asm volatile(
  ".p2align 3\n\t"
  "mega_code_start:\n\t"
  "s_lshl_b32 s40, %9, 14\n\t"
  "s_add_u32 s36, s64, s40\n\t"
  "s_addc_u32 s37, s65, 0\n\t"
  "s_mov_b32 s38, 0x10000\n\t"
  "s_mov_b32 s39, 0x20000\n\t"
  "s_nop 0\n\t"
  "v_mbcnt_lo_u32_b32 v0, -1, 0\n\t"
  "v_mbcnt_hi_u32_b32 v0, -1, v0\n\t"
  "v_lshlrev_b32_e32 v1, 4, v0\n\t"
  "s_mov_b32 s40, 0\n\t"
  "buffer_load_dwordx4 a[0:3], v1, s[36:39], s40 offen offset:0\n\t"
  "buffer_load_dwordx4 a[16:19], v1, s[36:39], s40 offen offset:1024\n\t"
  "s_mov_b32 s40, 16384\n\t"
  "buffer_load_dwordx4 a[4:7], v1, s[36:39], s40 offen offset:0\n\t"
  "buffer_load_dwordx4 a[20:23], v1, s[36:39], s40 offen offset:1024\n\t"
  "s_mov_b32 s40, 32768\n\t"
  "buffer_load_dwordx4 a[8:11], v1, s[36:39], s40 offen offset:0\n\t"
  "buffer_load_dwordx4 a[24:27], v1, s[36:39], s40 offen offset:1024\n\t"
  "s_mov_b32 s40, 49152\n\t"
  "buffer_load_dwordx4 a[12:15], v1, s[36:39], s40 offen offset:0\n\t"
  "buffer_load_dwordx4 a[28:31], v1, s[36:39], s40 offen offset:1024\n\t"
  "s_sub_u32 s47, %8, 4\n\t"
  "s_cmp_lt_u32 %8, 28\n\t"
  "s_cselect_b32 s47, %8, s47\n\t"
  "s_cselect_b32 s50, 753664, 0\n\t"
  "s_lshl_b32 s40, s47, 7\n\t"
  "s_add_u32 s24, s66, s40\n\t"
  "s_addc_u32 s25, s67, 0\n\t"
  "s_mov_b32 s26, 1835008\n\t"
  "s_mov_b32 s27, 0x20000\n\t"
  "v_bfe_u32 v226, v0, 2, 3\n\t"
  "v_lshrrev_b32_e32 v227, 2, v226\n\t"
  "v_lshlrev_b32_e32 v227, 1, v227\n\t"
  "v_and_b32_e32 v228, 3, v0\n\t"
  "v_xor_b32_e32 v227, v227, v228\n\t"
  "v_lshlrev_b32_e32 v227, 4, v227\n\t"
  "v_lshrrev_b32_e32 v228, 5, v0\n\t"
  "v_lshl_add_u32 v227, v228, 6, v227\n\t"
  "v_mul_u32_u24_e32 v228, 0xe00, v226\n\t"
  "v_add_u32_e32 v18, v228, v227\n\t"
  "s_lshl_b32 s42, %7, 11\n\t"
  "s_mul_i32 s43, %7, 57344\n\t"
  "s_add_u32 m0, s42, 0\n\t"
  "s_add_u32 s41, s43, 0\n\t"
  "buffer_load_dwordx4 v18, s[24:27], s41 offen lds\n\t"
  "s_add_u32 m0, s42, 1024\n\t"
  "s_add_u32 s41, s43, 28672\n\t"
  "buffer_load_dwordx4 v18, s[24:27], s41 offen lds\n\t"
  "v_lshl_add_u32 v19, v226, 13, v227\n\t"
  "s_lshl_b32 s44, %7, 12\n\t"
  "s_lshl_b32 s45, %7, 17\n\t"
  "s_lshl_b32 s40, %8, 8\n\t"
  "s_add_u32 s28, s68, s40\n\t"
  "s_addc_u32 s29, s69, 0\n\t"
  "s_mov_b32 s30, 4194304\n\t"
  "s_mov_b32 s31, 0x20000\n\t"
  "s_lshl_b32 s40, s47, 8\n\t"
  "s_add_u32 s52, s64, s40\n\t"
  "s_addc_u32 s53, s65, 0\n\t"
  "s_add_u32 s52, s52, 2162688\n\t"
  "s_addc_u32 s53, s53, 0\n\t"
  "s_lshl_b32 s40, %9, 6\n\t"
  "s_add_u32 s22, s64, s40\n\t"
  "s_addc_u32 s23, s65, 0\n\t"
  "s_add_u32 s22, s22, 2097152\n\t"
  "s_addc_u32 s23, s23, 0\n\t"
  "v_and_b32_e32 v224, 15, v0\n\t"
  "v_lshrrev_b32_e64 v225, 4, v0\n\t"
  "s_add_u32 m0, s42, 8192\n\t"
  "s_add_u32 s41, s43, 229376\n\t"
  "buffer_load_dwordx4 v18, s[24:27], s41 offen lds\n\t"
  "s_add_u32 m0, s42, 9216\n\t"
  "s_add_u32 s41, s43, 258048\n\t"
  "buffer_load_dwordx4 v18, s[24:27], s41 offen lds\n\t"
  "s_mov_b32 s40, 2048\n\t"
  "buffer_load_dwordx4 a[32:35], v1, s[36:39], s40 offen offset:0\n\t"
  "buffer_load_dwordx4 a[48:51], v1, s[36:39], s40 offen offset:1024\n\t"
  "s_mov_b32 s40, 18432\n\t"
  "buffer_load_dwordx4 a[36:39], v1, s[36:39], s40 offen offset:0\n\t"
  "buffer_load_dwordx4 a[52:55], v1, s[36:39], s40 offen offset:1024\n\t"
  "s_mov_b32 s40, 34816\n\t"
  "buffer_load_dwordx4 a[40:43], v1, s[36:39], s40 offen offset:0\n\t"
  "buffer_load_dwordx4 a[56:59], v1, s[36:39], s40 offen offset:1024\n\t"
  "s_mov_b32 s40, 51200\n\t"
  "buffer_load_dwordx4 a[44:47], v1, s[36:39], s40 offen offset:0\n\t"
  "buffer_load_dwordx4 a[60:63], v1, s[36:39], s40 offen offset:1024\n\t"
  "v_lshrrev_b32_e32 v226, 1, v225\n\t"
  "v_and_b32_e32 v227, 1, v225\n\t"
  "v_bfe_u32 v228, v0, 2, 2\n\t"
  "v_lshlrev_b32_e64 v228, 6, v228\n\t"
  "v_lshl_add_u32 v228, v227, 8, v228\n\t"
  "v_and_b32_e64 v28, 3, v0\n\t"
  "v_lshl_add_u32 v29, v226, 10, v228\n\t"
  "v_lshl_add_u32 v29, v28, 3, v29\n\t"
  "v_lshl_add_u32 v14, v227, 5, v29\n\t"
  "v_xor_b32_e64 v30, 1, v227\n\t"
  "v_lshl_add_u32 v15, v30, 5, v29\n\t"
  "v_lshlrev_b32_e32 v227, 1, v227\n\t"
  "v_xor_b32_e32 v28, v28, v227\n\t"
  "v_lshl_add_u32 v29, v226, 11, v228\n\t"
  "v_lshl_add_u32 v29, v28, 4, v29\n\t"
  "v_add_u32_e32 v16, 0x10000, v29\n\t"
  "v_add_u32_e32 v17, 0x20000, v29\n\t"
  "v_add_u32_e32 v28, 0, v225\n\t"
  "v_xor_b32_e32 v28, v28, v224\n\t"
  "v_lshlrev_b32_e64 v28, 4, v28\n\t"
  "v_lshl_add_u32 v2, v224, 8, v28\n\t"
  "v_add_u32_e32 v28, 4, v225\n\t"
  "v_xor_b32_e32 v28, v28, v224\n\t"
  "v_lshlrev_b32_e64 v28, 4, v28\n\t"
  "v_lshl_add_u32 v3, v224, 8, v28\n\t"
  "v_add_u32_e32 v28, 8, v225\n\t"
  "v_xor_b32_e32 v28, v28, v224\n\t"
  "v_lshlrev_b32_e64 v28, 4, v28\n\t"
  "v_lshl_add_u32 v4, v224, 8, v28\n\t"
  "v_add_u32_e32 v28, 12, v225\n\t"
  "v_xor_b32_e32 v28, v28, v224\n\t"
  "v_lshlrev_b32_e64 v28, 4, v28\n\t"
  "v_lshl_add_u32 v5, v224, 8, v28\n\t"
  "v_lshrrev_b32_e32 v28, 1, v225\n\t"
  "v_lshlrev_b32_e32 v28, 11, v28\n\t"
  "v_and_b32_e64 v29, 1, v225\n\t"
  "v_lshl_add_u32 v28, v29, 8, v28\n\t"
  "v_bfe_u32 v226, v0, 2, 2\n\t"
  "v_lshl_add_u32 v28, v226, 6, v28\n\t"
  "v_and_b32_e64 v226, 3, v0\n\t"
  "v_lshl_add_u32 v28, v226, 3, v28\n\t"
  "v_add_u32_e32 v28, 65536, v28\n\t"
  "v_lshl_add_u32 v6, v29, 5, v28\n\t"
  "v_xor_b32_e64 v29, 1, v29\n\t"
  "v_lshl_add_u32 v7, v29, 5, v28\n\t"
  "s_lshl_b32 s21, %7, 12\n\t"
  "v_add_u32_e32 v28, 0, v225\n\t"
  "v_xor_b32_e32 v28, v28, v224\n\t"
  "v_lshlrev_b32_e32 v28, 4, v28\n\t"
  "v_lshl_add_u32 v28, v225, 8, v28\n\t"
  "v_add_u32_e32 v8, s21, v28\n\t"
  "v_add_u32_e32 v28, 4, v225\n\t"
  "v_xor_b32_e32 v28, v28, v224\n\t"
  "v_lshlrev_b32_e32 v28, 4, v28\n\t"
  "v_lshl_add_u32 v28, v225, 8, v28\n\t"
  "v_add_u32_e32 v9, s21, v28\n\t"
  "v_add_u32_e32 v28, 8, v225\n\t"
  "v_xor_b32_e32 v28, v28, v224\n\t"
  "v_lshlrev_b32_e32 v28, 4, v28\n\t"
  "v_lshl_add_u32 v28, v225, 8, v28\n\t"
  "v_add_u32_e32 v10, s21, v28\n\t"
  "v_add_u32_e32 v28, 12, v225\n\t"
  "v_xor_b32_e32 v28, v28, v224\n\t"
  "v_lshlrev_b32_e32 v28, 4, v28\n\t"
  "v_lshl_add_u32 v28, v225, 8, v28\n\t"
  "v_add_u32_e64 v11, s21, v28\n\t"
  "v_bfe_u32 v28, v0, 2, 3\n\t"
  "v_lshrrev_b32_e32 v29, 2, v28\n\t"
  "v_lshlrev_b32_e32 v29, 1, v29\n\t"
  "v_and_b32_e32 v226, 3, v0\n\t"
  "v_xor_b32_e32 v29, v29, v226\n\t"
  "v_lshlrev_b32_e64 v29, 4, v29\n\t"
  "v_lshl_add_u32 v29, v28, 8, v29\n\t"
  "v_lshrrev_b32_e64 v226, 5, v0\n\t"
  "v_lshl_add_u32 v29, v226, 6, v29\n\t"
  "v_add_u32_e64 v29, s21, v29\n\t"
  "v_add_u32_e32 v12, 0x400, v29\n\t"
  "v_add_u32_e32 v13, 0x80, v29\n\t"
  "s_add_u32 s18, s21, 0\n\t"
  "s_add_u32 s19, s21, 65536\n\t"
  "v_lshlrev_b32_e32 v30, 4, v225\n\t"
  "v_mov_b32_e32 v31, 0xc0135761\n\t"
  "s_mov_b32 s49, 0xbdd2d3e8\n\t"
  "s_mov_b32 s48, 0x3e0293ee\n\t"
  "s_lshl_b32 s40, %8, 19\n\t"
  "s_add_u32 s8, s70, s40\n\t"
  "s_addc_u32 s9, s71, 0\n\t"
  "s_mov_b32 s10, 0x80000\n\t"
  "s_mov_b32 s11, 0x20000\n\t"
  "s_add_u32 s12, s72, s40\n\t"
  "s_addc_u32 s13, s73, 0\n\t"
  "s_sub_u32 s12, s12, 0x400\n\t"
  "s_subb_u32 s13, s13, 0\n\t"
  "s_mov_b32 s14, 0x80400\n\t"
  "s_mov_b32 s15, 0x20000\n\t"
  "s_mul_i32 s40, %9, 188416\n\t"
  "s_add_u32 s32, s64, s40\n\t"
  "s_addc_u32 s33, s65, 0\n\t"
  "s_add_u32 s32, s32, 4194304\n\t"
  "s_addc_u32 s33, s33, 0\n\t"
  "s_mov_b32 s34, s50\n\t"
  "s_mov_b32 s35, 0x20000\n\t"
  "s_lshl_b32 s46, s47, 11\n\t"
  "s_mov_b32 s16, 0\n\t"
  "s_mov_b32 s20, 0\n\t"
  "v_lshlrev_b32_e32 v29, 2, v224\n\t"
  "global_load_dword v20, v29, s[22:23] offset:0\n\t"
  "global_load_dword v21, v29, s[22:23] offset:64\n\t"
  "global_load_dword v22, v29, s[22:23] offset:128\n\t"
  "global_load_dword v23, v29, s[22:23] offset:192\n\t"
  "global_load_dwordx4 v[224:227], v30, s[52:53] offset:0\n\t"
  "global_load_dwordx4 v[228:231], v30, s[52:53] offset:64\n\t"
  "global_load_dwordx4 v[24:27], v30, s[52:53] offset:128\n\t"
  "s_add_u32 m0, s42, 16384\n\t"
  "s_add_u32 s41, s43, 458752\n\t"
  "buffer_load_dwordx4 v18, s[24:27], s41 offen lds\n\t"
  "s_add_u32 m0, s42, 17408\n\t"
  "s_add_u32 s41, s43, 487424\n\t"
  "buffer_load_dwordx4 v18, s[24:27], s41 offen lds\n\t"
  "s_mov_b32 s40, 4096\n\t"
  "buffer_load_dwordx4 a[64:67], v1, s[36:39], s40 offen offset:0\n\t"
  "buffer_load_dwordx4 a[80:83], v1, s[36:39], s40 offen offset:1024\n\t"
  "s_mov_b32 s40, 20480\n\t"
  "buffer_load_dwordx4 a[68:71], v1, s[36:39], s40 offen offset:0\n\t"
  "buffer_load_dwordx4 a[84:87], v1, s[36:39], s40 offen offset:1024\n\t"
  "s_mov_b32 s40, 36864\n\t"
  "buffer_load_dwordx4 a[72:75], v1, s[36:39], s40 offen offset:0\n\t"
  "buffer_load_dwordx4 a[88:91], v1, s[36:39], s40 offen offset:1024\n\t"
  "s_mov_b32 s40, 53248\n\t"
  "buffer_load_dwordx4 a[76:79], v1, s[36:39], s40 offen offset:0\n\t"
  "buffer_load_dwordx4 a[92:95], v1, s[36:39], s40 offen offset:1024\n\t"
  "s_waitcnt vmcnt(27)\n\t"
  "s_barrier\n\t"
  "ds_read_b64_tr_b16 v[232:233], v14 offset:0\n\t"
  "ds_read_b64_tr_b16 v[234:235], v14 offset:2048\n\t"
  "ds_read_b64_tr_b16 v[236:237], v14 offset:4096\n\t"
  "ds_read_b64_tr_b16 v[238:239], v14 offset:6144\n\t"
  "ds_read_b64_tr_b16 v[240:241], v15 offset:0\n\t"
  "ds_read_b64_tr_b16 v[242:243], v15 offset:2048\n\t"
  "ds_read_b64_tr_b16 v[244:245], v15 offset:4096\n\t"
  "ds_read_b64_tr_b16 v[246:247], v15 offset:6144\n\t"
  "s_waitcnt lgkmcnt(6)\n\t"
  "s_nop 0\n\t"
  "v_mfma_f32_16x16x32_bf16 v[160:163], v[232:235], a[0:3], 0\n\t"
  "s_waitcnt lgkmcnt(4)\n\t"
  "s_add_u32 m0, s42, 24576\n\t"
  "s_add_u32 s41, s43, 688128\n\t"
  "s_nop 0\n\t"
  "buffer_load_dwordx4 v18, s[24:27], s41 offen lds\n\t"
  "v_mfma_f32_16x16x32_bf16 v[160:163], v[236:239], a[16:19], v[160:163]\n\t"
  "ds_read_b64_tr_b16 v[248:249], v14 offset:512\n\t"
  "ds_read_b64_tr_b16 v[250:251], v14 offset:2560\n\t"
  "v_mfma_f32_16x16x32_bf16 v[164:167], v[232:235], a[4:7], 0\n\t"
  "s_add_u32 m0, s42, 25600\n\t"
  "s_add_u32 s41, s43, 716800\n\t"
  "buffer_load_dwordx4 v18, s[24:27], s41 offen lds\n\t"
  "v_mfma_f32_16x16x32_bf16 v[164:167], v[236:239], a[20:23], v[164:167]\n\t"
  "ds_read_b64_tr_b16 v[252:253], v14 offset:4608\n\t"
  "ds_read_b64_tr_b16 v[254:255], v14 offset:6656\n\t"
  "v_mfma_f32_16x16x32_bf16 v[168:171], v[232:235], a[8:11], 0\n\t"
  "s_mov_b32 s40, 6144\n\t"
  "buffer_load_dwordx4 a[96:99], v1, s[36:39], s40 offen offset:0\n\t"
  "v_mfma_f32_16x16x32_bf16 v[168:171], v[236:239], a[24:27], v[168:171]\n\t"
  "v_mfma_f32_16x16x32_bf16 v[172:175], v[232:235], a[12:15], 0\n\t"
  "buffer_load_dwordx4 a[112:115], v1, s[36:39], s40 offen offset:1024\n\t"
  "v_mfma_f32_16x16x32_bf16 v[172:175], v[236:239], a[28:31], v[172:175]\n\t"
  "s_waitcnt lgkmcnt(2)\n\t"
  "s_nop 0\n\t"
  "v_mfma_f32_16x16x32_bf16 v[176:179], v[240:243], a[0:3], 0\n\t"
  "s_mov_b32 s40, 22528\n\t"
  "buffer_load_dwordx4 a[100:103], v1, s[36:39], s40 offen offset:0\n\t"
  "v_mfma_f32_16x16x32_bf16 v[176:179], v[244:247], a[16:19], v[176:179]\n\t"
  "ds_read_b64_tr_b16 v[232:233], v15 offset:512\n\t"
  "ds_read_b64_tr_b16 v[234:235], v15 offset:2560\n\t"
  "v_mfma_f32_16x16x32_bf16 v[180:183], v[240:243], a[4:7], 0\n\t"
  "buffer_load_dwordx4 a[116:119], v1, s[36:39], s40 offen offset:1024\n\t"
  "v_mfma_f32_16x16x32_bf16 v[180:183], v[244:247], a[20:23], v[180:183]\n\t"
  "ds_read_b64_tr_b16 v[236:237], v15 offset:4608\n\t"
  "ds_read_b64_tr_b16 v[238:239], v15 offset:6656\n\t"
  "v_mfma_f32_16x16x32_bf16 v[184:187], v[240:243], a[8:11], 0\n\t"
  "s_mov_b32 s40, 38912\n\t"
  "buffer_load_dwordx4 a[104:107], v1, s[36:39], s40 offen offset:0\n\t"
  "v_mfma_f32_16x16x32_bf16 v[184:187], v[244:247], a[24:27], v[184:187]\n\t"
  "v_mfma_f32_16x16x32_bf16 v[188:191], v[240:243], a[12:15], 0\n\t"
  "buffer_load_dwordx4 a[120:123], v1, s[36:39], s40 offen offset:1024\n\t"
  "v_mfma_f32_16x16x32_bf16 v[188:191], v[244:247], a[28:31], v[188:191]\n\t"
  "s_waitcnt vmcnt(25)\n\t"
  "s_barrier\n\t"
  "v_mfma_f32_16x16x32_bf16 v[192:195], v[248:251], a[0:3], 0\n\t"
  "s_waitcnt lgkmcnt(0)\n\t"
  "s_mov_b32 s40, 55296\n\t"
  "s_nop 0\n\t"
  "buffer_load_dwordx4 a[108:111], v1, s[36:39], s40 offen offset:0\n\t"
  "v_mfma_f32_16x16x32_bf16 v[192:195], v[252:255], a[16:19], v[192:195]\n\t"
  "ds_read_b64_tr_b16 v[240:241], v14 offset:8192\n\t"
  "ds_read_b64_tr_b16 v[242:243], v14 offset:10240\n\t"
  "v_mfma_f32_16x16x32_bf16 v[196:199], v[248:251], a[4:7], 0\n\t"
  "buffer_load_dwordx4 a[124:127], v1, s[36:39], s40 offen offset:1024\n\t"
  "v_mfma_f32_16x16x32_bf16 v[196:199], v[252:255], a[20:23], v[196:199]\n\t"
  "ds_read_b64_tr_b16 v[244:245], v14 offset:12288\n\t"
  "ds_read_b64_tr_b16 v[246:247], v14 offset:14336\n\t"
  "v_mfma_f32_16x16x32_bf16 v[200:203], v[248:251], a[8:11], 0\n\t"
  "v_mfma_f32_16x16x32_bf16 v[200:203], v[252:255], a[24:27], v[200:203]\n\t"
  "v_mfma_f32_16x16x32_bf16 v[204:207], v[248:251], a[12:15], 0\n\t"
  "v_mfma_f32_16x16x32_bf16 v[204:207], v[252:255], a[28:31], v[204:207]\n\t"
  "v_mfma_f32_16x16x32_bf16 v[208:211], v[232:235], a[0:3], 0\n\t"
  "v_mfma_f32_16x16x32_bf16 v[208:211], v[236:239], a[16:19], v[208:211]\n\t"
  "ds_read_b64_tr_b16 v[248:249], v15 offset:8192\n\t"
  "ds_read_b64_tr_b16 v[250:251], v15 offset:10240\n\t"
  "v_mfma_f32_16x16x32_bf16 v[212:215], v[232:235], a[4:7], 0\n\t"
  "v_mfma_f32_16x16x32_bf16 v[212:215], v[236:239], a[20:23], v[212:215]\n\t"
  "ds_read_b64_tr_b16 v[252:253], v15 offset:12288\n\t"
  "ds_read_b64_tr_b16 v[254:255], v15 offset:14336\n\t"
  "v_mfma_f32_16x16x32_bf16 v[216:219], v[232:235], a[8:11], 0\n\t"
  "v_mfma_f32_16x16x32_bf16 v[216:219], v[236:239], a[24:27], v[216:219]\n\t"
  "v_mfma_f32_16x16x32_bf16 v[220:223], v[232:235], a[12:15], 0\n\t"
  "v_mfma_f32_16x16x32_bf16 v[220:223], v[236:239], a[28:31], v[220:223]\n\t"
  "s_waitcnt lgkmcnt(2)\n\t"
  "s_nop 0\n\t"
  "v_mfma_f32_16x16x32_bf16 v[160:163], v[240:243], a[32:35], v[160:163]\n\t"
  "s_add_u32 m0, s42, 32768\n\t"
  "s_add_u32 s41, s43, 917504\n\t"
  "buffer_load_dwordx4 v18, s[24:27], s41 offen lds\n\t"
  "v_mfma_f32_16x16x32_bf16 v[160:163], v[244:247], a[48:51], v[160:163]\n\t"
  "ds_read_b64_tr_b16 v[232:233], v14 offset:8704\n\t"
  "ds_read_b64_tr_b16 v[234:235], v14 offset:10752\n\t"
  "v_mfma_f32_16x16x32_bf16 v[164:167], v[240:243], a[36:39], v[164:167]\n\t"
  "s_add_u32 m0, s42, 33792\n\t"
  "s_add_u32 s41, s43, 946176\n\t"
  "buffer_load_dwordx4 v18, s[24:27], s41 offen lds\n\t"
  "v_mfma_f32_16x16x32_bf16 v[164:167], v[244:247], a[52:55], v[164:167]\n\t"
  "ds_read_b64_tr_b16 v[236:237], v14 offset:12800\n\t"
  "ds_read_b64_tr_b16 v[238:239], v14 offset:14848\n\t"
  "v_mfma_f32_16x16x32_bf16 v[168:171], v[240:243], a[40:43], v[168:171]\n\t"
  "s_mov_b32 s40, 8192\n\t"
  "buffer_load_dwordx4 a[128:131], v1, s[36:39], s40 offen offset:0\n\t"
  "v_mfma_f32_16x16x32_bf16 v[168:171], v[244:247], a[56:59], v[168:171]\n\t"
  "v_mfma_f32_16x16x32_bf16 v[172:175], v[240:243], a[44:47], v[172:175]\n\t"
  "buffer_load_dwordx4 a[144:147], v1, s[36:39], s40 offen offset:1024\n\t"
  "v_mfma_f32_16x16x32_bf16 v[172:175], v[244:247], a[60:63], v[172:175]\n\t"
  "v_mfma_f32_16x16x32_bf16 v[176:179], v[248:251], a[32:35], v[176:179]\n\t"
  "s_waitcnt lgkmcnt(0)\n\t"
  "s_mov_b32 s40, 24576\n\t"
  "s_nop 0\n\t"
  "buffer_load_dwordx4 a[132:135], v1, s[36:39], s40 offen offset:0\n\t"
  "v_mfma_f32_16x16x32_bf16 v[176:179], v[252:255], a[48:51], v[176:179]\n\t"
  "ds_read_b64_tr_b16 v[240:241], v15 offset:8704\n\t"
  "ds_read_b64_tr_b16 v[242:243], v15 offset:10752\n\t"
  "v_mfma_f32_16x16x32_bf16 v[180:183], v[248:251], a[36:39], v[180:183]\n\t"
  "buffer_load_dwordx4 a[148:151], v1, s[36:39], s40 offen offset:1024\n\t"
  "v_mfma_f32_16x16x32_bf16 v[180:183], v[252:255], a[52:55], v[180:183]\n\t"
  "ds_read_b64_tr_b16 v[244:245], v15 offset:12800\n\t"
  "ds_read_b64_tr_b16 v[246:247], v15 offset:14848\n\t"
  "v_mfma_f32_16x16x32_bf16 v[184:187], v[248:251], a[40:43], v[184:187]\n\t"
  "s_mov_b32 s40, 40960\n\t"
  "buffer_load_dwordx4 a[136:139], v1, s[36:39], s40 offen offset:0\n\t"
  "v_mfma_f32_16x16x32_bf16 v[184:187], v[252:255], a[56:59], v[184:187]\n\t"
  "v_mfma_f32_16x16x32_bf16 v[188:191], v[248:251], a[44:47], v[188:191]\n\t"
  "buffer_load_dwordx4 a[152:155], v1, s[36:39], s40 offen offset:1024\n\t"
  "v_mfma_f32_16x16x32_bf16 v[188:191], v[252:255], a[60:63], v[188:191]\n\t"
  "s_waitcnt vmcnt(18)\n\t"
  "s_barrier\n\t"
  "v_mfma_f32_16x16x32_bf16 v[192:195], v[232:235], a[32:35], v[192:195]\n\t"
  "s_mov_b32 s40, 57344\n\t"
  "buffer_load_dwordx4 a[140:143], v1, s[36:39], s40 offen offset:0\n\t"
  "v_mfma_f32_16x16x32_bf16 v[192:195], v[236:239], a[48:51], v[192:195]\n\t"
  "ds_read_b64_tr_b16 v[248:249], v14 offset:16384\n\t"
  "ds_read_b64_tr_b16 v[250:251], v14 offset:18432\n\t"
  "v_mfma_f32_16x16x32_bf16 v[196:199], v[232:235], a[36:39], v[196:199]\n\t"
  "buffer_load_dwordx4 a[156:159], v1, s[36:39], s40 offen offset:1024\n\t"
  "v_mfma_f32_16x16x32_bf16 v[196:199], v[236:239], a[52:55], v[196:199]\n\t"
  "ds_read_b64_tr_b16 v[252:253], v14 offset:20480\n\t"
  "ds_read_b64_tr_b16 v[254:255], v14 offset:22528\n\t"
  "v_mfma_f32_16x16x32_bf16 v[200:203], v[232:235], a[40:43], v[200:203]\n\t"
  "v_mfma_f32_16x16x32_bf16 v[200:203], v[236:239], a[56:59], v[200:203]\n\t"
  "v_mfma_f32_16x16x32_bf16 v[204:207], v[232:235], a[44:47], v[204:207]\n\t"
  "v_mfma_f32_16x16x32_bf16 v[204:207], v[236:239], a[60:63], v[204:207]\n\t"
  "s_waitcnt lgkmcnt(2)\n\t"
  "s_nop 0\n\t"
  "v_mfma_f32_16x16x32_bf16 v[208:211], v[240:243], a[32:35], v[208:211]\n\t"
  "v_mfma_f32_16x16x32_bf16 v[208:211], v[244:247], a[48:51], v[208:211]\n\t"
  "ds_read_b64_tr_b16 v[232:233], v15 offset:16384\n\t"
  "ds_read_b64_tr_b16 v[234:235], v15 offset:18432\n\t"
  "v_mfma_f32_16x16x32_bf16 v[212:215], v[240:243], a[36:39], v[212:215]\n\t"
  "v_mfma_f32_16x16x32_bf16 v[212:215], v[244:247], a[52:55], v[212:215]\n\t"
  "ds_read_b64_tr_b16 v[236:237], v15 offset:20480\n\t"
  "ds_read_b64_tr_b16 v[238:239], v15 offset:22528\n\t"
  "v_mfma_f32_16x16x32_bf16 v[216:219], v[240:243], a[40:43], v[216:219]\n\t"
  "v_mfma_f32_16x16x32_bf16 v[216:219], v[244:247], a[56:59], v[216:219]\n\t"
  "v_mfma_f32_16x16x32_bf16 v[220:223], v[240:243], a[44:47], v[220:223]\n\t"
  "v_mfma_f32_16x16x32_bf16 v[220:223], v[244:247], a[60:63], v[220:223]\n\t"
  "v_mfma_f32_16x16x32_bf16 v[160:163], v[248:251], a[64:67], v[160:163]\n\t"
  "s_waitcnt lgkmcnt(0)\n\t"
  "s_add_u32 m0, s42, 40960\n\t"
  "s_add_u32 s41, s43, 1146880\n\t"
  "s_nop 0\n\t"
  "buffer_load_dwordx4 v18, s[24:27], s41 offen lds\n\t"
  "v_mfma_f32_16x16x32_bf16 v[160:163], v[252:255], a[80:83], v[160:163]\n\t"
  "ds_read_b64_tr_b16 v[240:241], v14 offset:16896\n\t"
  "ds_read_b64_tr_b16 v[242:243], v14 offset:18944\n\t"
  "v_mfma_f32_16x16x32_bf16 v[164:167], v[248:251], a[68:71], v[164:167]\n\t"
  "s_add_u32 m0, s42, 41984\n\t"
  "s_add_u32 s41, s43, 1175552\n\t"
  "buffer_load_dwordx4 v18, s[24:27], s41 offen lds\n\t"
  "v_mfma_f32_16x16x32_bf16 v[164:167], v[252:255], a[84:87], v[164:167]\n\t"
  "ds_read_b64_tr_b16 v[244:245], v14 offset:20992\n\t"
  "ds_read_b64_tr_b16 v[246:247], v14 offset:23040\n\t"
  "v_mfma_f32_16x16x32_bf16 v[168:171], v[248:251], a[72:75], v[168:171]\n\t"
  "s_mov_b32 s40, 10240\n\t"
  "buffer_load_dwordx4 a[160:163], v1, s[36:39], s40 offen offset:0\n\t"
  "v_mfma_f32_16x16x32_bf16 v[168:171], v[252:255], a[88:91], v[168:171]\n\t"
  "v_mfma_f32_16x16x32_bf16 v[172:175], v[248:251], a[76:79], v[172:175]\n\t"
  "buffer_load_dwordx4 a[176:179], v1, s[36:39], s40 offen offset:1024\n\t"
  "v_mfma_f32_16x16x32_bf16 v[172:175], v[252:255], a[92:95], v[172:175]\n\t"
  "v_mfma_f32_16x16x32_bf16 v[176:179], v[232:235], a[64:67], v[176:179]\n\t"
  "s_mov_b32 s40, 26624\n\t"
  "buffer_load_dwordx4 a[164:167], v1, s[36:39], s40 offen offset:0\n\t"
  "v_mfma_f32_16x16x32_bf16 v[176:179], v[236:239], a[80:83], v[176:179]\n\t"
  "ds_read_b64_tr_b16 v[248:249], v15 offset:16896\n\t"
  "ds_read_b64_tr_b16 v[250:251], v15 offset:18944\n\t"
  "v_mfma_f32_16x16x32_bf16 v[180:183], v[232:235], a[68:71], v[180:183]\n\t"
  "buffer_load_dwordx4 a[180:183], v1, s[36:39], s40 offen offset:1024\n\t"
  "v_mfma_f32_16x16x32_bf16 v[180:183], v[236:239], a[84:87], v[180:183]\n\t"
  "ds_read_b64_tr_b16 v[252:253], v15 offset:20992\n\t"
  "ds_read_b64_tr_b16 v[254:255], v15 offset:23040\n\t"
  "v_mfma_f32_16x16x32_bf16 v[184:187], v[232:235], a[72:75], v[184:187]\n\t"
  "s_mov_b32 s40, 43008\n\t"
  "buffer_load_dwordx4 a[168:171], v1, s[36:39], s40 offen offset:0\n\t"
  "v_mfma_f32_16x16x32_bf16 v[184:187], v[236:239], a[88:91], v[184:187]\n\t"
  "v_mfma_f32_16x16x32_bf16 v[188:191], v[232:235], a[76:79], v[188:191]\n\t"
  "buffer_load_dwordx4 a[184:187], v1, s[36:39], s40 offen offset:1024\n\t"
  "v_mfma_f32_16x16x32_bf16 v[188:191], v[236:239], a[92:95], v[188:191]\n\t"
  "s_waitcnt vmcnt(18)\n\t"
  "s_barrier\n\t"
  "s_waitcnt lgkmcnt(2)\n\t"
  "s_nop 0\n\t"
  "v_mfma_f32_16x16x32_bf16 v[192:195], v[240:243], a[64:67], v[192:195]\n\t"
  "s_mov_b32 s40, 59392\n\t"
  "buffer_load_dwordx4 a[172:175], v1, s[36:39], s40 offen offset:0\n\t"
  "v_mfma_f32_16x16x32_bf16 v[192:195], v[244:247], a[80:83], v[192:195]\n\t"
  "ds_read_b64_tr_b16 v[232:233], v14 offset:24576\n\t"
  "ds_read_b64_tr_b16 v[234:235], v14 offset:26624\n\t"
  "v_mfma_f32_16x16x32_bf16 v[196:199], v[240:243], a[68:71], v[196:199]\n\t"
  "buffer_load_dwordx4 a[188:191], v1, s[36:39], s40 offen offset:1024\n\t"
  "v_mfma_f32_16x16x32_bf16 v[196:199], v[244:247], a[84:87], v[196:199]\n\t"
  "ds_read_b64_tr_b16 v[236:237], v14 offset:28672\n\t"
  "ds_read_b64_tr_b16 v[238:239], v14 offset:30720\n\t"
  "v_mfma_f32_16x16x32_bf16 v[200:203], v[240:243], a[72:75], v[200:203]\n\t"
  "v_mfma_f32_16x16x32_bf16 v[200:203], v[244:247], a[88:91], v[200:203]\n\t"
  "v_mfma_f32_16x16x32_bf16 v[204:207], v[240:243], a[76:79], v[204:207]\n\t"
  "v_mfma_f32_16x16x32_bf16 v[204:207], v[244:247], a[92:95], v[204:207]\n\t"
  "v_mfma_f32_16x16x32_bf16 v[208:211], v[248:251], a[64:67], v[208:211]\n\t"
  "s_waitcnt lgkmcnt(0)\n\t"
  "s_nop 0\n\t"
  "v_mfma_f32_16x16x32_bf16 v[208:211], v[252:255], a[80:83], v[208:211]\n\t"
  "ds_read_b64_tr_b16 v[240:241], v15 offset:24576\n\t"
  "ds_read_b64_tr_b16 v[242:243], v15 offset:26624\n\t"
  "v_mfma_f32_16x16x32_bf16 v[212:215], v[248:251], a[68:71], v[212:215]\n\t"
  "v_mfma_f32_16x16x32_bf16 v[212:215], v[252:255], a[84:87], v[212:215]\n\t"
  "ds_read_b64_tr_b16 v[244:245], v15 offset:28672\n\t"
  "ds_read_b64_tr_b16 v[246:247], v15 offset:30720\n\t"
  "v_mfma_f32_16x16x32_bf16 v[216:219], v[248:251], a[72:75], v[216:219]\n\t"
  "v_mfma_f32_16x16x32_bf16 v[216:219], v[252:255], a[88:91], v[216:219]\n\t"
  "v_mfma_f32_16x16x32_bf16 v[220:223], v[248:251], a[76:79], v[220:223]\n\t"
  "v_mfma_f32_16x16x32_bf16 v[220:223], v[252:255], a[92:95], v[220:223]\n\t"
  "v_mfma_f32_16x16x32_bf16 v[160:163], v[232:235], a[96:99], v[160:163]\n\t"
  "s_add_u32 m0, s42, 49152\n\t"
  "s_add_u32 s41, s43, 1376256\n\t"
  "buffer_load_dwordx4 v18, s[24:27], s41 offen lds\n\t"
  "v_mfma_f32_16x16x32_bf16 v[160:163], v[236:239], a[112:115], v[160:163]\n\t"
  "ds_read_b64_tr_b16 v[248:249], v14 offset:25088\n\t"
  "ds_read_b64_tr_b16 v[250:251], v14 offset:27136\n\t"
  "v_mfma_f32_16x16x32_bf16 v[164:167], v[232:235], a[100:103], v[164:167]\n\t"
  "s_add_u32 m0, s42, 50176\n\t"
  "s_add_u32 s41, s43, 1404928\n\t"
  "buffer_load_dwordx4 v18, s[24:27], s41 offen lds\n\t"
  "v_mfma_f32_16x16x32_bf16 v[164:167], v[236:239], a[116:119], v[164:167]\n\t"
  "ds_read_b64_tr_b16 v[252:253], v14 offset:29184\n\t"
  "ds_read_b64_tr_b16 v[254:255], v14 offset:31232\n\t"
  "v_mfma_f32_16x16x32_bf16 v[168:171], v[232:235], a[104:107], v[168:171]\n\t"
  "s_mov_b32 s40, 12288\n\t"
  "buffer_load_dwordx4 a[192:195], v1, s[36:39], s40 offen offset:0\n\t"
  "v_mfma_f32_16x16x32_bf16 v[168:171], v[236:239], a[120:123], v[168:171]\n\t"
  "v_mfma_f32_16x16x32_bf16 v[172:175], v[232:235], a[108:111], v[172:175]\n\t"
  "buffer_load_dwordx4 a[208:211], v1, s[36:39], s40 offen offset:1024\n\t"
  "v_mfma_f32_16x16x32_bf16 v[172:175], v[236:239], a[124:127], v[172:175]\n\t"
  "s_waitcnt lgkmcnt(2)\n\t"
  "s_nop 0\n\t"
  "v_mfma_f32_16x16x32_bf16 v[176:179], v[240:243], a[96:99], v[176:179]\n\t"
  "s_mov_b32 s40, 28672\n\t"
  "buffer_load_dwordx4 a[196:199], v1, s[36:39], s40 offen offset:0\n\t"
  "v_mfma_f32_16x16x32_bf16 v[176:179], v[244:247], a[112:115], v[176:179]\n\t"
  "ds_read_b64_tr_b16 v[232:233], v15 offset:25088\n\t"
  "ds_read_b64_tr_b16 v[234:235], v15 offset:27136\n\t"
  "v_mfma_f32_16x16x32_bf16 v[180:183], v[240:243], a[100:103], v[180:183]\n\t"
  "buffer_load_dwordx4 a[212:215], v1, s[36:39], s40 offen offset:1024\n\t"
  "v_mfma_f32_16x16x32_bf16 v[180:183], v[244:247], a[116:119], v[180:183]\n\t"
  "ds_read_b64_tr_b16 v[236:237], v15 offset:29184\n\t"
  "ds_read_b64_tr_b16 v[238:239], v15 offset:31232\n\t"
  "v_mfma_f32_16x16x32_bf16 v[184:187], v[240:243], a[104:107], v[184:187]\n\t"
  "s_mov_b32 s40, 45056\n\t"
  "buffer_load_dwordx4 a[200:203], v1, s[36:39], s40 offen offset:0\n\t"
  "v_mfma_f32_16x16x32_bf16 v[184:187], v[244:247], a[120:123], v[184:187]\n\t"
  "v_mfma_f32_16x16x32_bf16 v[188:191], v[240:243], a[108:111], v[188:191]\n\t"
  "buffer_load_dwordx4 a[216:219], v1, s[36:39], s40 offen offset:1024\n\t"
  "v_mfma_f32_16x16x32_bf16 v[188:191], v[244:247], a[124:127], v[188:191]\n\t"
  "s_waitcnt vmcnt(18)\n\t"
  "s_barrier\n\t"
  "v_mfma_f32_16x16x32_bf16 v[192:195], v[248:251], a[96:99], v[192:195]\n\t"
  "s_waitcnt lgkmcnt(0)\n\t"
  "s_mov_b32 s40, 61440\n\t"
  "s_nop 0\n\t"
  "buffer_load_dwordx4 a[204:207], v1, s[36:39], s40 offen offset:0\n\t"
  "v_mfma_f32_16x16x32_bf16 v[192:195], v[252:255], a[112:115], v[192:195]\n\t"
  "ds_read_b64_tr_b16 v[240:241], v14 offset:32768\n\t"
  "ds_read_b64_tr_b16 v[242:243], v14 offset:34816\n\t"
  "v_mfma_f32_16x16x32_bf16 v[196:199], v[248:251], a[100:103], v[196:199]\n\t"
  "buffer_load_dwordx4 a[220:223], v1, s[36:39], s40 offen offset:1024\n\t"
  "v_mfma_f32_16x16x32_bf16 v[196:199], v[252:255], a[116:119], v[196:199]\n\t"
  "ds_read_b64_tr_b16 v[244:245], v14 offset:36864\n\t"
  "ds_read_b64_tr_b16 v[246:247], v14 offset:38912\n\t"
  "v_mfma_f32_16x16x32_bf16 v[200:203], v[248:251], a[104:107], v[200:203]\n\t"
  "v_mfma_f32_16x16x32_bf16 v[200:203], v[252:255], a[120:123], v[200:203]\n\t"
  "v_mfma_f32_16x16x32_bf16 v[204:207], v[248:251], a[108:111], v[204:207]\n\t"
  "v_mfma_f32_16x16x32_bf16 v[204:207], v[252:255], a[124:127], v[204:207]\n\t"
  "v_mfma_f32_16x16x32_bf16 v[208:211], v[232:235], a[96:99], v[208:211]\n\t"
  "v_mfma_f32_16x16x32_bf16 v[208:211], v[236:239], a[112:115], v[208:211]\n\t"
  "ds_read_b64_tr_b16 v[248:249], v15 offset:32768\n\t"
  "ds_read_b64_tr_b16 v[250:251], v15 offset:34816\n\t"
  "v_mfma_f32_16x16x32_bf16 v[212:215], v[232:235], a[100:103], v[212:215]\n\t"
  "v_mfma_f32_16x16x32_bf16 v[212:215], v[236:239], a[116:119], v[212:215]\n\t"
  "ds_read_b64_tr_b16 v[252:253], v15 offset:36864\n\t"
  "ds_read_b64_tr_b16 v[254:255], v15 offset:38912\n\t"
  "v_mfma_f32_16x16x32_bf16 v[216:219], v[232:235], a[104:107], v[216:219]\n\t"
  "v_mfma_f32_16x16x32_bf16 v[216:219], v[236:239], a[120:123], v[216:219]\n\t"
  "v_mfma_f32_16x16x32_bf16 v[220:223], v[232:235], a[108:111], v[220:223]\n\t"
  "v_mfma_f32_16x16x32_bf16 v[220:223], v[236:239], a[124:127], v[220:223]\n\t"
  "s_waitcnt lgkmcnt(2)\n\t"
  "s_nop 0\n\t"
  "v_mfma_f32_16x16x32_bf16 v[160:163], v[240:243], a[128:131], v[160:163]\n\t"
  "s_add_u32 m0, s42, 57344\n\t"
  "s_add_u32 s41, s43, 1605632\n\t"
  "buffer_load_dwordx4 v18, s[24:27], s41 offen lds\n\t"
  "v_mfma_f32_16x16x32_bf16 v[160:163], v[244:247], a[144:147], v[160:163]\n\t"
  "ds_read_b64_tr_b16 v[232:233], v14 offset:33280\n\t"
  "ds_read_b64_tr_b16 v[234:235], v14 offset:35328\n\t"
  "v_mfma_f32_16x16x32_bf16 v[164:167], v[240:243], a[132:135], v[164:167]\n\t"
  "s_add_u32 m0, s42, 58368\n\t"
  "s_add_u32 s41, s43, 1634304\n\t"
  "buffer_load_dwordx4 v18, s[24:27], s41 offen lds\n\t"
  "v_mfma_f32_16x16x32_bf16 v[164:167], v[244:247], a[148:151], v[164:167]\n\t"
  "ds_read_b64_tr_b16 v[236:237], v14 offset:37376\n\t"
  "ds_read_b64_tr_b16 v[238:239], v14 offset:39424\n\t"
  "v_mfma_f32_16x16x32_bf16 v[168:171], v[240:243], a[136:139], v[168:171]\n\t"
  "s_mov_b32 s40, 14336\n\t"
  "buffer_load_dwordx4 a[224:227], v1, s[36:39], s40 offen offset:0\n\t"
  "v_mfma_f32_16x16x32_bf16 v[168:171], v[244:247], a[152:155], v[168:171]\n\t"
  "v_mfma_f32_16x16x32_bf16 v[172:175], v[240:243], a[140:143], v[172:175]\n\t"
  "buffer_load_dwordx4 a[240:243], v1, s[36:39], s40 offen offset:1024\n\t"
  "v_mfma_f32_16x16x32_bf16 v[172:175], v[244:247], a[156:159], v[172:175]\n\t"
  "v_mfma_f32_16x16x32_bf16 v[176:179], v[248:251], a[128:131], v[176:179]\n\t"
  "s_waitcnt lgkmcnt(0)\n\t"
  "s_mov_b32 s40, 30720\n\t"
  "s_nop 0\n\t"
  "buffer_load_dwordx4 a[228:231], v1, s[36:39], s40 offen offset:0\n\t"
  "v_mfma_f32_16x16x32_bf16 v[176:179], v[252:255], a[144:147], v[176:179]\n\t"
  "ds_read_b64_tr_b16 v[240:241], v15 offset:33280\n\t"
  "ds_read_b64_tr_b16 v[242:243], v15 offset:35328\n\t"
  "v_mfma_f32_16x16x32_bf16 v[180:183], v[248:251], a[132:135], v[180:183]\n\t"
  "buffer_load_dwordx4 a[244:247], v1, s[36:39], s40 offen offset:1024\n\t"
  "v_mfma_f32_16x16x32_bf16 v[180:183], v[252:255], a[148:151], v[180:183]\n\t"
  "ds_read_b64_tr_b16 v[244:245], v15 offset:37376\n\t"
  "ds_read_b64_tr_b16 v[246:247], v15 offset:39424\n\t"
  "v_mfma_f32_16x16x32_bf16 v[184:187], v[248:251], a[136:139], v[184:187]\n\t"
  "s_mov_b32 s40, 47104\n\t"
  "buffer_load_dwordx4 a[232:235], v1, s[36:39], s40 offen offset:0\n\t"
  "v_mfma_f32_16x16x32_bf16 v[184:187], v[252:255], a[152:155], v[184:187]\n\t"
  "v_mfma_f32_16x16x32_bf16 v[188:191], v[248:251], a[140:143], v[188:191]\n\t"
  "buffer_load_dwordx4 a[248:251], v1, s[36:39], s40 offen offset:1024\n\t"
  "v_mfma_f32_16x16x32_bf16 v[188:191], v[252:255], a[156:159], v[188:191]\n\t"
  "s_waitcnt vmcnt(18)\n\t"
  "s_barrier\n\t"
  "v_mfma_f32_16x16x32_bf16 v[192:195], v[232:235], a[128:131], v[192:195]\n\t"
  "s_mov_b32 s40, 63488\n\t"
  "buffer_load_dwordx4 a[236:239], v1, s[36:39], s40 offen offset:0\n\t"
  "v_mfma_f32_16x16x32_bf16 v[192:195], v[236:239], a[144:147], v[192:195]\n\t"
  "ds_read_b64_tr_b16 v[248:249], v14 offset:40960\n\t"
  "ds_read_b64_tr_b16 v[250:251], v14 offset:43008\n\t"
  "v_mfma_f32_16x16x32_bf16 v[196:199], v[232:235], a[132:135], v[196:199]\n\t"
  "buffer_load_dwordx4 a[252:255], v1, s[36:39], s40 offen offset:1024\n\t"
  "v_mfma_f32_16x16x32_bf16 v[196:199], v[236:239], a[148:151], v[196:199]\n\t"
  "ds_read_b64_tr_b16 v[252:253], v14 offset:45056\n\t"
  "ds_read_b64_tr_b16 v[254:255], v14 offset:47104\n\t"
  "v_mfma_f32_16x16x32_bf16 v[200:203], v[232:235], a[136:139], v[200:203]\n\t"
  "v_mfma_f32_16x16x32_bf16 v[200:203], v[236:239], a[152:155], v[200:203]\n\t"
  "v_mfma_f32_16x16x32_bf16 v[204:207], v[232:235], a[140:143], v[204:207]\n\t"
  "v_mfma_f32_16x16x32_bf16 v[204:207], v[236:239], a[156:159], v[204:207]\n\t"
  "s_waitcnt lgkmcnt(2)\n\t"
  "s_nop 0\n\t"
  "v_mfma_f32_16x16x32_bf16 v[208:211], v[240:243], a[128:131], v[208:211]\n\t"
  "v_mfma_f32_16x16x32_bf16 v[208:211], v[244:247], a[144:147], v[208:211]\n\t"
  "ds_read_b64_tr_b16 v[232:233], v15 offset:40960\n\t"
  "ds_read_b64_tr_b16 v[234:235], v15 offset:43008\n\t"
  "v_mfma_f32_16x16x32_bf16 v[212:215], v[240:243], a[132:135], v[212:215]\n\t"
  "v_mfma_f32_16x16x32_bf16 v[212:215], v[244:247], a[148:151], v[212:215]\n\t"
  "ds_read_b64_tr_b16 v[236:237], v15 offset:45056\n\t"
  "ds_read_b64_tr_b16 v[238:239], v15 offset:47104\n\t"
  "v_mfma_f32_16x16x32_bf16 v[216:219], v[240:243], a[136:139], v[216:219]\n\t"
  "v_mfma_f32_16x16x32_bf16 v[216:219], v[244:247], a[152:155], v[216:219]\n\t"
  "v_mfma_f32_16x16x32_bf16 v[220:223], v[240:243], a[140:143], v[220:223]\n\t"
  "v_mfma_f32_16x16x32_bf16 v[220:223], v[244:247], a[156:159], v[220:223]\n\t"
  "v_mfma_f32_16x16x32_bf16 v[160:163], v[248:251], a[160:163], v[160:163]\n\t"
  "s_waitcnt lgkmcnt(0)\n\t"
  "s_add_u32 m0, s44, 98304\n\t"
  "s_add_u32 s41, s45, 0\n\t"
  "buffer_load_dwordx4 v19, s[28:31], s41 offen lds\n\t"
  "v_mfma_f32_16x16x32_bf16 v[160:163], v[252:255], a[176:179], v[160:163]\n\t"
  "ds_read_b64_tr_b16 v[240:241], v14 offset:41472\n\t"
  "ds_read_b64_tr_b16 v[242:243], v14 offset:43520\n\t"
  "v_mfma_f32_16x16x32_bf16 v[164:167], v[248:251], a[164:167], v[164:167]\n\t"
  "s_add_u32 m0, s44, 99328\n\t"
  "s_add_u32 s41, s45, 128\n\t"
  "buffer_load_dwordx4 v19, s[28:31], s41 offen lds\n\t"
  "v_mfma_f32_16x16x32_bf16 v[164:167], v[252:255], a[180:183], v[164:167]\n\t"
  "ds_read_b64_tr_b16 v[244:245], v14 offset:45568\n\t"
  "ds_read_b64_tr_b16 v[246:247], v14 offset:47616\n\t"
  "v_mfma_f32_16x16x32_bf16 v[168:171], v[248:251], a[168:171], v[168:171]\n\t"
  "s_add_u32 m0, s44, 100352\n\t"
  "s_add_u32 s41, s45, 65536\n\t"
  "buffer_load_dwordx4 v19, s[28:31], s41 offen lds\n\t"
  "v_mfma_f32_16x16x32_bf16 v[168:171], v[252:255], a[184:187], v[168:171]\n\t"
  "v_mfma_f32_16x16x32_bf16 v[172:175], v[248:251], a[172:175], v[172:175]\n\t"
  "s_add_u32 m0, s44, 101376\n\t"
  "s_add_u32 s41, s45, 65664\n\t"
  "buffer_load_dwordx4 v19, s[28:31], s41 offen lds\n\t"
  "v_mfma_f32_16x16x32_bf16 v[172:175], v[252:255], a[188:191], v[172:175]\n\t"
  "v_mfma_f32_16x16x32_bf16 v[176:179], v[232:235], a[160:163], v[176:179]\n\t"
  "v_mfma_f32_16x16x32_bf16 v[176:179], v[236:239], a[176:179], v[176:179]\n\t"
  "ds_read_b64_tr_b16 v[248:249], v15 offset:41472\n\t"
  "ds_read_b64_tr_b16 v[250:251], v15 offset:43520\n\t"
  "v_mfma_f32_16x16x32_bf16 v[180:183], v[232:235], a[164:167], v[180:183]\n\t"
  "v_mfma_f32_16x16x32_bf16 v[180:183], v[236:239], a[180:183], v[180:183]\n\t"
  "ds_read_b64_tr_b16 v[252:253], v15 offset:45568\n\t"
  "ds_read_b64_tr_b16 v[254:255], v15 offset:47616\n\t"
  "v_mfma_f32_16x16x32_bf16 v[184:187], v[232:235], a[168:171], v[184:187]\n\t"
  "v_mfma_f32_16x16x32_bf16 v[184:187], v[236:239], a[184:187], v[184:187]\n\t"
  "v_mfma_f32_16x16x32_bf16 v[188:191], v[232:235], a[172:175], v[188:191]\n\t"
  "v_mfma_f32_16x16x32_bf16 v[188:191], v[236:239], a[188:191], v[188:191]\n\t"
  "s_waitcnt vmcnt(14)\n\t"
  "s_barrier\n\t"
  "s_waitcnt lgkmcnt(2)\n\t"
  "s_nop 0\n\t"
  "v_mfma_f32_16x16x32_bf16 v[192:195], v[240:243], a[160:163], v[192:195]\n\t"
  "v_mfma_f32_16x16x32_bf16 v[192:195], v[244:247], a[176:179], v[192:195]\n\t"
  "ds_read_b64_tr_b16 v[232:233], v14 offset:49152\n\t"
  "ds_read_b64_tr_b16 v[234:235], v14 offset:51200\n\t"
  "v_mfma_f32_16x16x32_bf16 v[196:199], v[240:243], a[164:167], v[196:199]\n\t"
  "v_mfma_f32_16x16x32_bf16 v[196:199], v[244:247], a[180:183], v[196:199]\n\t"
  "ds_read_b64_tr_b16 v[236:237], v14 offset:53248\n\t"
  "ds_read_b64_tr_b16 v[238:239], v14 offset:55296\n\t"
  "v_mfma_f32_16x16x32_bf16 v[200:203], v[240:243], a[168:171], v[200:203]\n\t"
  "v_mfma_f32_16x16x32_bf16 v[200:203], v[244:247], a[184:187], v[200:203]\n\t"
  "v_mfma_f32_16x16x32_bf16 v[204:207], v[240:243], a[172:175], v[204:207]\n\t"
  "v_mfma_f32_16x16x32_bf16 v[204:207], v[244:247], a[188:191], v[204:207]\n\t"
  "v_mfma_f32_16x16x32_bf16 v[208:211], v[248:251], a[160:163], v[208:211]\n\t"
  "s_waitcnt lgkmcnt(0)\n\t"
  "s_nop 0\n\t"
  "v_mfma_f32_16x16x32_bf16 v[208:211], v[252:255], a[176:179], v[208:211]\n\t"
  "ds_read_b64_tr_b16 v[240:241], v15 offset:49152\n\t"
  "ds_read_b64_tr_b16 v[242:243], v15 offset:51200\n\t"
  "v_mfma_f32_16x16x32_bf16 v[212:215], v[248:251], a[164:167], v[212:215]\n\t"
  "v_mfma_f32_16x16x32_bf16 v[212:215], v[252:255], a[180:183], v[212:215]\n\t"
  "ds_read_b64_tr_b16 v[244:245], v15 offset:53248\n\t"
  "ds_read_b64_tr_b16 v[246:247], v15 offset:55296\n\t"
  "v_mfma_f32_16x16x32_bf16 v[216:219], v[248:251], a[168:171], v[216:219]\n\t"
  "v_mfma_f32_16x16x32_bf16 v[216:219], v[252:255], a[184:187], v[216:219]\n\t"
  "v_mfma_f32_16x16x32_bf16 v[220:223], v[248:251], a[172:175], v[220:223]\n\t"
  "v_mfma_f32_16x16x32_bf16 v[220:223], v[252:255], a[188:191], v[220:223]\n\t"
  "v_mfma_f32_16x16x32_bf16 v[160:163], v[232:235], a[192:195], v[160:163]\n\t"
  "s_add_u32 m0, s44, 114688\n\t"
  "s_add_u32 s41, s45, 524288\n\t"
  "buffer_load_dwordx4 v19, s[28:31], s41 offen lds\n\t"
  "v_mfma_f32_16x16x32_bf16 v[160:163], v[236:239], a[208:211], v[160:163]\n\t"
  "ds_read_b64_tr_b16 v[248:249], v14 offset:49664\n\t"
  "ds_read_b64_tr_b16 v[250:251], v14 offset:51712\n\t"
  "v_mfma_f32_16x16x32_bf16 v[164:167], v[232:235], a[196:199], v[164:167]\n\t"
  "s_add_u32 m0, s44, 115712\n\t"
  "s_add_u32 s41, s45, 524416\n\t"
  "buffer_load_dwordx4 v19, s[28:31], s41 offen lds\n\t"
  "v_mfma_f32_16x16x32_bf16 v[164:167], v[236:239], a[212:215], v[164:167]\n\t"
  "ds_read_b64_tr_b16 v[252:253], v14 offset:53760\n\t"
  "ds_read_b64_tr_b16 v[254:255], v14 offset:55808\n\t"
  "v_mfma_f32_16x16x32_bf16 v[168:171], v[232:235], a[200:203], v[168:171]\n\t"
  "s_add_u32 m0, s44, 116736\n\t"
  "s_add_u32 s41, s45, 589824\n\t"
  "buffer_load_dwordx4 v19, s[28:31], s41 offen lds\n\t"
  "v_mfma_f32_16x16x32_bf16 v[168:171], v[236:239], a[216:219], v[168:171]\n\t"
  "v_mfma_f32_16x16x32_bf16 v[172:175], v[232:235], a[204:207], v[172:175]\n\t"
  "s_add_u32 m0, s44, 117760\n\t"
  "s_add_u32 s41, s45, 589952\n\t"
  "buffer_load_dwordx4 v19, s[28:31], s41 offen lds\n\t"
  "v_mfma_f32_16x16x32_bf16 v[172:175], v[236:239], a[220:223], v[172:175]\n\t"
  "s_waitcnt lgkmcnt(2)\n\t"
  "s_nop 0\n\t"
  "v_mfma_f32_16x16x32_bf16 v[176:179], v[240:243], a[192:195], v[176:179]\n\t"
  "v_mfma_f32_16x16x32_bf16 v[176:179], v[244:247], a[208:211], v[176:179]\n\t"
  "ds_read_b64_tr_b16 v[232:233], v15 offset:49664\n\t"
  "ds_read_b64_tr_b16 v[234:235], v15 offset:51712\n\t"
  "v_mfma_f32_16x16x32_bf16 v[180:183], v[240:243], a[196:199], v[180:183]\n\t"
  "v_mfma_f32_16x16x32_bf16 v[180:183], v[244:247], a[212:215], v[180:183]\n\t"
  "ds_read_b64_tr_b16 v[236:237], v15 offset:53760\n\t"
  "ds_read_b64_tr_b16 v[238:239], v15 offset:55808\n\t"
  "v_mfma_f32_16x16x32_bf16 v[184:187], v[240:243], a[200:203], v[184:187]\n\t"
  "v_mfma_f32_16x16x32_bf16 v[184:187], v[244:247], a[216:219], v[184:187]\n\t"
  "v_mfma_f32_16x16x32_bf16 v[188:191], v[240:243], a[204:207], v[188:191]\n\t"
  "v_mfma_f32_16x16x32_bf16 v[188:191], v[244:247], a[220:223], v[188:191]\n\t"
  "s_waitcnt vmcnt(8)\n\t"
  "s_barrier\n\t"
  "v_mfma_f32_16x16x32_bf16 v[192:195], v[248:251], a[192:195], v[192:195]\n\t"
  "s_waitcnt lgkmcnt(0)\n\t"
  "s_nop 0\n\t"
  "v_mfma_f32_16x16x32_bf16 v[192:195], v[252:255], a[208:211], v[192:195]\n\t"
  "ds_read_b64_tr_b16 v[240:241], v14 offset:57344\n\t"
  "ds_read_b64_tr_b16 v[242:243], v14 offset:59392\n\t"
  "v_mfma_f32_16x16x32_bf16 v[196:199], v[248:251], a[196:199], v[196:199]\n\t"
  "v_mfma_f32_16x16x32_bf16 v[196:199], v[252:255], a[212:215], v[196:199]\n\t"
  "ds_read_b64_tr_b16 v[244:245], v14 offset:61440\n\t"
  "ds_read_b64_tr_b16 v[246:247], v14 offset:63488\n\t"
  "v_mfma_f32_16x16x32_bf16 v[200:203], v[248:251], a[200:203], v[200:203]\n\t"
  "v_mfma_f32_16x16x32_bf16 v[200:203], v[252:255], a[216:219], v[200:203]\n\t"
  "v_mfma_f32_16x16x32_bf16 v[204:207], v[248:251], a[204:207], v[204:207]\n\t"
  "v_mfma_f32_16x16x32_bf16 v[204:207], v[252:255], a[220:223], v[204:207]\n\t"
  "v_mfma_f32_16x16x32_bf16 v[208:211], v[232:235], a[192:195], v[208:211]\n\t"
  "v_mfma_f32_16x16x32_bf16 v[208:211], v[236:239], a[208:211], v[208:211]\n\t"
  "ds_read_b64_tr_b16 v[248:249], v15 offset:57344\n\t"
  "ds_read_b64_tr_b16 v[250:251], v15 offset:59392\n\t"
  "v_mfma_f32_16x16x32_bf16 v[212:215], v[232:235], a[196:199], v[212:215]\n\t"
  "v_mfma_f32_16x16x32_bf16 v[212:215], v[236:239], a[212:215], v[212:215]\n\t"
  "ds_read_b64_tr_b16 v[252:253], v15 offset:61440\n\t"
  "ds_read_b64_tr_b16 v[254:255], v15 offset:63488\n\t"
  "v_mfma_f32_16x16x32_bf16 v[216:219], v[232:235], a[200:203], v[216:219]\n\t"
  "v_mfma_f32_16x16x32_bf16 v[216:219], v[236:239], a[216:219], v[216:219]\n\t"
  "v_mfma_f32_16x16x32_bf16 v[220:223], v[232:235], a[204:207], v[220:223]\n\t"
  "v_mfma_f32_16x16x32_bf16 v[220:223], v[236:239], a[220:223], v[220:223]\n\t"
  "s_waitcnt lgkmcnt(2)\n\t"
  "s_nop 0\n\t"
  "v_mfma_f32_16x16x32_bf16 v[160:163], v[240:243], a[224:227], v[160:163]\n\t"
  "v_mfma_f32_16x16x32_bf16 v[160:163], v[244:247], a[240:243], v[160:163]\n\t"
  "ds_read_b64_tr_b16 v[232:233], v14 offset:57856\n\t"
  "ds_read_b64_tr_b16 v[234:235], v14 offset:59904\n\t"
  "v_mfma_f32_16x16x32_bf16 v[164:167], v[240:243], a[228:231], v[164:167]\n\t"
  "v_mfma_f32_16x16x32_bf16 v[164:167], v[244:247], a[244:247], v[164:167]\n\t"
  "ds_read_b64_tr_b16 v[236:237], v14 offset:61952\n\t"
  "ds_read_b64_tr_b16 v[238:239], v14 offset:64000\n\t"
  "v_mfma_f32_16x16x32_bf16 v[168:171], v[240:243], a[232:235], v[168:171]\n\t"
  "v_mfma_f32_16x16x32_bf16 v[168:171], v[244:247], a[248:251], v[168:171]\n\t"
  "v_mfma_f32_16x16x32_bf16 v[172:175], v[240:243], a[236:239], v[172:175]\n\t"
  "v_mfma_f32_16x16x32_bf16 v[172:175], v[244:247], a[252:255], v[172:175]\n\t"
  "v_mfma_f32_16x16x32_bf16 v[176:179], v[248:251], a[224:227], v[176:179]\n\t"
  "s_waitcnt lgkmcnt(0)\n\t"
  "s_nop 0\n\t"
  "v_mfma_f32_16x16x32_bf16 v[176:179], v[252:255], a[240:243], v[176:179]\n\t"
  "ds_read_b64_tr_b16 v[240:241], v15 offset:57856\n\t"
  "ds_read_b64_tr_b16 v[242:243], v15 offset:59904\n\t"
  "v_mfma_f32_16x16x32_bf16 v[180:183], v[248:251], a[228:231], v[180:183]\n\t"
  "v_mfma_f32_16x16x32_bf16 v[180:183], v[252:255], a[244:247], v[180:183]\n\t"
  "ds_read_b64_tr_b16 v[244:245], v15 offset:61952\n\t"
  "ds_read_b64_tr_b16 v[246:247], v15 offset:64000\n\t"
  "v_mfma_f32_16x16x32_bf16 v[184:187], v[248:251], a[232:235], v[184:187]\n\t"
  "v_mfma_f32_16x16x32_bf16 v[184:187], v[252:255], a[248:251], v[184:187]\n\t"
  "v_mfma_f32_16x16x32_bf16 v[188:191], v[248:251], a[236:239], v[188:191]\n\t"
  "v_mfma_f32_16x16x32_bf16 v[188:191], v[252:255], a[252:255], v[188:191]\n\t"
  "s_waitcnt vmcnt(4)\n\t"
  "s_barrier\n\t"
  "v_mfma_f32_16x16x32_bf16 v[192:195], v[232:235], a[224:227], v[192:195]\n\t"
  "s_add_u32 m0, s44, 131072\n\t"
  "s_add_u32 s41, s45, 1048576\n\t"
  "buffer_load_dwordx4 v19, s[28:31], s41 offen lds\n\t"
  "v_mfma_f32_16x16x32_bf16 v[192:195], v[236:239], a[240:243], v[192:195]\n\t"
  "ds_read_b64_tr_b16 v[248:249], v16 offset:32768\n\t"
  "ds_read_b64_tr_b16 v[250:251], v16 offset:36864\n\t"
  "v_mfma_f32_16x16x32_bf16 v[196:199], v[232:235], a[228:231], v[196:199]\n\t"
  "s_add_u32 m0, s44, 132096\n\t"
  "s_add_u32 s41, s45, 1048704\n\t"
  "buffer_load_dwordx4 v19, s[28:31], s41 offen lds\n\t"
  "v_mfma_f32_16x16x32_bf16 v[196:199], v[236:239], a[244:247], v[196:199]\n\t"
  "ds_read_b64_tr_b16 v[252:253], v16 offset:40960\n\t"
  "ds_read_b64_tr_b16 v[254:255], v16 offset:45056\n\t"
  "v_mfma_f32_16x16x32_bf16 v[200:203], v[232:235], a[232:235], v[200:203]\n\t"
  "s_add_u32 m0, s44, 133120\n\t"
  "s_add_u32 s41, s45, 1114112\n\t"
  "buffer_load_dwordx4 v19, s[28:31], s41 offen lds\n\t"
  "v_mfma_f32_16x16x32_bf16 v[200:203], v[236:239], a[248:251], v[200:203]\n\t"
  "v_mfma_f32_16x16x32_bf16 v[204:207], v[232:235], a[236:239], v[204:207]\n\t"
  "s_add_u32 m0, s44, 134144\n\t"
  "s_add_u32 s41, s45, 1114240\n\t"
  "buffer_load_dwordx4 v19, s[28:31], s41 offen lds\n\t"
  "v_mfma_f32_16x16x32_bf16 v[204:207], v[236:239], a[252:255], v[204:207]\n\t"
  "s_waitcnt lgkmcnt(2)\n\t"
  "s_nop 0\n\t"
  "v_mfma_f32_16x16x32_bf16 v[208:211], v[240:243], a[224:227], v[208:211]\n\t"
  "v_mfma_f32_16x16x32_bf16 v[208:211], v[244:247], a[240:243], v[208:211]\n\t"
  "ds_read_b64_tr_b16 v[232:233], v16 offset:32776\n\t"
  "ds_read_b64_tr_b16 v[234:235], v16 offset:36872\n\t"
  "v_mfma_f32_16x16x32_bf16 v[212:215], v[240:243], a[228:231], v[212:215]\n\t"
  "v_mfma_f32_16x16x32_bf16 v[212:215], v[244:247], a[244:247], v[212:215]\n\t"
  "ds_read_b64_tr_b16 v[236:237], v16 offset:40968\n\t"
  "ds_read_b64_tr_b16 v[238:239], v16 offset:45064\n\t"
  "v_mfma_f32_16x16x32_bf16 v[216:219], v[240:243], a[232:235], v[216:219]\n\t"
  "v_mfma_f32_16x16x32_bf16 v[216:219], v[244:247], a[248:251], v[216:219]\n\t"
  "v_mfma_f32_16x16x32_bf16 v[220:223], v[240:243], a[236:239], v[220:223]\n\t"
  "v_mfma_f32_16x16x32_bf16 v[220:223], v[244:247], a[252:255], v[220:223]\n\t"
  "v_mfma_f32_16x16x32_bf16 v[32:35], v[248:251], a[0:3], 0\n\t"
  "s_waitcnt lgkmcnt(0)\n\t"
  "s_add_u32 m0, s44, 147456\n\t"
  "s_add_u32 s41, s45, 1572864\n\t"
  "s_nop 0\n\t"
  "buffer_load_dwordx4 v19, s[28:31], s41 offen lds\n\t"
  "v_fma_f32 v160, v20, v160, v224\n\t"
  "v_mfma_f32_16x16x32_bf16 v[32:35], v[252:255], a[16:19], v[32:35]\n\t"
  "ds_read_b64_tr_b16 v[240:241], v16 offset:33280\n\t"
  "ds_read_b64_tr_b16 v[242:243], v16 offset:37376\n\t"
  "v_mfma_f32_16x16x32_bf16 v[36:39], v[248:251], a[4:7], 0\n\t"
  "s_add_u32 m0, s44, 148480\n\t"
  "s_add_u32 s41, s45, 1572992\n\t"
  "buffer_load_dwordx4 v19, s[28:31], s41 offen lds\n\t"
  "v_fma_f32 v162, v20, v162, v226\n\t"
  "v_mfma_f32_16x16x32_bf16 v[36:39], v[252:255], a[20:23], v[36:39]\n\t"
  "ds_read_b64_tr_b16 v[244:245], v16 offset:41472\n\t"
  "ds_read_b64_tr_b16 v[246:247], v16 offset:45568\n\t"
  "v_mfma_f32_16x16x32_bf16 v[40:43], v[248:251], a[8:11], 0\n\t"
  "s_add_u32 m0, s44, 149504\n\t"
  "s_add_u32 s41, s45, 1638400\n\t"
  "buffer_load_dwordx4 v19, s[28:31], s41 offen lds\n\t"
  "v_fma_f32 v161, v20, v161, v225\n\t"
  "v_mfma_f32_16x16x32_bf16 v[40:43], v[252:255], a[24:27], v[40:43]\n\t"
  "v_fma_f32 v163, v20, v163, v227\n\t"
  "v_mul_f32_e64 v28, v160, v160\n\t"
  "v_mfma_f32_16x16x32_bf16 v[44:47], v[248:251], a[12:15], 0\n\t"
  "s_add_u32 m0, s44, 150528\n\t"
  "s_add_u32 s41, s45, 1638528\n\t"
  "buffer_load_dwordx4 v19, s[28:31], s41 offen lds\n\t"
  "v_mul_f32_e64 v14, v162, v162\n\t"
  "v_mfma_f32_16x16x32_bf16 v[44:47], v[252:255], a[28:31], v[44:47]\n\t"
  "v_mul_f32_e32 v29, v161, v161\n\t"
  "v_mul_f32_e32 v15, v163, v163\n\t"
  "v_mfma_f32_16x16x32_bf16 v[48:51], v[232:235], a[0:3], 0\n\t"
  "v_fma_f32 v28, v28, s49, v31\n\t"
  "v_fma_f32 v14, v14, s49, v31\n\t"
  "v_mfma_f32_16x16x32_bf16 v[48:51], v[236:239], a[16:19], v[48:51]\n\t"
  "ds_read_b64_tr_b16 v[248:249], v16 offset:33288\n\t"
  "ds_read_b64_tr_b16 v[250:251], v16 offset:37384\n\t"
  "v_mfma_f32_16x16x32_bf16 v[52:55], v[232:235], a[4:7], 0\n\t"
  "v_fma_f32 v29, v29, s49, v31\n\t"
  "v_fma_f32 v15, v15, s49, v31\n\t"
  "v_mfma_f32_16x16x32_bf16 v[52:55], v[236:239], a[20:23], v[52:55]\n\t"
  "ds_read_b64_tr_b16 v[252:253], v16 offset:41480\n\t"
  "ds_read_b64_tr_b16 v[254:255], v16 offset:45576\n\t"
  "v_mfma_f32_16x16x32_bf16 v[56:59], v[232:235], a[8:11], 0\n\t"
  "v_mul_f32_e32 v28, v160, v28\n\t"
  "v_mul_f32_e32 v14, v162, v14\n\t"
  "v_mfma_f32_16x16x32_bf16 v[56:59], v[236:239], a[24:27], v[56:59]\n\t"
  "v_mul_f32_e32 v29, v161, v29\n\t"
  "v_mul_f32_e32 v15, v163, v15\n\t"
  "v_mfma_f32_16x16x32_bf16 v[60:63], v[232:235], a[12:15], 0\n\t"
  "v_exp_f32_e64 v28, v28\n\t"
  "v_mfma_f32_16x16x32_bf16 v[60:63], v[236:239], a[28:31], v[60:63]\n\t"
  "s_waitcnt lgkmcnt(2)\n\t"
  "v_exp_f32_e32 v14, v14\n\t"
  "v_mfma_f32_16x16x32_bf16 v[64:67], v[240:243], a[0:3], 0\n\t"
  "s_add_u32 m0, s44, 65536\n\t"
  "s_add_u32 s41, s45, 2097152\n\t"
  "buffer_load_dwordx4 v19, s[28:31], s41 offen lds\n\t"
  "v_mfma_f32_16x16x32_bf16 v[64:67], v[244:247], a[16:19], v[64:67]\n\t"
  "ds_read_b64_tr_b16 v[232:233], v16 offset:33792\n\t"
  "ds_read_b64_tr_b16 v[234:235], v16 offset:37888\n\t"
  "v_mfma_f32_16x16x32_bf16 v[68:71], v[240:243], a[4:7], 0\n\t"
  "s_add_u32 m0, s44, 66560\n\t"
  "s_add_u32 s41, s45, 2097280\n\t"
  "buffer_load_dwordx4 v19, s[28:31], s41 offen lds\n\t"
  "v_mfma_f32_16x16x32_bf16 v[68:71], v[244:247], a[20:23], v[68:71]\n\t"
  "ds_read_b64_tr_b16 v[236:237], v16 offset:41984\n\t"
  "ds_read_b64_tr_b16 v[238:239], v16 offset:46080\n\t"
  "v_mfma_f32_16x16x32_bf16 v[72:75], v[240:243], a[8:11], 0\n\t"
  "s_add_u32 m0, s44, 67584\n\t"
  "s_add_u32 s41, s45, 2162688\n\t"
  "buffer_load_dwordx4 v19, s[28:31], s41 offen lds\n\t"
  "v_mfma_f32_16x16x32_bf16 v[72:75], v[244:247], a[24:27], v[72:75]\n\t"
  "v_exp_f32_e64 v29, v29\n\t"
  "v_mfma_f32_16x16x32_bf16 v[76:79], v[240:243], a[12:15], 0\n\t"
  "s_add_u32 m0, s44, 68608\n\t"
  "s_add_u32 s41, s45, 2162816\n\t"
  "buffer_load_dwordx4 v19, s[28:31], s41 offen lds\n\t"
  "v_add_f32_e64 v28, 1.0, v28\n\t"
  "v_mfma_f32_16x16x32_bf16 v[76:79], v[244:247], a[28:31], v[76:79]\n\t"
  "s_waitcnt lgkmcnt(2)\n\t"
  "v_exp_f32_e32 v15, v15\n\t"
  "v_mfma_f32_16x16x32_bf16 v[80:83], v[248:251], a[0:3], 0\n\t"
  "v_add_f32_e32 v14, 1.0, v14\n\t"
  "v_add_f32_e32 v29, 1.0, v29\n\t"
  "v_mfma_f32_16x16x32_bf16 v[80:83], v[252:255], a[16:19], v[80:83]\n\t"
  "ds_read_b64_tr_b16 v[240:241], v16 offset:33800\n\t"
  "ds_read_b64_tr_b16 v[242:243], v16 offset:37896\n\t"
  "v_mfma_f32_16x16x32_bf16 v[84:87], v[248:251], a[4:7], 0\n\t"
  "v_add_f32_e64 v15, 1.0, v15\n\t"
  "v_mfma_f32_16x16x32_bf16 v[84:87], v[252:255], a[20:23], v[84:87]\n\t"
  "ds_read_b64_tr_b16 v[244:245], v16 offset:41992\n\t"
  "ds_read_b64_tr_b16 v[246:247], v16 offset:46088\n\t"
  "v_mfma_f32_16x16x32_bf16 v[88:91], v[248:251], a[8:11], 0\n\t"
  "v_rcp_f32_e64 v28, v28\n\t"
  "v_mfma_f32_16x16x32_bf16 v[88:91], v[252:255], a[24:27], v[88:91]\n\t"
  "v_rcp_f32_e64 v14, v14\n\t"
  "v_mfma_f32_16x16x32_bf16 v[92:95], v[248:251], a[12:15], 0\n\t"
  "v_rcp_f32_e64 v29, v29\n\t"
  "v_mfma_f32_16x16x32_bf16 v[92:95], v[252:255], a[28:31], v[92:95]\n\t"
  "v_rcp_f32_e64 v15, v15\n\t"
  "v_mfma_f32_16x16x32_bf16 v[96:99], v[232:235], a[0:3], 0\n\t"
  "s_waitcnt lgkmcnt(0)\n\t"
  "s_add_u32 m0, s44, 81920\n\t"
  "s_add_u32 s41, s45, 2621440\n\t"
  "s_nop 0\n\t"
  "buffer_load_dwordx4 v19, s[28:31], s41 offen lds\n\t"
  "v_mul_f32_e64 v160, v160, v28\n\t"
  "v_mfma_f32_16x16x32_bf16 v[96:99], v[236:239], a[16:19], v[96:99]\n\t"
  "ds_read_b64_tr_b16 v[248:249], v16 offset:34304\n\t"
  "ds_read_b64_tr_b16 v[250:251], v16 offset:38400\n\t"
  "v_mfma_f32_16x16x32_bf16 v[100:103], v[232:235], a[4:7], 0\n\t"
  "s_add_u32 m0, s44, 82944\n\t"
  "s_add_u32 s41, s45, 2621568\n\t"
  "buffer_load_dwordx4 v19, s[28:31], s41 offen lds\n\t"
  "v_mul_f32_e64 v162, v162, v14\n\t"
  "v_mfma_f32_16x16x32_bf16 v[100:103], v[236:239], a[20:23], v[100:103]\n\t"
  "ds_read_b64_tr_b16 v[252:253], v16 offset:42496\n\t"
  "ds_read_b64_tr_b16 v[254:255], v16 offset:46592\n\t"
  "v_mfma_f32_16x16x32_bf16 v[104:107], v[232:235], a[8:11], 0\n\t"
  "s_add_u32 m0, s44, 83968\n\t"
  "s_add_u32 s41, s45, 2686976\n\t"
  "buffer_load_dwordx4 v19, s[28:31], s41 offen lds\n\t"
  "v_mul_f32_e64 v161, v161, v29\n\t"
  "v_mfma_f32_16x16x32_bf16 v[104:107], v[236:239], a[24:27], v[104:107]\n\t"
  "v_mul_f32_e64 v163, v163, v15\n\t"
  "v_cvt_pk_bf16_f32 v160, v160, v161\n\t"
  "v_mfma_f32_16x16x32_bf16 v[108:111], v[232:235], a[12:15], 0\n\t"
  "s_add_u32 m0, s44, 84992\n\t"
  "s_add_u32 s41, s45, 2687104\n\t"
  "buffer_load_dwordx4 v19, s[28:31], s41 offen lds\n\t"
  "v_cvt_pk_bf16_f32 v161, v162, v163\n\t"
  "v_mfma_f32_16x16x32_bf16 v[108:111], v[236:239], a[28:31], v[108:111]\n\t"
  "v_fma_f32 v164, v21, v164, v224\n\t"
  "v_fma_f32 v166, v21, v166, v226\n\t"
  "v_mfma_f32_16x16x32_bf16 v[112:115], v[240:243], a[0:3], 0\n\t"
  "v_fma_f32 v165, v21, v165, v225\n\t"
  "v_fma_f32 v167, v21, v167, v227\n\t"
  "v_mfma_f32_16x16x32_bf16 v[112:115], v[244:247], a[16:19], v[112:115]\n\t"
  "ds_read_b64_tr_b16 v[232:233], v16 offset:34312\n\t"
  "ds_read_b64_tr_b16 v[234:235], v16 offset:38408\n\t"
  "v_mfma_f32_16x16x32_bf16 v[116:119], v[240:243], a[4:7], 0\n\t"
  "v_mul_f32_e32 v28, v164, v164\n\t"
  "v_mul_f32_e32 v14, v166, v166\n\t"
  "v_mfma_f32_16x16x32_bf16 v[116:119], v[244:247], a[20:23], v[116:119]\n\t"
  "ds_read_b64_tr_b16 v[236:237], v16 offset:42504\n\t"
  "ds_read_b64_tr_b16 v[238:239], v16 offset:46600\n\t"
  "v_mfma_f32_16x16x32_bf16 v[120:123], v[240:243], a[8:11], 0\n\t"
  "v_mul_f32_e32 v29, v165, v165\n\t"
  "v_mul_f32_e32 v15, v167, v167\n\t"
  "v_mfma_f32_16x16x32_bf16 v[120:123], v[244:247], a[24:27], v[120:123]\n\t"
  "v_fma_f32 v28, v28, s49, v31\n\t"
  "v_fma_f32 v14, v14, s49, v31\n\t"
  "v_mfma_f32_16x16x32_bf16 v[124:127], v[240:243], a[12:15], 0\n\t"
  "v_fma_f32 v29, v29, s49, v31\n\t"
  "v_fma_f32 v15, v15, s49, v31\n\t"
  "v_mfma_f32_16x16x32_bf16 v[124:127], v[244:247], a[28:31], v[124:127]\n\t"
  "v_mul_f32_e32 v28, v164, v28\n\t"
  "v_mul_f32_e64 v14, v166, v14\n\t"
  "s_waitcnt vmcnt(16)\n\t"
  "s_barrier\n\t"
  "s_waitcnt lgkmcnt(2)\n\t"
  "v_mfma_f32_16x16x32_bf16 v[128:131], v[248:251], a[0:3], 0\n\t"
  "s_add_u32 m0, s18, 0\n\t"
  "s_nop 0\n\t"
  "buffer_load_dwordx4 v8, s[8:11], s16 offen offset:0 lds\n\t"
  "v_mul_f32_e64 v29, v165, v29\n\t"
  "v_mfma_f32_16x16x32_bf16 v[128:131], v[252:255], a[16:19], v[128:131]\n\t"
  "ds_read_b64_tr_b16 v[240:241], v16 offset:49152\n\t"
  "ds_read_b64_tr_b16 v[242:243], v16 offset:53248\n\t"
  "v_mfma_f32_16x16x32_bf16 v[132:135], v[248:251], a[4:7], 0\n\t"
  "buffer_load_dwordx4 v9, s[8:11], s16 offen offset:1024 lds\n\t"
  "v_mul_f32_e64 v15, v167, v15\n\t"
  "v_mfma_f32_16x16x32_bf16 v[132:135], v[252:255], a[20:23], v[132:135]\n\t"
  "ds_read_b64_tr_b16 v[244:245], v16 offset:57344\n\t"
  "ds_read_b64_tr_b16 v[246:247], v16 offset:61440\n\t"
  "v_mfma_f32_16x16x32_bf16 v[136:139], v[248:251], a[8:11], 0\n\t"
  "buffer_load_dwordx4 v10, s[8:11], s16 offen offset:2048 lds\n\t"
  "v_mfma_f32_16x16x32_bf16 v[136:139], v[252:255], a[24:27], v[136:139]\n\t"
  "v_exp_f32_e64 v28, v28\n\t"
  "v_mfma_f32_16x16x32_bf16 v[140:143], v[248:251], a[12:15], 0\n\t"
  "buffer_load_dwordx4 v11, s[8:11], s16 offen offset:3072 lds\n\t"
  "v_mfma_f32_16x16x32_bf16 v[140:143], v[252:255], a[28:31], v[140:143]\n\t"
  "s_waitcnt lgkmcnt(2)\n\t"
  "v_exp_f32_e32 v14, v14\n\t"
  "v_mfma_f32_16x16x32_bf16 v[144:147], v[232:235], a[0:3], 0\n\t"
  "s_add_u32 s16, s16, 0x4000\n\t"
  "s_add_u32 m0, s18, 16384\n\t"
  "s_nop 0\n\t"
  "s_nop 0\n\t"
  "buffer_load_dwordx4 v8, s[8:11], s16 offen offset:0 lds\n\t"
  "v_mfma_f32_16x16x32_bf16 v[144:147], v[236:239], a[16:19], v[144:147]\n\t"
  "ds_read_b64_tr_b16 v[248:249], v16 offset:49160\n\t"
  "ds_read_b64_tr_b16 v[250:251], v16 offset:53256\n\t"
  "v_mfma_f32_16x16x32_bf16 v[148:151], v[232:235], a[4:7], 0\n\t"
  "buffer_load_dwordx4 v9, s[8:11], s16 offen offset:1024 lds\n\t"
  "v_mfma_f32_16x16x32_bf16 v[148:151], v[236:239], a[20:23], v[148:151]\n\t"
  "ds_read_b64_tr_b16 v[252:253], v16 offset:57352\n\t"
  "ds_read_b64_tr_b16 v[254:255], v16 offset:61448\n\t"
  "v_mfma_f32_16x16x32_bf16 v[152:155], v[232:235], a[8:11], 0\n\t"
  "buffer_load_dwordx4 v10, s[8:11], s16 offen offset:2048 lds\n\t"
  "v_mfma_f32_16x16x32_bf16 v[152:155], v[236:239], a[24:27], v[152:155]\n\t"
  "v_exp_f32_e64 v29, v29\n\t"
  "v_mfma_f32_16x16x32_bf16 v[156:159], v[232:235], a[12:15], 0\n\t"
  "buffer_load_dwordx4 v11, s[8:11], s16 offen offset:3072 lds\n\t"
  "v_add_f32_e64 v28, 1.0, v28\n\t"
  "v_mfma_f32_16x16x32_bf16 v[156:159], v[236:239], a[28:31], v[156:159]\n\t"
  "s_waitcnt lgkmcnt(2)\n\t"
  "v_exp_f32_e32 v15, v15\n\t"
  "v_mfma_f32_16x16x32_bf16 v[32:35], v[240:243], a[32:35], v[32:35]\n\t"
  "s_add_u32 s16, s16, 0x4000\n\t"
  "s_add_u32 m0, s18, 32768\n\t"
  "s_nop 0\n\t"
  "s_nop 0\n\t"
  "buffer_load_dwordx4 v8, s[8:11], s16 offen offset:0 lds\n\t"
  "v_add_f32_e64 v14, 1.0, v14\n\t"
  "v_mfma_f32_16x16x32_bf16 v[32:35], v[244:247], a[48:51], v[32:35]\n\t"
  "ds_read_b64_tr_b16 v[232:233], v16 offset:49664\n\t"
  "ds_read_b64_tr_b16 v[234:235], v16 offset:53760\n\t"
  "v_mfma_f32_16x16x32_bf16 v[36:39], v[240:243], a[36:39], v[36:39]\n\t"
  "buffer_load_dwordx4 v9, s[8:11], s16 offen offset:1024 lds\n\t"
  "v_add_f32_e64 v29, 1.0, v29\n\t"
  "v_mfma_f32_16x16x32_bf16 v[36:39], v[244:247], a[52:55], v[36:39]\n\t"
  "ds_read_b64_tr_b16 v[236:237], v16 offset:57856\n\t"
  "ds_read_b64_tr_b16 v[238:239], v16 offset:61952\n\t"
  "v_mfma_f32_16x16x32_bf16 v[40:43], v[240:243], a[40:43], v[40:43]\n\t"
  "buffer_load_dwordx4 v10, s[8:11], s16 offen offset:2048 lds\n\t"
  "v_add_f32_e64 v15, 1.0, v15\n\t"
  "v_mfma_f32_16x16x32_bf16 v[40:43], v[244:247], a[56:59], v[40:43]\n\t"
  "v_rcp_f32_e64 v28, v28\n\t"
  "v_mfma_f32_16x16x32_bf16 v[44:47], v[240:243], a[44:47], v[44:47]\n\t"
  "buffer_load_dwordx4 v11, s[8:11], s16 offen offset:3072 lds\n\t"
  "s_add_u32 s16, s16, 0x4000\n\t"
  "v_mfma_f32_16x16x32_bf16 v[44:47], v[244:247], a[60:63], v[44:47]\n\t"
  "v_rcp_f32_e64 v14, v14\n\t"
  "v_mfma_f32_16x16x32_bf16 v[48:51], v[248:251], a[32:35], v[48:51]\n\t"
  "s_waitcnt lgkmcnt(0)\n\t"
  "v_rcp_f32_e32 v29, v29\n\t"
  "v_mfma_f32_16x16x32_bf16 v[48:51], v[252:255], a[48:51], v[48:51]\n\t"
  "ds_read_b64_tr_b16 v[240:241], v16 offset:49672\n\t"
  "ds_read_b64_tr_b16 v[242:243], v16 offset:53768\n\t"
  "v_mfma_f32_16x16x32_bf16 v[52:55], v[248:251], a[36:39], v[52:55]\n\t"
  "v_rcp_f32_e64 v15, v15\n\t"
  "v_mfma_f32_16x16x32_bf16 v[52:55], v[252:255], a[52:55], v[52:55]\n\t"
  "ds_read_b64_tr_b16 v[244:245], v16 offset:57864\n\t"
  "ds_read_b64_tr_b16 v[246:247], v16 offset:61960\n\t"
  "v_mfma_f32_16x16x32_bf16 v[56:59], v[248:251], a[40:43], v[56:59]\n\t"
  "v_mul_f32_e32 v164, v164, v28\n\t"
  "v_mul_f32_e32 v166, v166, v14\n\t"
  "v_mfma_f32_16x16x32_bf16 v[56:59], v[252:255], a[56:59], v[56:59]\n\t"
  "v_mul_f32_e32 v165, v165, v29\n\t"
  "v_mul_f32_e32 v167, v167, v15\n\t"
  "v_mfma_f32_16x16x32_bf16 v[60:63], v[248:251], a[44:47], v[60:63]\n\t"
  "v_cvt_pk_bf16_f32 v164, v164, v165\n\t"
  "v_cvt_pk_bf16_f32 v165, v166, v167\n\t"
  "v_mfma_f32_16x16x32_bf16 v[60:63], v[252:255], a[60:63], v[60:63]\n\t"
  "v_fma_f32 v168, v22, v168, v224\n\t"
  "v_fma_f32 v170, v22, v170, v226\n\t"
  "v_mfma_f32_16x16x32_bf16 v[64:67], v[232:235], a[32:35], v[64:67]\n\t"
  "v_fma_f32 v169, v22, v169, v225\n\t"
  "v_fma_f32 v171, v22, v171, v227\n\t"
  "v_mfma_f32_16x16x32_bf16 v[64:67], v[236:239], a[48:51], v[64:67]\n\t"
  "ds_read_b64_tr_b16 v[248:249], v16 offset:50176\n\t"
  "ds_read_b64_tr_b16 v[250:251], v16 offset:54272\n\t"
  "v_mfma_f32_16x16x32_bf16 v[68:71], v[232:235], a[36:39], v[68:71]\n\t"
  "v_mul_f32_e32 v28, v168, v168\n\t"
  "v_mul_f32_e32 v14, v170, v170\n\t"
  "v_mfma_f32_16x16x32_bf16 v[68:71], v[236:239], a[52:55], v[68:71]\n\t"
  "ds_read_b64_tr_b16 v[252:253], v16 offset:58368\n\t"
  "ds_read_b64_tr_b16 v[254:255], v16 offset:62464\n\t"
  "v_mfma_f32_16x16x32_bf16 v[72:75], v[232:235], a[40:43], v[72:75]\n\t"
  "v_mul_f32_e32 v29, v169, v169\n\t"
  "v_mul_f32_e32 v15, v171, v171\n\t"
  "v_mfma_f32_16x16x32_bf16 v[72:75], v[236:239], a[56:59], v[72:75]\n\t"
  "v_fma_f32 v28, v28, s49, v31\n\t"
  "v_fma_f32 v14, v14, s49, v31\n\t"
  "v_mfma_f32_16x16x32_bf16 v[76:79], v[232:235], a[44:47], v[76:79]\n\t"
  "v_fma_f32 v29, v29, s49, v31\n\t"
  "v_fma_f32 v15, v15, s49, v31\n\t"
  "v_mfma_f32_16x16x32_bf16 v[76:79], v[236:239], a[60:63], v[76:79]\n\t"
  "s_waitcnt lgkmcnt(2)\n\t"
  "v_mul_f32_e32 v28, v168, v28\n\t"
  "v_mul_f32_e64 v14, v170, v14\n\t"
  "v_mfma_f32_16x16x32_bf16 v[80:83], v[240:243], a[32:35], v[80:83]\n\t"
  "v_mul_f32_e32 v29, v169, v29\n\t"
  "v_mul_f32_e32 v15, v171, v15\n\t"
  "v_mfma_f32_16x16x32_bf16 v[80:83], v[244:247], a[48:51], v[80:83]\n\t"
  "ds_read_b64_tr_b16 v[232:233], v16 offset:50184\n\t"
  "ds_read_b64_tr_b16 v[234:235], v16 offset:54280\n\t"
  "v_mfma_f32_16x16x32_bf16 v[84:87], v[240:243], a[36:39], v[84:87]\n\t"
  "v_exp_f32_e64 v28, v28\n\t"
  "v_mfma_f32_16x16x32_bf16 v[84:87], v[244:247], a[52:55], v[84:87]\n\t"
  "ds_read_b64_tr_b16 v[236:237], v16 offset:58376\n\t"
  "ds_read_b64_tr_b16 v[238:239], v16 offset:62472\n\t"
  "v_mfma_f32_16x16x32_bf16 v[88:91], v[240:243], a[40:43], v[88:91]\n\t"
  "v_exp_f32_e64 v14, v14\n\t"
  "v_mfma_f32_16x16x32_bf16 v[88:91], v[244:247], a[56:59], v[88:91]\n\t"
  "v_exp_f32_e64 v29, v29\n\t"
  "v_mfma_f32_16x16x32_bf16 v[92:95], v[240:243], a[44:47], v[92:95]\n\t"
  "v_exp_f32_e64 v15, v15\n\t"
  "v_mfma_f32_16x16x32_bf16 v[92:95], v[244:247], a[60:63], v[92:95]\n\t"
  "v_add_f32_e32 v28, 1.0, v28\n\t"
  "v_add_f32_e32 v14, 1.0, v14\n\t"
  "v_mfma_f32_16x16x32_bf16 v[96:99], v[248:251], a[32:35], v[96:99]\n\t"
  "s_waitcnt lgkmcnt(0)\n\t"
  "v_add_f32_e32 v29, 1.0, v29\n\t"
  "v_add_f32_e64 v15, 1.0, v15\n\t"
  "v_mfma_f32_16x16x32_bf16 v[96:99], v[252:255], a[48:51], v[96:99]\n\t"
  "ds_read_b64_tr_b16 v[240:241], v16 offset:50688\n\t"
  "ds_read_b64_tr_b16 v[242:243], v16 offset:54784\n\t"
  "v_mfma_f32_16x16x32_bf16 v[100:103], v[248:251], a[36:39], v[100:103]\n\t"
  "v_rcp_f32_e64 v28, v28\n\t"
  "v_mfma_f32_16x16x32_bf16 v[100:103], v[252:255], a[52:55], v[100:103]\n\t"
  "ds_read_b64_tr_b16 v[244:245], v16 offset:58880\n\t"
  "ds_read_b64_tr_b16 v[246:247], v16 offset:62976\n\t"
  "v_mfma_f32_16x16x32_bf16 v[104:107], v[248:251], a[40:43], v[104:107]\n\t"
  "v_rcp_f32_e64 v14, v14\n\t"
  "v_mfma_f32_16x16x32_bf16 v[104:107], v[252:255], a[56:59], v[104:107]\n\t"
  "v_rcp_f32_e64 v29, v29\n\t"
  "v_mfma_f32_16x16x32_bf16 v[108:111], v[248:251], a[44:47], v[108:111]\n\t"
  "v_rcp_f32_e64 v15, v15\n\t"
  "v_mfma_f32_16x16x32_bf16 v[108:111], v[252:255], a[60:63], v[108:111]\n\t"
  "v_mul_f32_e32 v168, v168, v28\n\t"
  "v_mul_f32_e32 v170, v170, v14\n\t"
  "v_mfma_f32_16x16x32_bf16 v[112:115], v[232:235], a[32:35], v[112:115]\n\t"
  "v_mul_f32_e32 v169, v169, v29\n\t"
  "v_mul_f32_e32 v171, v171, v15\n\t"
  "v_mfma_f32_16x16x32_bf16 v[112:115], v[236:239], a[48:51], v[112:115]\n\t"
  "ds_read_b64_tr_b16 v[248:249], v16 offset:50696\n\t"
  "ds_read_b64_tr_b16 v[250:251], v16 offset:54792\n\t"
  "v_mfma_f32_16x16x32_bf16 v[116:119], v[232:235], a[36:39], v[116:119]\n\t"
  "v_cvt_pk_bf16_f32 v168, v168, v169\n\t"
  "v_cvt_pk_bf16_f32 v169, v170, v171\n\t"
  "v_mfma_f32_16x16x32_bf16 v[116:119], v[236:239], a[52:55], v[116:119]\n\t"
  "ds_read_b64_tr_b16 v[252:253], v16 offset:58888\n\t"
  "ds_read_b64_tr_b16 v[254:255], v16 offset:62984\n\t"
  "v_mfma_f32_16x16x32_bf16 v[120:123], v[232:235], a[40:43], v[120:123]\n\t"
  "v_fma_f32 v172, v23, v172, v224\n\t"
  "v_fma_f32 v174, v23, v174, v226\n\t"
  "v_mfma_f32_16x16x32_bf16 v[120:123], v[236:239], a[56:59], v[120:123]\n\t"
  "v_fma_f32 v173, v23, v173, v225\n\t"
  "v_fma_f32 v175, v23, v175, v227\n\t"
  "v_mfma_f32_16x16x32_bf16 v[124:127], v[232:235], a[44:47], v[124:127]\n\t"
  "global_load_dwordx4 v[224:227], v30, s[52:53] offset:192\n\t"
  "v_mul_f32_e64 v28, v172, v172\n\t"
  "v_mfma_f32_16x16x32_bf16 v[124:127], v[236:239], a[60:63], v[124:127]\n\t"
  "v_mul_f32_e32 v14, v174, v174\n\t"
  "v_mul_f32_e64 v29, v173, v173\n\t"
  "s_waitcnt vmcnt(25)\n\t"
  "s_barrier\n\t"
  "s_waitcnt lgkmcnt(2)\n\t"
  "v_mfma_f32_16x16x32_bf16 v[128:131], v[240:243], a[32:35], v[128:131]\n\t"
  "v_mul_f32_e64 v15, v175, v175\n\t"
  "v_fma_f32 v28, v28, s49, v31\n\t"
  "v_mfma_f32_16x16x32_bf16 v[128:131], v[244:247], a[48:51], v[128:131]\n\t"
  "ds_read_b64_tr_b16 v[232:233], v17 offset:0\n\t"
  "ds_read_b64_tr_b16 v[234:235], v17 offset:4096\n\t"
  "v_mfma_f32_16x16x32_bf16 v[132:135], v[240:243], a[36:39], v[132:135]\n\t"
  "v_fma_f32 v14, v14, s49, v31\n\t"
  "v_fma_f32 v29, v29, s49, v31\n\t"
  "v_mfma_f32_16x16x32_bf16 v[132:135], v[244:247], a[52:55], v[132:135]\n\t"
  "ds_read_b64_tr_b16 v[236:237], v17 offset:8192\n\t"
  "ds_read_b64_tr_b16 v[238:239], v17 offset:12288\n\t"
  "v_mfma_f32_16x16x32_bf16 v[136:139], v[240:243], a[40:43], v[136:139]\n\t"
  "v_fma_f32 v15, v15, s49, v31\n\t"
  "v_mul_f32_e64 v28, v172, v28\n\t"
  "v_mfma_f32_16x16x32_bf16 v[136:139], v[244:247], a[56:59], v[136:139]\n\t"
  "v_mul_f32_e32 v14, v174, v14\n\t"
  "v_mul_f32_e32 v29, v173, v29\n\t"
  "v_mfma_f32_16x16x32_bf16 v[140:143], v[240:243], a[44:47], v[140:143]\n\t"
  "v_mul_f32_e64 v15, v175, v15\n\t"
  "v_mfma_f32_16x16x32_bf16 v[140:143], v[244:247], a[60:63], v[140:143]\n\t"
  "v_exp_f32_e64 v28, v28\n\t"
  "v_mfma_f32_16x16x32_bf16 v[144:147], v[248:251], a[32:35], v[144:147]\n\t"
  "s_waitcnt lgkmcnt(0)\n\t"
  "v_exp_f32_e32 v14, v14\n\t"
  "v_mfma_f32_16x16x32_bf16 v[144:147], v[252:255], a[48:51], v[144:147]\n\t"
  "ds_read_b64_tr_b16 v[240:241], v17 offset:8\n\t"
  "ds_read_b64_tr_b16 v[242:243], v17 offset:4104\n\t"
  "v_mfma_f32_16x16x32_bf16 v[148:151], v[248:251], a[36:39], v[148:151]\n\t"
  "v_exp_f32_e64 v29, v29\n\t"
  "v_mfma_f32_16x16x32_bf16 v[148:151], v[252:255], a[52:55], v[148:151]\n\t"
  "ds_read_b64_tr_b16 v[244:245], v17 offset:8200\n\t"
  "ds_read_b64_tr_b16 v[246:247], v17 offset:12296\n\t"
  "v_mfma_f32_16x16x32_bf16 v[152:155], v[248:251], a[40:43], v[152:155]\n\t"
  "v_exp_f32_e64 v15, v15\n\t"
  "v_mfma_f32_16x16x32_bf16 v[152:155], v[252:255], a[56:59], v[152:155]\n\t"
  "v_add_f32_e32 v28, 1.0, v28\n\t"
  "v_add_f32_e32 v14, 1.0, v14\n\t"
  "v_mfma_f32_16x16x32_bf16 v[156:159], v[248:251], a[44:47], v[156:159]\n\t"
  "v_add_f32_e32 v29, 1.0, v29\n\t"
  "v_add_f32_e32 v15, 1.0, v15\n\t"
  "v_mfma_f32_16x16x32_bf16 v[156:159], v[252:255], a[60:63], v[156:159]\n\t"
  "v_rcp_f32_e64 v28, v28\n\t"
  "v_mfma_f32_16x16x32_bf16 v[32:35], v[232:235], a[64:67], v[32:35]\n\t"
  "v_rcp_f32_e64 v14, v14\n\t"
  "v_mfma_f32_16x16x32_bf16 v[32:35], v[236:239], a[80:83], v[32:35]\n\t"
  "ds_read_b64_tr_b16 v[248:249], v17 offset:512\n\t"
  "ds_read_b64_tr_b16 v[250:251], v17 offset:4608\n\t"
  "v_mfma_f32_16x16x32_bf16 v[36:39], v[232:235], a[68:71], v[36:39]\n\t"
  "v_rcp_f32_e64 v29, v29\n\t"
  "v_mfma_f32_16x16x32_bf16 v[36:39], v[236:239], a[84:87], v[36:39]\n\t"
  "ds_read_b64_tr_b16 v[252:253], v17 offset:8704\n\t"
  "ds_read_b64_tr_b16 v[254:255], v17 offset:12800\n\t"
  "v_mfma_f32_16x16x32_bf16 v[40:43], v[232:235], a[72:75], v[40:43]\n\t"
  "v_rcp_f32_e64 v15, v15\n\t"
  "v_mfma_f32_16x16x32_bf16 v[40:43], v[236:239], a[88:91], v[40:43]\n\t"
  "v_mul_f32_e32 v172, v172, v28\n\t"
  "v_mul_f32_e32 v174, v174, v14\n\t"
  "v_mfma_f32_16x16x32_bf16 v[44:47], v[232:235], a[76:79], v[44:47]\n\t"
  "v_mul_f32_e32 v173, v173, v29\n\t"
  "v_mul_f32_e32 v175, v175, v15\n\t"
  "v_mfma_f32_16x16x32_bf16 v[44:47], v[236:239], a[92:95], v[44:47]\n\t"
  "s_waitcnt lgkmcnt(2)\n\t"
  "s_nop 0\n\t"
  "v_cvt_pk_bf16_f32 v172, v172, v173\n\t"
  "v_cvt_pk_bf16_f32 v173, v174, v175\n\t"
  "v_mfma_f32_16x16x32_bf16 v[48:51], v[240:243], a[64:67], v[48:51]\n\t"
  "v_fma_f32 v176, v20, v176, v228\n\t"
  "v_fma_f32 v178, v20, v178, v230\n\t"
  "v_mfma_f32_16x16x32_bf16 v[48:51], v[244:247], a[80:83], v[48:51]\n\t"
  "ds_read_b64_tr_b16 v[232:233], v17 offset:520\n\t"
  "ds_read_b64_tr_b16 v[234:235], v17 offset:4616\n\t"
  "v_mfma_f32_16x16x32_bf16 v[52:55], v[240:243], a[68:71], v[52:55]\n\t"
  "v_fma_f32 v177, v20, v177, v229\n\t"
  "v_fma_f32 v179, v20, v179, v231\n\t"
  "v_mfma_f32_16x16x32_bf16 v[52:55], v[244:247], a[84:87], v[52:55]\n\t"
  "ds_read_b64_tr_b16 v[236:237], v17 offset:8712\n\t"
  "ds_read_b64_tr_b16 v[238:239], v17 offset:12808\n\t"
  "v_mfma_f32_16x16x32_bf16 v[56:59], v[240:243], a[72:75], v[56:59]\n\t"
  "v_mul_f32_e32 v28, v176, v176\n\t"
  "v_mul_f32_e32 v14, v178, v178\n\t"
  "v_mfma_f32_16x16x32_bf16 v[56:59], v[244:247], a[88:91], v[56:59]\n\t"
  "v_mul_f32_e32 v29, v177, v177\n\t"
  "v_mul_f32_e32 v15, v179, v179\n\t"
  "v_mfma_f32_16x16x32_bf16 v[60:63], v[240:243], a[76:79], v[60:63]\n\t"
  "v_fma_f32 v28, v28, s49, v31\n\t"
  "v_fma_f32 v14, v14, s49, v31\n\t"
  "v_mfma_f32_16x16x32_bf16 v[60:63], v[244:247], a[92:95], v[60:63]\n\t"
  "v_fma_f32 v29, v29, s49, v31\n\t"
  "v_fma_f32 v15, v15, s49, v31\n\t"
  "v_mfma_f32_16x16x32_bf16 v[64:67], v[248:251], a[64:67], v[64:67]\n\t"
  "s_waitcnt lgkmcnt(0)\n\t"
  "s_add_u32 m0, s44, 98304\n\t"
  "s_add_u32 s41, s45, 3145728\n\t"
  "s_nop 0\n\t"
  "buffer_load_dwordx4 v19, s[28:31], s41 offen lds\n\t"
  "v_mul_f32_e64 v28, v176, v28\n\t"
  "v_mfma_f32_16x16x32_bf16 v[64:67], v[252:255], a[80:83], v[64:67]\n\t"
  "ds_read_b64_tr_b16 v[240:241], v17 offset:1024\n\t"
  "ds_read_b64_tr_b16 v[242:243], v17 offset:5120\n\t"
  "v_mfma_f32_16x16x32_bf16 v[68:71], v[248:251], a[68:71], v[68:71]\n\t"
  "s_add_u32 m0, s44, 99328\n\t"
  "s_add_u32 s41, s45, 3145856\n\t"
  "buffer_load_dwordx4 v19, s[28:31], s41 offen lds\n\t"
  "v_mul_f32_e64 v14, v178, v14\n\t"
  "v_mfma_f32_16x16x32_bf16 v[68:71], v[252:255], a[84:87], v[68:71]\n\t"
  "ds_read_b64_tr_b16 v[244:245], v17 offset:9216\n\t"
  "ds_read_b64_tr_b16 v[246:247], v17 offset:13312\n\t"
  "v_mfma_f32_16x16x32_bf16 v[72:75], v[248:251], a[72:75], v[72:75]\n\t"
  "s_add_u32 m0, s44, 100352\n\t"
  "s_add_u32 s41, s45, 3211264\n\t"
  "buffer_load_dwordx4 v19, s[28:31], s41 offen lds\n\t"
  "v_mul_f32_e64 v29, v177, v29\n\t"
  "v_mfma_f32_16x16x32_bf16 v[72:75], v[252:255], a[88:91], v[72:75]\n\t"
  "v_mul_f32_e64 v15, v179, v15\n\t"
  "v_mfma_f32_16x16x32_bf16 v[76:79], v[248:251], a[76:79], v[76:79]\n\t"
  "s_add_u32 m0, s44, 101376\n\t"
  "s_add_u32 s41, s45, 3211392\n\t"
  "buffer_load_dwordx4 v19, s[28:31], s41 offen lds\n\t"
  "v_mfma_f32_16x16x32_bf16 v[76:79], v[252:255], a[92:95], v[76:79]\n\t"
  "v_exp_f32_e64 v28, v28\n\t"
  "v_mfma_f32_16x16x32_bf16 v[80:83], v[232:235], a[64:67], v[80:83]\n\t"
  "v_exp_f32_e64 v14, v14\n\t"
  "v_mfma_f32_16x16x32_bf16 v[80:83], v[236:239], a[80:83], v[80:83]\n\t"
  "ds_read_b64_tr_b16 v[248:249], v17 offset:1032\n\t"
  "ds_read_b64_tr_b16 v[250:251], v17 offset:5128\n\t"
  "v_mfma_f32_16x16x32_bf16 v[84:87], v[232:235], a[68:71], v[84:87]\n\t"
  "v_exp_f32_e64 v29, v29\n\t"
  "v_mfma_f32_16x16x32_bf16 v[84:87], v[236:239], a[84:87], v[84:87]\n\t"
  "ds_read_b64_tr_b16 v[252:253], v17 offset:9224\n\t"
  "ds_read_b64_tr_b16 v[254:255], v17 offset:13320\n\t"
  "v_mfma_f32_16x16x32_bf16 v[88:91], v[232:235], a[72:75], v[88:91]\n\t"
  "v_exp_f32_e64 v15, v15\n\t"
  "v_mfma_f32_16x16x32_bf16 v[88:91], v[236:239], a[88:91], v[88:91]\n\t"
  "v_add_f32_e32 v28, 1.0, v28\n\t"
  "v_add_f32_e32 v14, 1.0, v14\n\t"
  "v_mfma_f32_16x16x32_bf16 v[92:95], v[232:235], a[76:79], v[92:95]\n\t"
  "v_add_f32_e32 v29, 1.0, v29\n\t"
  "v_add_f32_e32 v15, 1.0, v15\n\t"
  "v_mfma_f32_16x16x32_bf16 v[92:95], v[236:239], a[92:95], v[92:95]\n\t"
  "s_waitcnt lgkmcnt(2)\n\t"
  "v_rcp_f32_e32 v28, v28\n\t"
  "v_mfma_f32_16x16x32_bf16 v[96:99], v[240:243], a[64:67], v[96:99]\n\t"
  "v_rcp_f32_e64 v14, v14\n\t"
  "v_mfma_f32_16x16x32_bf16 v[96:99], v[244:247], a[80:83], v[96:99]\n\t"
  "ds_read_b64_tr_b16 v[232:233], v17 offset:1536\n\t"
  "ds_read_b64_tr_b16 v[234:235], v17 offset:5632\n\t"
  "v_mfma_f32_16x16x32_bf16 v[100:103], v[240:243], a[68:71], v[100:103]\n\t"
  "v_rcp_f32_e64 v29, v29\n\t"
  "v_mfma_f32_16x16x32_bf16 v[100:103], v[244:247], a[84:87], v[100:103]\n\t"
  "ds_read_b64_tr_b16 v[236:237], v17 offset:9728\n\t"
  "ds_read_b64_tr_b16 v[238:239], v17 offset:13824\n\t"
  "v_mfma_f32_16x16x32_bf16 v[104:107], v[240:243], a[72:75], v[104:107]\n\t"
  "v_rcp_f32_e64 v15, v15\n\t"
  "v_mfma_f32_16x16x32_bf16 v[104:107], v[244:247], a[88:91], v[104:107]\n\t"
  "v_mul_f32_e32 v176, v176, v28\n\t"
  "v_mul_f32_e32 v178, v178, v14\n\t"
  "v_mfma_f32_16x16x32_bf16 v[108:111], v[240:243], a[76:79], v[108:111]\n\t"
  "v_mul_f32_e32 v177, v177, v29\n\t"
  "v_mul_f32_e32 v179, v179, v15\n\t"
  "v_mfma_f32_16x16x32_bf16 v[108:111], v[244:247], a[92:95], v[108:111]\n\t"
  "v_cvt_pk_bf16_f32 v162, v176, v177\n\t"
  "v_cvt_pk_bf16_f32 v163, v178, v179\n\t"
  "v_mfma_f32_16x16x32_bf16 v[112:115], v[248:251], a[64:67], v[112:115]\n\t"
  "s_waitcnt lgkmcnt(0)\n\t"
  "s_add_u32 s40, s46, 0\n\t"
  "buffer_store_dwordx4 v[160:163], v1, s[32:35], s40 offen sc1\n\t"
  "v_fma_f32 v180, v21, v180, v228\n\t"
  "v_mfma_f32_16x16x32_bf16 v[112:115], v[252:255], a[80:83], v[112:115]\n\t"
  "ds_read_b64_tr_b16 v[240:241], v17 offset:1544\n\t"
  "ds_read_b64_tr_b16 v[242:243], v17 offset:5640\n\t"
  "v_mfma_f32_16x16x32_bf16 v[116:119], v[248:251], a[68:71], v[116:119]\n\t"
  "v_fma_f32 v182, v21, v182, v230\n\t"
  "v_fma_f32 v181, v21, v181, v229\n\t"
  "v_mfma_f32_16x16x32_bf16 v[116:119], v[252:255], a[84:87], v[116:119]\n\t"
  "ds_read_b64_tr_b16 v[244:245], v17 offset:9736\n\t"
  "ds_read_b64_tr_b16 v[246:247], v17 offset:13832\n\t"
  "v_mfma_f32_16x16x32_bf16 v[120:123], v[248:251], a[72:75], v[120:123]\n\t"
  "v_fma_f32 v183, v21, v183, v231\n\t"
  "v_mul_f32_e64 v28, v180, v180\n\t"
  "v_mfma_f32_16x16x32_bf16 v[120:123], v[252:255], a[88:91], v[120:123]\n\t"
  "v_mul_f32_e32 v14, v182, v182\n\t"
  "v_mul_f32_e32 v29, v181, v181\n\t"
  "v_mfma_f32_16x16x32_bf16 v[124:127], v[248:251], a[76:79], v[124:127]\n\t"
  "v_mul_f32_e64 v15, v183, v183\n\t"
  "v_fma_f32 v28, v28, s49, v31\n\t"
  "v_mfma_f32_16x16x32_bf16 v[124:127], v[252:255], a[92:95], v[124:127]\n\t"
  "v_fma_f32 v14, v14, s49, v31\n\t"
  "v_fma_f32 v29, v29, s49, v31\n\t"
  "s_waitcnt vmcnt(5)\n\t"
  "s_barrier\n\t"
  "v_mfma_f32_16x16x32_bf16 v[128:131], v[232:235], a[64:67], v[128:131]\n\t"
  "s_add_u32 m0, s44, 114688\n\t"
  "s_add_u32 s41, s45, 3670016\n\t"
  "buffer_load_dwordx4 v19, s[28:31], s41 offen lds\n\t"
  "v_fma_f32 v15, v15, s49, v31\n\t"
  "v_mfma_f32_16x16x32_bf16 v[128:131], v[236:239], a[80:83], v[128:131]\n\t"
  "ds_read_b64_tr_b16 v[248:249], v17 offset:16384\n\t"
  "ds_read_b64_tr_b16 v[250:251], v17 offset:20480\n\t"
  "v_mfma_f32_16x16x32_bf16 v[132:135], v[232:235], a[68:71], v[132:135]\n\t"
  "s_add_u32 m0, s44, 115712\n\t"
  "s_add_u32 s41, s45, 3670144\n\t"
  "buffer_load_dwordx4 v19, s[28:31], s41 offen lds\n\t"
  "v_mul_f32_e64 v28, v180, v28\n\t"
  "v_mfma_f32_16x16x32_bf16 v[132:135], v[236:239], a[84:87], v[132:135]\n\t"
  "ds_read_b64_tr_b16 v[252:253], v17 offset:24576\n\t"
  "ds_read_b64_tr_b16 v[254:255], v17 offset:28672\n\t"
  "v_mfma_f32_16x16x32_bf16 v[136:139], v[232:235], a[72:75], v[136:139]\n\t"
  "s_add_u32 m0, s44, 116736\n\t"
  "s_add_u32 s41, s45, 3735552\n\t"
  "buffer_load_dwordx4 v19, s[28:31], s41 offen lds\n\t"
  "v_mul_f32_e64 v14, v182, v14\n\t"
  "v_mfma_f32_16x16x32_bf16 v[136:139], v[236:239], a[88:91], v[136:139]\n\t"
  "v_mul_f32_e32 v29, v181, v29\n\t"
  "v_mul_f32_e32 v15, v183, v15\n\t"
  "v_mfma_f32_16x16x32_bf16 v[140:143], v[232:235], a[76:79], v[140:143]\n\t"
  "s_add_u32 m0, s44, 117760\n\t"
  "s_add_u32 s41, s45, 3735680\n\t"
  "buffer_load_dwordx4 v19, s[28:31], s41 offen lds\n\t"
  "v_mfma_f32_16x16x32_bf16 v[140:143], v[236:239], a[92:95], v[140:143]\n\t"
  "s_waitcnt lgkmcnt(2)\n\t"
  "v_exp_f32_e32 v28, v28\n\t"
  "v_mfma_f32_16x16x32_bf16 v[144:147], v[240:243], a[64:67], v[144:147]\n\t"
  "v_exp_f32_e64 v14, v14\n\t"
  "v_mfma_f32_16x16x32_bf16 v[144:147], v[244:247], a[80:83], v[144:147]\n\t"
  "ds_read_b64_tr_b16 v[232:233], v17 offset:16392\n\t"
  "ds_read_b64_tr_b16 v[234:235], v17 offset:20488\n\t"
  "v_mfma_f32_16x16x32_bf16 v[148:151], v[240:243], a[68:71], v[148:151]\n\t"
  "v_exp_f32_e64 v29, v29\n\t"
  "v_mfma_f32_16x16x32_bf16 v[148:151], v[244:247], a[84:87], v[148:151]\n\t"
  "ds_read_b64_tr_b16 v[236:237], v17 offset:24584\n\t"
  "ds_read_b64_tr_b16 v[238:239], v17 offset:28680\n\t"
  "v_mfma_f32_16x16x32_bf16 v[152:155], v[240:243], a[72:75], v[152:155]\n\t"
  "v_exp_f32_e64 v15, v15\n\t"
  "v_mfma_f32_16x16x32_bf16 v[152:155], v[244:247], a[88:91], v[152:155]\n\t"
  "v_add_f32_e32 v28, 1.0, v28\n\t"
  "v_add_f32_e32 v14, 1.0, v14\n\t"
  "v_mfma_f32_16x16x32_bf16 v[156:159], v[240:243], a[76:79], v[156:159]\n\t"
  "v_add_f32_e32 v29, 1.0, v29\n\t"
  "v_add_f32_e32 v15, 1.0, v15\n\t"
  "v_mfma_f32_16x16x32_bf16 v[156:159], v[244:247], a[92:95], v[156:159]\n\t"
  "v_rcp_f32_e64 v28, v28\n\t"
  "v_mfma_f32_16x16x32_bf16 v[32:35], v[248:251], a[96:99], v[32:35]\n\t"
  "s_waitcnt lgkmcnt(0)\n\t"
  "v_rcp_f32_e32 v14, v14\n\t"
  "v_mfma_f32_16x16x32_bf16 v[32:35], v[252:255], a[112:115], v[32:35]\n\t"
  "ds_read_b64_tr_b16 v[240:241], v17 offset:16896\n\t"
  "ds_read_b64_tr_b16 v[242:243], v17 offset:20992\n\t"
  "v_mfma_f32_16x16x32_bf16 v[36:39], v[248:251], a[100:103], v[36:39]\n\t"
  "v_rcp_f32_e64 v29, v29\n\t"
  "v_mfma_f32_16x16x32_bf16 v[36:39], v[252:255], a[116:119], v[36:39]\n\t"
  "ds_read_b64_tr_b16 v[244:245], v17 offset:25088\n\t"
  "ds_read_b64_tr_b16 v[246:247], v17 offset:29184\n\t"
  "v_mfma_f32_16x16x32_bf16 v[40:43], v[248:251], a[104:107], v[40:43]\n\t"
  "v_rcp_f32_e64 v15, v15\n\t"
  "v_mfma_f32_16x16x32_bf16 v[40:43], v[252:255], a[120:123], v[40:43]\n\t"
  "v_mul_f32_e32 v180, v180, v28\n\t"
  "v_mul_f32_e32 v182, v182, v14\n\t"
  "v_mfma_f32_16x16x32_bf16 v[44:47], v[248:251], a[108:111], v[44:47]\n\t"
  "v_mul_f32_e32 v181, v181, v29\n\t"
  "v_mul_f32_e32 v183, v183, v15\n\t"
  "v_mfma_f32_16x16x32_bf16 v[44:47], v[252:255], a[124:127], v[44:47]\n\t"
  "v_cvt_pk_bf16_f32 v166, v180, v181\n\t"
  "v_cvt_pk_bf16_f32 v167, v182, v183\n\t"
  "v_mfma_f32_16x16x32_bf16 v[48:51], v[232:235], a[96:99], v[48:51]\n\t"
  "s_add_u32 s40, s46, 188416\n\t"
  "buffer_store_dwordx4 v[164:167], v1, s[32:35], s40 offen sc1\n\t"
  "v_fma_f32 v184, v22, v184, v228\n\t"
  "v_mfma_f32_16x16x32_bf16 v[48:51], v[236:239], a[112:115], v[48:51]\n\t"
  "ds_read_b64_tr_b16 v[248:249], v17 offset:16904\n\t"
  "ds_read_b64_tr_b16 v[250:251], v17 offset:21000\n\t"
  "v_mfma_f32_16x16x32_bf16 v[52:55], v[232:235], a[100:103], v[52:55]\n\t"
  "v_fma_f32 v186, v22, v186, v230\n\t"
  "v_fma_f32 v185, v22, v185, v229\n\t"
  "v_mfma_f32_16x16x32_bf16 v[52:55], v[236:239], a[116:119], v[52:55]\n\t"
  "ds_read_b64_tr_b16 v[252:253], v17 offset:25096\n\t"
  "ds_read_b64_tr_b16 v[254:255], v17 offset:29192\n\t"
  "v_mfma_f32_16x16x32_bf16 v[56:59], v[232:235], a[104:107], v[56:59]\n\t"
  "v_fma_f32 v187, v22, v187, v231\n\t"
  "v_mul_f32_e64 v28, v184, v184\n\t"
  "v_mfma_f32_16x16x32_bf16 v[56:59], v[236:239], a[120:123], v[56:59]\n\t"
  "v_mul_f32_e32 v14, v186, v186\n\t"
  "v_mul_f32_e32 v29, v185, v185\n\t"
  "v_mfma_f32_16x16x32_bf16 v[60:63], v[232:235], a[108:111], v[60:63]\n\t"
  "v_mul_f32_e64 v15, v187, v187\n\t"
  "v_fma_f32 v28, v28, s49, v31\n\t"
  "v_mfma_f32_16x16x32_bf16 v[60:63], v[236:239], a[124:127], v[60:63]\n\t"
  "s_waitcnt lgkmcnt(2)\n\t"
  "s_nop 0\n\t"
  "v_fma_f32 v14, v14, s49, v31\n\t"
  "v_fma_f32 v29, v29, s49, v31\n\t"
  "v_mfma_f32_16x16x32_bf16 v[64:67], v[240:243], a[96:99], v[64:67]\n\t"
  "v_fma_f32 v15, v15, s49, v31\n\t"
  "v_mul_f32_e64 v28, v184, v28\n\t"
  "v_mfma_f32_16x16x32_bf16 v[64:67], v[244:247], a[112:115], v[64:67]\n\t"
  "ds_read_b64_tr_b16 v[232:233], v17 offset:17408\n\t"
  "ds_read_b64_tr_b16 v[234:235], v17 offset:21504\n\t"
  "v_mfma_f32_16x16x32_bf16 v[68:71], v[240:243], a[100:103], v[68:71]\n\t"
  "v_mul_f32_e32 v14, v186, v14\n\t"
  "v_mul_f32_e32 v29, v185, v29\n\t"
  "v_mfma_f32_16x16x32_bf16 v[68:71], v[244:247], a[116:119], v[68:71]\n\t"
  "ds_read_b64_tr_b16 v[236:237], v17 offset:25600\n\t"
  "ds_read_b64_tr_b16 v[238:239], v17 offset:29696\n\t"
  "v_mfma_f32_16x16x32_bf16 v[72:75], v[240:243], a[104:107], v[72:75]\n\t"
  "v_mul_f32_e64 v15, v187, v15\n\t"
  "v_mfma_f32_16x16x32_bf16 v[72:75], v[244:247], a[120:123], v[72:75]\n\t"
  "v_exp_f32_e64 v28, v28\n\t"
  "v_mfma_f32_16x16x32_bf16 v[76:79], v[240:243], a[108:111], v[76:79]\n\t"
  "v_exp_f32_e64 v14, v14\n\t"
  "v_mfma_f32_16x16x32_bf16 v[76:79], v[244:247], a[124:127], v[76:79]\n\t"
  "v_exp_f32_e64 v29, v29\n\t"
  "v_mfma_f32_16x16x32_bf16 v[80:83], v[248:251], a[96:99], v[80:83]\n\t"
  "s_waitcnt lgkmcnt(0)\n\t"
  "v_exp_f32_e32 v15, v15\n\t"
  "v_mfma_f32_16x16x32_bf16 v[80:83], v[252:255], a[112:115], v[80:83]\n\t"
  "ds_read_b64_tr_b16 v[240:241], v17 offset:17416\n\t"
  "ds_read_b64_tr_b16 v[242:243], v17 offset:21512\n\t"
  "v_mfma_f32_16x16x32_bf16 v[84:87], v[248:251], a[100:103], v[84:87]\n\t"
  "v_add_f32_e32 v28, 1.0, v28\n\t"
  "v_add_f32_e32 v14, 1.0, v14\n\t"
  "v_mfma_f32_16x16x32_bf16 v[84:87], v[252:255], a[116:119], v[84:87]\n\t"
  "ds_read_b64_tr_b16 v[244:245], v17 offset:25608\n\t"
  "ds_read_b64_tr_b16 v[246:247], v17 offset:29704\n\t"
  "v_mfma_f32_16x16x32_bf16 v[88:91], v[248:251], a[104:107], v[88:91]\n\t"
  "v_add_f32_e32 v29, 1.0, v29\n\t"
  "v_add_f32_e32 v15, 1.0, v15\n\t"
  "v_mfma_f32_16x16x32_bf16 v[88:91], v[252:255], a[120:123], v[88:91]\n\t"
  "v_rcp_f32_e64 v28, v28\n\t"
  "v_mfma_f32_16x16x32_bf16 v[92:95], v[248:251], a[108:111], v[92:95]\n\t"
  "v_rcp_f32_e64 v14, v14\n\t"
  "v_mfma_f32_16x16x32_bf16 v[92:95], v[252:255], a[124:127], v[92:95]\n\t"
  "v_rcp_f32_e64 v29, v29\n\t"
  "v_mfma_f32_16x16x32_bf16 v[96:99], v[232:235], a[96:99], v[96:99]\n\t"
  "v_rcp_f32_e64 v15, v15\n\t"
  "v_mfma_f32_16x16x32_bf16 v[96:99], v[236:239], a[112:115], v[96:99]\n\t"
  "ds_read_b64_tr_b16 v[248:249], v17 offset:17920\n\t"
  "ds_read_b64_tr_b16 v[250:251], v17 offset:22016\n\t"
  "v_mfma_f32_16x16x32_bf16 v[100:103], v[232:235], a[100:103], v[100:103]\n\t"
  "v_mul_f32_e32 v184, v184, v28\n\t"
  "v_mul_f32_e32 v186, v186, v14\n\t"
  "v_mfma_f32_16x16x32_bf16 v[100:103], v[236:239], a[116:119], v[100:103]\n\t"
  "ds_read_b64_tr_b16 v[252:253], v17 offset:26112\n\t"
  "ds_read_b64_tr_b16 v[254:255], v17 offset:30208\n\t"
  "v_mfma_f32_16x16x32_bf16 v[104:107], v[232:235], a[104:107], v[104:107]\n\t"
  "v_mul_f32_e32 v185, v185, v29\n\t"
  "v_mul_f32_e32 v187, v187, v15\n\t"
  "v_mfma_f32_16x16x32_bf16 v[104:107], v[236:239], a[120:123], v[104:107]\n\t"
  "v_cvt_pk_bf16_f32 v170, v184, v185\n\t"
  "v_cvt_pk_bf16_f32 v171, v186, v187\n\t"
  "v_mfma_f32_16x16x32_bf16 v[108:111], v[232:235], a[108:111], v[108:111]\n\t"
  "s_add_u32 s40, s46, 376832\n\t"
  "buffer_store_dwordx4 v[168:171], v1, s[32:35], s40 offen sc1\n\t"
  "v_fma_f32 v188, v23, v188, v228\n\t"
  "v_mfma_f32_16x16x32_bf16 v[108:111], v[236:239], a[124:127], v[108:111]\n\t"
  "s_waitcnt lgkmcnt(2)\n\t"
  "s_nop 0\n\t"
  "v_fma_f32 v190, v23, v190, v230\n\t"
  "v_fma_f32 v189, v23, v189, v229\n\t"
  "v_mfma_f32_16x16x32_bf16 v[112:115], v[240:243], a[96:99], v[112:115]\n\t"
  "v_fma_f32 v191, v23, v191, v231\n\t"
  "v_mul_f32_e64 v28, v188, v188\n\t"
  "v_mfma_f32_16x16x32_bf16 v[112:115], v[244:247], a[112:115], v[112:115]\n\t"
  "ds_read_b64_tr_b16 v[232:233], v17 offset:17928\n\t"
  "ds_read_b64_tr_b16 v[234:235], v17 offset:22024\n\t"
  "v_mfma_f32_16x16x32_bf16 v[116:119], v[240:243], a[100:103], v[116:119]\n\t"
  "v_mul_f32_e32 v14, v190, v190\n\t"
  "v_mul_f32_e32 v29, v189, v189\n\t"
  "v_mfma_f32_16x16x32_bf16 v[116:119], v[244:247], a[116:119], v[116:119]\n\t"
  "ds_read_b64_tr_b16 v[236:237], v17 offset:26120\n\t"
  "ds_read_b64_tr_b16 v[238:239], v17 offset:30216\n\t"
  "v_mfma_f32_16x16x32_bf16 v[120:123], v[240:243], a[104:107], v[120:123]\n\t"
  "v_mul_f32_e64 v15, v191, v191\n\t"
  "v_fma_f32 v28, v28, s49, v31\n\t"
  "v_mfma_f32_16x16x32_bf16 v[120:123], v[244:247], a[120:123], v[120:123]\n\t"
  "v_fma_f32 v14, v14, s49, v31\n\t"
  "v_fma_f32 v29, v29, s49, v31\n\t"
  "v_mfma_f32_16x16x32_bf16 v[124:127], v[240:243], a[108:111], v[124:127]\n\t"
  "v_fma_f32 v15, v15, s49, v31\n\t"
  "v_mul_f32_e64 v28, v188, v28\n\t"
  "v_mfma_f32_16x16x32_bf16 v[124:127], v[244:247], a[124:127], v[124:127]\n\t"
  "v_mul_f32_e32 v14, v190, v14\n\t"
  "v_mul_f32_e32 v29, v189, v29\n\t"
  "s_waitcnt vmcnt(28)\n\t"
  "s_barrier\n\t"
  "v_mfma_f32_16x16x32_bf16 v[128:131], v[248:251], a[96:99], v[128:131]\n\t"
  "s_waitcnt lgkmcnt(0)\n\t"
  "v_mul_f32_e32 v15, v191, v15\n\t"
  "v_mfma_f32_16x16x32_bf16 v[128:131], v[252:255], a[112:115], v[128:131]\n\t"
  "ds_read_b64_tr_b16 v[240:241], v16 offset:0\n\t"
  "ds_read_b64_tr_b16 v[242:243], v16 offset:4096\n\t"
  "v_mfma_f32_16x16x32_bf16 v[132:135], v[248:251], a[100:103], v[132:135]\n\t"
  "v_exp_f32_e64 v28, v28\n\t"
  "v_mfma_f32_16x16x32_bf16 v[132:135], v[252:255], a[116:119], v[132:135]\n\t"
  "ds_read_b64_tr_b16 v[244:245], v16 offset:8192\n\t"
  "ds_read_b64_tr_b16 v[246:247], v16 offset:12288\n\t"
  "v_mfma_f32_16x16x32_bf16 v[136:139], v[248:251], a[104:107], v[136:139]\n\t"
  "v_exp_f32_e64 v14, v14\n\t"
  "v_mfma_f32_16x16x32_bf16 v[136:139], v[252:255], a[120:123], v[136:139]\n\t"
  "v_exp_f32_e64 v29, v29\n\t"
  "v_mfma_f32_16x16x32_bf16 v[140:143], v[248:251], a[108:111], v[140:143]\n\t"
  "v_exp_f32_e64 v15, v15\n\t"
  "v_mfma_f32_16x16x32_bf16 v[140:143], v[252:255], a[124:127], v[140:143]\n\t"
  "v_add_f32_e32 v28, 1.0, v28\n\t"
  "v_add_f32_e32 v14, 1.0, v14\n\t"
  "v_mfma_f32_16x16x32_bf16 v[144:147], v[232:235], a[96:99], v[144:147]\n\t"
  "v_add_f32_e32 v29, 1.0, v29\n\t"
  "v_add_f32_e32 v15, 1.0, v15\n\t"
  "v_mfma_f32_16x16x32_bf16 v[144:147], v[236:239], a[112:115], v[144:147]\n\t"
  "ds_read_b64_tr_b16 v[248:249], v16 offset:8\n\t"
  "ds_read_b64_tr_b16 v[250:251], v16 offset:4104\n\t"
  "v_mfma_f32_16x16x32_bf16 v[148:151], v[232:235], a[100:103], v[148:151]\n\t"
  "v_rcp_f32_e64 v28, v28\n\t"
  "v_mfma_f32_16x16x32_bf16 v[148:151], v[236:239], a[116:119], v[148:151]\n\t"
  "ds_read_b64_tr_b16 v[252:253], v16 offset:8200\n\t"
  "ds_read_b64_tr_b16 v[254:255], v16 offset:12296\n\t"
  "v_mfma_f32_16x16x32_bf16 v[152:155], v[232:235], a[104:107], v[152:155]\n\t"
  "v_rcp_f32_e64 v14, v14\n\t"
  "v_mfma_f32_16x16x32_bf16 v[152:155], v[236:239], a[120:123], v[152:155]\n\t"
  "v_rcp_f32_e64 v29, v29\n\t"
  "v_mfma_f32_16x16x32_bf16 v[156:159], v[232:235], a[108:111], v[156:159]\n\t"
  "v_rcp_f32_e64 v15, v15\n\t"
  "v_mfma_f32_16x16x32_bf16 v[156:159], v[236:239], a[124:127], v[156:159]\n\t"
  "s_waitcnt lgkmcnt(2)\n\t"
  "v_mul_f32_e32 v188, v188, v28\n\t"
  "v_mul_f32_e64 v190, v190, v14\n\t"
  "v_mfma_f32_16x16x32_bf16 v[32:35], v[240:243], a[128:131], v[32:35]\n\t"
  "v_mul_f32_e32 v189, v189, v29\n\t"
  "v_mul_f32_e32 v191, v191, v15\n\t"
  "v_mfma_f32_16x16x32_bf16 v[32:35], v[244:247], a[144:147], v[32:35]\n\t"
  "ds_read_b64_tr_b16 v[232:233], v16 offset:512\n\t"
  "ds_read_b64_tr_b16 v[234:235], v16 offset:4608\n\t"
  "v_mfma_f32_16x16x32_bf16 v[36:39], v[240:243], a[132:135], v[36:39]\n\t"
  "v_cvt_pk_bf16_f32 v174, v188, v189\n\t"
  "v_cvt_pk_bf16_f32 v175, v190, v191\n\t"
  "v_mfma_f32_16x16x32_bf16 v[36:39], v[244:247], a[148:151], v[36:39]\n\t"
  "ds_read_b64_tr_b16 v[236:237], v16 offset:8704\n\t"
  "ds_read_b64_tr_b16 v[238:239], v16 offset:12800\n\t"
  "v_mfma_f32_16x16x32_bf16 v[40:43], v[240:243], a[136:139], v[40:43]\n\t"
  "s_add_u32 s40, s46, 565248\n\t"
  "buffer_store_dwordx4 v[172:175], v1, s[32:35], s40 offen sc1\n\t"
  "v_fma_f32 v192, v20, v192, v24\n\t"
  "v_mfma_f32_16x16x32_bf16 v[40:43], v[244:247], a[152:155], v[40:43]\n\t"
  "v_fma_f32 v194, v20, v194, v26\n\t"
  "v_fma_f32 v193, v20, v193, v25\n\t"
  "v_mfma_f32_16x16x32_bf16 v[44:47], v[240:243], a[140:143], v[44:47]\n\t"
  "v_fma_f32 v195, v20, v195, v27\n\t"
  "v_mul_f32_e64 v28, v192, v192\n\t"
  "v_mfma_f32_16x16x32_bf16 v[44:47], v[244:247], a[156:159], v[44:47]\n\t"
  "v_mul_f32_e32 v14, v194, v194\n\t"
  "v_mul_f32_e32 v29, v193, v193\n\t"
  "v_mfma_f32_16x16x32_bf16 v[48:51], v[248:251], a[128:131], v[48:51]\n\t"
  "s_waitcnt lgkmcnt(0)\n\t"
  "v_mul_f32_e32 v15, v195, v195\n\t"
  "v_fma_f32 v28, v28, s49, v31\n\t"
  "v_mfma_f32_16x16x32_bf16 v[48:51], v[252:255], a[144:147], v[48:51]\n\t"
  "ds_read_b64_tr_b16 v[240:241], v16 offset:520\n\t"
  "ds_read_b64_tr_b16 v[242:243], v16 offset:4616\n\t"
  "v_mfma_f32_16x16x32_bf16 v[52:55], v[248:251], a[132:135], v[52:55]\n\t"
  "v_fma_f32 v14, v14, s49, v31\n\t"
  "v_fma_f32 v29, v29, s49, v31\n\t"
  "v_mfma_f32_16x16x32_bf16 v[52:55], v[252:255], a[148:151], v[52:55]\n\t"
  "ds_read_b64_tr_b16 v[244:245], v16 offset:8712\n\t"
  "ds_read_b64_tr_b16 v[246:247], v16 offset:12808\n\t"
  "v_mfma_f32_16x16x32_bf16 v[56:59], v[248:251], a[136:139], v[56:59]\n\t"
  "v_fma_f32 v15, v15, s49, v31\n\t"
  "v_mul_f32_e64 v28, v192, v28\n\t"
  "v_mfma_f32_16x16x32_bf16 v[56:59], v[252:255], a[152:155], v[56:59]\n\t"
  "v_mul_f32_e32 v14, v194, v14\n\t"
  "v_mul_f32_e32 v29, v193, v29\n\t"
  "v_mfma_f32_16x16x32_bf16 v[60:63], v[248:251], a[140:143], v[60:63]\n\t"
  "v_mul_f32_e64 v15, v195, v15\n\t"
  "v_mfma_f32_16x16x32_bf16 v[60:63], v[252:255], a[156:159], v[60:63]\n\t"
  "v_exp_f32_e64 v28, v28\n\t"
  "v_mfma_f32_16x16x32_bf16 v[64:67], v[232:235], a[128:131], v[64:67]\n\t"
  "v_exp_f32_e64 v14, v14\n\t"
  "v_mfma_f32_16x16x32_bf16 v[64:67], v[236:239], a[144:147], v[64:67]\n\t"
  "ds_read_b64_tr_b16 v[248:249], v16 offset:1024\n\t"
  "ds_read_b64_tr_b16 v[250:251], v16 offset:5120\n\t"
  "v_mfma_f32_16x16x32_bf16 v[68:71], v[232:235], a[132:135], v[68:71]\n\t"
  "v_exp_f32_e64 v29, v29\n\t"
  "v_mfma_f32_16x16x32_bf16 v[68:71], v[236:239], a[148:151], v[68:71]\n\t"
  "ds_read_b64_tr_b16 v[252:253], v16 offset:9216\n\t"
  "ds_read_b64_tr_b16 v[254:255], v16 offset:13312\n\t"
  "v_mfma_f32_16x16x32_bf16 v[72:75], v[232:235], a[136:139], v[72:75]\n\t"
  "v_exp_f32_e64 v15, v15\n\t"
  "v_mfma_f32_16x16x32_bf16 v[72:75], v[236:239], a[152:155], v[72:75]\n\t"
  "v_add_f32_e32 v28, 1.0, v28\n\t"
  "v_add_f32_e32 v14, 1.0, v14\n\t"
  "v_mfma_f32_16x16x32_bf16 v[76:79], v[232:235], a[140:143], v[76:79]\n\t"
  "v_add_f32_e32 v29, 1.0, v29\n\t"
  "v_add_f32_e32 v15, 1.0, v15\n\t"
  "v_mfma_f32_16x16x32_bf16 v[76:79], v[236:239], a[156:159], v[76:79]\n\t"
  "s_waitcnt lgkmcnt(2)\n\t"
  "v_rcp_f32_e32 v28, v28\n\t"
  "v_mfma_f32_16x16x32_bf16 v[80:83], v[240:243], a[128:131], v[80:83]\n\t"
  "v_rcp_f32_e64 v14, v14\n\t"
  "v_mfma_f32_16x16x32_bf16 v[80:83], v[244:247], a[144:147], v[80:83]\n\t"
  "ds_read_b64_tr_b16 v[232:233], v16 offset:1032\n\t"
  "ds_read_b64_tr_b16 v[234:235], v16 offset:5128\n\t"
  "v_mfma_f32_16x16x32_bf16 v[84:87], v[240:243], a[132:135], v[84:87]\n\t"
  "v_rcp_f32_e64 v29, v29\n\t"
  "v_mfma_f32_16x16x32_bf16 v[84:87], v[244:247], a[148:151], v[84:87]\n\t"
  "ds_read_b64_tr_b16 v[236:237], v16 offset:9224\n\t"
  "ds_read_b64_tr_b16 v[238:239], v16 offset:13320\n\t"
  "v_mfma_f32_16x16x32_bf16 v[88:91], v[240:243], a[136:139], v[88:91]\n\t"
  "v_rcp_f32_e64 v15, v15\n\t"
  "v_mfma_f32_16x16x32_bf16 v[88:91], v[244:247], a[152:155], v[88:91]\n\t"
  "v_mul_f32_e32 v192, v192, v28\n\t"
  "v_mul_f32_e32 v194, v194, v14\n\t"
  "v_mfma_f32_16x16x32_bf16 v[92:95], v[240:243], a[140:143], v[92:95]\n\t"
  "v_mul_f32_e32 v193, v193, v29\n\t"
  "v_mul_f32_e32 v195, v195, v15\n\t"
  "v_mfma_f32_16x16x32_bf16 v[92:95], v[244:247], a[156:159], v[92:95]\n\t"
  "v_cvt_pk_bf16_f32 v192, v192, v193\n\t"
  "v_cvt_pk_bf16_f32 v193, v194, v195\n\t"
  "v_mfma_f32_16x16x32_bf16 v[96:99], v[248:251], a[128:131], v[96:99]\n\t"
  "s_waitcnt lgkmcnt(0)\n\t"
  "s_nop 0\n\t"
  "v_fma_f32 v196, v21, v196, v24\n\t"
  "v_fma_f32 v198, v21, v198, v26\n\t"
  "v_mfma_f32_16x16x32_bf16 v[96:99], v[252:255], a[144:147], v[96:99]\n\t"
  "ds_read_b64_tr_b16 v[240:241], v16 offset:1536\n\t"
  "ds_read_b64_tr_b16 v[242:243], v16 offset:5632\n\t"
  "v_mfma_f32_16x16x32_bf16 v[100:103], v[248:251], a[132:135], v[100:103]\n\t"
  "v_fma_f32 v197, v21, v197, v25\n\t"
  "v_fma_f32 v199, v21, v199, v27\n\t"
  "v_mfma_f32_16x16x32_bf16 v[100:103], v[252:255], a[148:151], v[100:103]\n\t"
  "ds_read_b64_tr_b16 v[244:245], v16 offset:9728\n\t"
  "ds_read_b64_tr_b16 v[246:247], v16 offset:13824\n\t"
  "v_mfma_f32_16x16x32_bf16 v[104:107], v[248:251], a[136:139], v[104:107]\n\t"
  "v_mul_f32_e32 v28, v196, v196\n\t"
  "v_mul_f32_e32 v14, v198, v198\n\t"
  "v_mfma_f32_16x16x32_bf16 v[104:107], v[252:255], a[152:155], v[104:107]\n\t"
  "v_mul_f32_e32 v29, v197, v197\n\t"
  "v_mul_f32_e32 v15, v199, v199\n\t"
  "v_mfma_f32_16x16x32_bf16 v[108:111], v[248:251], a[140:143], v[108:111]\n\t"
  "v_fma_f32 v28, v28, s49, v31\n\t"
  "v_fma_f32 v14, v14, s49, v31\n\t"
  "v_mfma_f32_16x16x32_bf16 v[108:111], v[252:255], a[156:159], v[108:111]\n\t"
  "v_fma_f32 v29, v29, s49, v31\n\t"
  "v_fma_f32 v15, v15, s49, v31\n\t"
  "v_mfma_f32_16x16x32_bf16 v[112:115], v[232:235], a[128:131], v[112:115]\n\t"
  "v_mul_f32_e32 v28, v196, v28\n\t"
  "v_mul_f32_e32 v14, v198, v14\n\t"
  "v_mfma_f32_16x16x32_bf16 v[112:115], v[236:239], a[144:147], v[112:115]\n\t"
  "ds_read_b64_tr_b16 v[248:249], v16 offset:1544\n\t"
  "ds_read_b64_tr_b16 v[250:251], v16 offset:5640\n\t"
  "v_mfma_f32_16x16x32_bf16 v[116:119], v[232:235], a[132:135], v[116:119]\n\t"
  "v_mul_f32_e32 v29, v197, v29\n\t"
  "v_mul_f32_e32 v15, v199, v15\n\t"
  "v_mfma_f32_16x16x32_bf16 v[116:119], v[236:239], a[148:151], v[116:119]\n\t"
  "ds_read_b64_tr_b16 v[252:253], v16 offset:9736\n\t"
  "ds_read_b64_tr_b16 v[254:255], v16 offset:13832\n\t"
  "v_mfma_f32_16x16x32_bf16 v[120:123], v[232:235], a[136:139], v[120:123]\n\t"
  "v_exp_f32_e64 v28, v28\n\t"
  "v_mfma_f32_16x16x32_bf16 v[120:123], v[236:239], a[152:155], v[120:123]\n\t"
  "v_exp_f32_e64 v14, v14\n\t"
  "v_mfma_f32_16x16x32_bf16 v[124:127], v[232:235], a[140:143], v[124:127]\n\t"
  "v_exp_f32_e64 v29, v29\n\t"
  "v_mfma_f32_16x16x32_bf16 v[124:127], v[236:239], a[156:159], v[124:127]\n\t"
  "v_exp_f32_e32 v15, v15\n\t"
  "s_waitcnt vmcnt(25)\n\t"
  "s_barrier\n\t"
  "s_waitcnt lgkmcnt(2)\n\t"
  "v_mfma_f32_16x16x32_bf16 v[128:131], v[240:243], a[128:131], v[128:131]\n\t"
  "v_add_f32_e32 v28, 1.0, v28\n\t"
  "v_add_f32_e32 v14, 1.0, v14\n\t"
  "v_mfma_f32_16x16x32_bf16 v[128:131], v[244:247], a[144:147], v[128:131]\n\t"
  "ds_read_b64_tr_b16 v[232:233], v16 offset:16384\n\t"
  "ds_read_b64_tr_b16 v[234:235], v16 offset:20480\n\t"
  "v_mfma_f32_16x16x32_bf16 v[132:135], v[240:243], a[132:135], v[132:135]\n\t"
  "v_add_f32_e32 v29, 1.0, v29\n\t"
  "v_add_f32_e32 v15, 1.0, v15\n\t"
  "v_mfma_f32_16x16x32_bf16 v[132:135], v[244:247], a[148:151], v[132:135]\n\t"
  "ds_read_b64_tr_b16 v[236:237], v16 offset:24576\n\t"
  "ds_read_b64_tr_b16 v[238:239], v16 offset:28672\n\t"
  "v_mfma_f32_16x16x32_bf16 v[136:139], v[240:243], a[136:139], v[136:139]\n\t"
  "v_rcp_f32_e64 v28, v28\n\t"
  "v_mfma_f32_16x16x32_bf16 v[136:139], v[244:247], a[152:155], v[136:139]\n\t"
  "v_rcp_f32_e64 v14, v14\n\t"
  "v_mfma_f32_16x16x32_bf16 v[140:143], v[240:243], a[140:143], v[140:143]\n\t"
  "v_rcp_f32_e64 v29, v29\n\t"
  "v_mfma_f32_16x16x32_bf16 v[140:143], v[244:247], a[156:159], v[140:143]\n\t"
  "v_rcp_f32_e64 v15, v15\n\t"
  "v_mfma_f32_16x16x32_bf16 v[144:147], v[248:251], a[128:131], v[144:147]\n\t"
  "s_waitcnt lgkmcnt(0)\n\t"
  "v_mul_f32_e32 v196, v196, v28\n\t"
  "v_mul_f32_e64 v198, v198, v14\n\t"
  "v_mfma_f32_16x16x32_bf16 v[144:147], v[252:255], a[144:147], v[144:147]\n\t"
  "ds_read_b64_tr_b16 v[240:241], v16 offset:16392\n\t"
  "ds_read_b64_tr_b16 v[242:243], v16 offset:20488\n\t"
  "v_mfma_f32_16x16x32_bf16 v[148:151], v[248:251], a[132:135], v[148:151]\n\t"
  "v_mul_f32_e32 v197, v197, v29\n\t"
  "v_mul_f32_e32 v199, v199, v15\n\t"
  "v_mfma_f32_16x16x32_bf16 v[148:151], v[252:255], a[148:151], v[148:151]\n\t"
  "ds_read_b64_tr_b16 v[244:245], v16 offset:24584\n\t"
  "ds_read_b64_tr_b16 v[246:247], v16 offset:28680\n\t"
  "v_mfma_f32_16x16x32_bf16 v[152:155], v[248:251], a[136:139], v[152:155]\n\t"
  "v_cvt_pk_bf16_f32 v196, v196, v197\n\t"
  "v_cvt_pk_bf16_f32 v197, v198, v199\n\t"
  "v_mfma_f32_16x16x32_bf16 v[152:155], v[252:255], a[152:155], v[152:155]\n\t"
  "v_fma_f32 v200, v22, v200, v24\n\t"
  "v_fma_f32 v202, v22, v202, v26\n\t"
  "v_mfma_f32_16x16x32_bf16 v[156:159], v[248:251], a[140:143], v[156:159]\n\t"
  "v_fma_f32 v201, v22, v201, v25\n\t"
  "v_fma_f32 v203, v22, v203, v27\n\t"
  "v_mfma_f32_16x16x32_bf16 v[156:159], v[252:255], a[156:159], v[156:159]\n\t"
  "v_mul_f32_e32 v28, v200, v200\n\t"
  "v_mul_f32_e32 v14, v202, v202\n\t"
  "v_mfma_f32_16x16x32_bf16 v[32:35], v[232:235], a[160:163], v[32:35]\n\t"
  "v_mul_f32_e32 v29, v201, v201\n\t"
  "v_mul_f32_e32 v15, v203, v203\n\t"
  "v_mfma_f32_16x16x32_bf16 v[32:35], v[236:239], a[176:179], v[32:35]\n\t"
  "ds_read_b64_tr_b16 v[248:249], v16 offset:16896\n\t"
  "ds_read_b64_tr_b16 v[250:251], v16 offset:20992\n\t"
  "v_mfma_f32_16x16x32_bf16 v[36:39], v[232:235], a[164:167], v[36:39]\n\t"
  "v_fma_f32 v28, v28, s49, v31\n\t"
  "v_fma_f32 v14, v14, s49, v31\n\t"
  "v_mfma_f32_16x16x32_bf16 v[36:39], v[236:239], a[180:183], v[36:39]\n\t"
  "ds_read_b64_tr_b16 v[252:253], v16 offset:25088\n\t"
  "ds_read_b64_tr_b16 v[254:255], v16 offset:29184\n\t"
  "v_mfma_f32_16x16x32_bf16 v[40:43], v[232:235], a[168:171], v[40:43]\n\t"
  "v_fma_f32 v29, v29, s49, v31\n\t"
  "v_fma_f32 v15, v15, s49, v31\n\t"
  "v_mfma_f32_16x16x32_bf16 v[40:43], v[236:239], a[184:187], v[40:43]\n\t"
  "v_mul_f32_e32 v28, v200, v28\n\t"
  "v_mul_f32_e32 v14, v202, v14\n\t"
  "v_mfma_f32_16x16x32_bf16 v[44:47], v[232:235], a[172:175], v[44:47]\n\t"
  "v_mul_f32_e32 v29, v201, v29\n\t"
  "v_mul_f32_e32 v15, v203, v15\n\t"
  "v_mfma_f32_16x16x32_bf16 v[44:47], v[236:239], a[188:191], v[44:47]\n\t"
  "s_waitcnt lgkmcnt(2)\n\t"
  "v_exp_f32_e32 v28, v28\n\t"
  "v_mfma_f32_16x16x32_bf16 v[48:51], v[240:243], a[160:163], v[48:51]\n\t"
  "v_exp_f32_e64 v14, v14\n\t"
  "v_mfma_f32_16x16x32_bf16 v[48:51], v[244:247], a[176:179], v[48:51]\n\t"
  "ds_read_b64_tr_b16 v[232:233], v16 offset:16904\n\t"
  "ds_read_b64_tr_b16 v[234:235], v16 offset:21000\n\t"
  "v_mfma_f32_16x16x32_bf16 v[52:55], v[240:243], a[164:167], v[52:55]\n\t"
  "v_exp_f32_e64 v29, v29\n\t"
  "v_mfma_f32_16x16x32_bf16 v[52:55], v[244:247], a[180:183], v[52:55]\n\t"
  "ds_read_b64_tr_b16 v[236:237], v16 offset:25096\n\t"
  "ds_read_b64_tr_b16 v[238:239], v16 offset:29192\n\t"
  "v_mfma_f32_16x16x32_bf16 v[56:59], v[240:243], a[168:171], v[56:59]\n\t"
  "v_exp_f32_e64 v15, v15\n\t"
  "v_mfma_f32_16x16x32_bf16 v[56:59], v[244:247], a[184:187], v[56:59]\n\t"
  "v_add_f32_e32 v28, 1.0, v28\n\t"
  "v_add_f32_e32 v14, 1.0, v14\n\t"
  "v_mfma_f32_16x16x32_bf16 v[60:63], v[240:243], a[172:175], v[60:63]\n\t"
  "v_add_f32_e32 v29, 1.0, v29\n\t"
  "v_add_f32_e32 v15, 1.0, v15\n\t"
  "v_mfma_f32_16x16x32_bf16 v[60:63], v[244:247], a[188:191], v[60:63]\n\t"
  "v_rcp_f32_e64 v28, v28\n\t"
  "v_mfma_f32_16x16x32_bf16 v[64:67], v[248:251], a[160:163], v[64:67]\n\t"
  "s_waitcnt lgkmcnt(0)\n\t"
  "v_rcp_f32_e32 v14, v14\n\t"
  "v_mfma_f32_16x16x32_bf16 v[64:67], v[252:255], a[176:179], v[64:67]\n\t"
  "ds_read_b64_tr_b16 v[240:241], v16 offset:17408\n\t"
  "ds_read_b64_tr_b16 v[242:243], v16 offset:21504\n\t"
  "v_mfma_f32_16x16x32_bf16 v[68:71], v[248:251], a[164:167], v[68:71]\n\t"
  "v_rcp_f32_e64 v29, v29\n\t"
  "v_mfma_f32_16x16x32_bf16 v[68:71], v[252:255], a[180:183], v[68:71]\n\t"
  "ds_read_b64_tr_b16 v[244:245], v16 offset:25600\n\t"
  "ds_read_b64_tr_b16 v[246:247], v16 offset:29696\n\t"
  "v_mfma_f32_16x16x32_bf16 v[72:75], v[248:251], a[168:171], v[72:75]\n\t"
  "v_rcp_f32_e64 v15, v15\n\t"
  "v_mfma_f32_16x16x32_bf16 v[72:75], v[252:255], a[184:187], v[72:75]\n\t"
  "v_mul_f32_e32 v200, v200, v28\n\t"
  "v_mul_f32_e32 v202, v202, v14\n\t"
  "v_mfma_f32_16x16x32_bf16 v[76:79], v[248:251], a[172:175], v[76:79]\n\t"
  "v_mul_f32_e32 v201, v201, v29\n\t"
  "v_mul_f32_e32 v203, v203, v15\n\t"
  "v_mfma_f32_16x16x32_bf16 v[76:79], v[252:255], a[188:191], v[76:79]\n\t"
  "v_cvt_pk_bf16_f32 v200, v200, v201\n\t"
  "v_cvt_pk_bf16_f32 v201, v202, v203\n\t"
  "v_mfma_f32_16x16x32_bf16 v[80:83], v[232:235], a[160:163], v[80:83]\n\t"
  "v_fma_f32 v204, v23, v204, v24\n\t"
  "v_fma_f32 v206, v23, v206, v26\n\t"
  "v_mfma_f32_16x16x32_bf16 v[80:83], v[236:239], a[176:179], v[80:83]\n\t"
  "ds_read_b64_tr_b16 v[248:249], v16 offset:17416\n\t"
  "ds_read_b64_tr_b16 v[250:251], v16 offset:21512\n\t"
  "v_mfma_f32_16x16x32_bf16 v[84:87], v[232:235], a[164:167], v[84:87]\n\t"
  "v_fma_f32 v205, v23, v205, v25\n\t"
  "v_fma_f32 v207, v23, v207, v27\n\t"
  "v_mfma_f32_16x16x32_bf16 v[84:87], v[236:239], a[180:183], v[84:87]\n\t"
  "ds_read_b64_tr_b16 v[252:253], v16 offset:25608\n\t"
  "ds_read_b64_tr_b16 v[254:255], v16 offset:29704\n\t"
  "v_mfma_f32_16x16x32_bf16 v[88:91], v[232:235], a[168:171], v[88:91]\n\t"
  "v_mul_f32_e32 v28, v204, v204\n\t"
  "v_mul_f32_e32 v14, v206, v206\n\t"
  "v_mfma_f32_16x16x32_bf16 v[88:91], v[236:239], a[184:187], v[88:91]\n\t"
  "v_mul_f32_e32 v29, v205, v205\n\t"
  "v_mul_f32_e32 v15, v207, v207\n\t"
  "v_mfma_f32_16x16x32_bf16 v[92:95], v[232:235], a[172:175], v[92:95]\n\t"
  "v_fma_f32 v28, v28, s49, v31\n\t"
  "v_fma_f32 v14, v14, s49, v31\n\t"
  "v_mfma_f32_16x16x32_bf16 v[92:95], v[236:239], a[188:191], v[92:95]\n\t"
  "s_waitcnt lgkmcnt(2)\n\t"
  "s_nop 0\n\t"
  "v_fma_f32 v29, v29, s49, v31\n\t"
  "v_fma_f32 v15, v15, s49, v31\n\t"
  "v_mfma_f32_16x16x32_bf16 v[96:99], v[240:243], a[160:163], v[96:99]\n\t"
  "v_mul_f32_e32 v28, v204, v28\n\t"
  "v_mul_f32_e32 v14, v206, v14\n\t"
  "v_mfma_f32_16x16x32_bf16 v[96:99], v[244:247], a[176:179], v[96:99]\n\t"
  "ds_read_b64_tr_b16 v[232:233], v16 offset:17920\n\t"
  "ds_read_b64_tr_b16 v[234:235], v16 offset:22016\n\t"
  "v_mfma_f32_16x16x32_bf16 v[100:103], v[240:243], a[164:167], v[100:103]\n\t"
  "v_mul_f32_e32 v29, v205, v29\n\t"
  "v_mul_f32_e32 v15, v207, v15\n\t"
  "v_mfma_f32_16x16x32_bf16 v[100:103], v[244:247], a[180:183], v[100:103]\n\t"
  "ds_read_b64_tr_b16 v[236:237], v16 offset:26112\n\t"
  "ds_read_b64_tr_b16 v[238:239], v16 offset:30208\n\t"
  "v_mfma_f32_16x16x32_bf16 v[104:107], v[240:243], a[168:171], v[104:107]\n\t"
  "v_exp_f32_e64 v28, v28\n\t"
  "v_mfma_f32_16x16x32_bf16 v[104:107], v[244:247], a[184:187], v[104:107]\n\t"
  "v_exp_f32_e64 v14, v14\n\t"
  "v_mfma_f32_16x16x32_bf16 v[108:111], v[240:243], a[172:175], v[108:111]\n\t"
  "v_exp_f32_e64 v29, v29\n\t"
  "v_mfma_f32_16x16x32_bf16 v[108:111], v[244:247], a[188:191], v[108:111]\n\t"
  "s_waitcnt lgkmcnt(2)\n\t"
  "v_exp_f32_e32 v15, v15\n\t"
  "v_mfma_f32_16x16x32_bf16 v[112:115], v[248:251], a[160:163], v[112:115]\n\t"
  "v_add_f32_e32 v28, 1.0, v28\n\t"
  "v_add_f32_e32 v14, 1.0, v14\n\t"
  "v_mfma_f32_16x16x32_bf16 v[112:115], v[252:255], a[176:179], v[112:115]\n\t"
  "ds_read_b64_tr_b16 v[240:241], v16 offset:17928\n\t"
  "ds_read_b64_tr_b16 v[242:243], v16 offset:22024\n\t"
  "v_mfma_f32_16x16x32_bf16 v[116:119], v[248:251], a[164:167], v[116:119]\n\t"
  "v_add_f32_e32 v29, 1.0, v29\n\t"
  "v_add_f32_e32 v15, 1.0, v15\n\t"
  "v_mfma_f32_16x16x32_bf16 v[116:119], v[252:255], a[180:183], v[116:119]\n\t"
  "ds_read_b64_tr_b16 v[244:245], v16 offset:26120\n\t"
  "ds_read_b64_tr_b16 v[246:247], v16 offset:30216\n\t"
  "v_mfma_f32_16x16x32_bf16 v[120:123], v[248:251], a[168:171], v[120:123]\n\t"
  "v_rcp_f32_e64 v28, v28\n\t"
  "v_mfma_f32_16x16x32_bf16 v[120:123], v[252:255], a[184:187], v[120:123]\n\t"
  "v_rcp_f32_e64 v14, v14\n\t"
  "v_mfma_f32_16x16x32_bf16 v[124:127], v[248:251], a[172:175], v[124:127]\n\t"
  "v_rcp_f32_e64 v29, v29\n\t"
  "v_mfma_f32_16x16x32_bf16 v[124:127], v[252:255], a[188:191], v[124:127]\n\t"
  "v_rcp_f32_e64 v15, v15\n\t"
  "s_waitcnt vmcnt(8)\n\t"
  "s_barrier\n\t"
  "v_mfma_f32_16x16x32_bf16 v[128:131], v[232:235], a[160:163], v[128:131]\n\t"
  "s_waitcnt lgkmcnt(0)\n\t"
  "s_add_u32 m0, s19, 0\n\t"
  "s_nop 0\n\t"
  "s_nop 0\n\t"
  "buffer_load_dwordx4 v12, s[12:15], s20 offen offset:0 lds\n\t"
  "v_mul_f32_e64 v204, v204, v28\n\t"
  "v_mfma_f32_16x16x32_bf16 v[128:131], v[236:239], a[176:179], v[128:131]\n\t"
  "ds_read_b64_tr_b16 v[248:249], v16 offset:32768\n\t"
  "ds_read_b64_tr_b16 v[250:251], v16 offset:36864\n\t"
  "v_mfma_f32_16x16x32_bf16 v[132:135], v[232:235], a[164:167], v[132:135]\n\t"
  "buffer_load_dwordx4 v13, s[12:15], s20 offen offset:1024 lds\n\t"
  "v_mul_f32_e64 v206, v206, v14\n\t"
  "v_mfma_f32_16x16x32_bf16 v[132:135], v[236:239], a[180:183], v[132:135]\n\t"
  "ds_read_b64_tr_b16 v[252:253], v16 offset:40960\n\t"
  "ds_read_b64_tr_b16 v[254:255], v16 offset:45056\n\t"
  "v_mfma_f32_16x16x32_bf16 v[136:139], v[232:235], a[168:171], v[136:139]\n\t"
  "buffer_load_dwordx4 v12, s[12:15], s20 offen offset:2048 lds\n\t"
  "v_mul_f32_e64 v205, v205, v29\n\t"
  "v_mfma_f32_16x16x32_bf16 v[136:139], v[236:239], a[184:187], v[136:139]\n\t"
  "v_mul_f32_e64 v207, v207, v15\n\t"
  "v_cvt_pk_bf16_f32 v204, v204, v205\n\t"
  "v_mfma_f32_16x16x32_bf16 v[140:143], v[232:235], a[172:175], v[140:143]\n\t"
  "buffer_load_dwordx4 v13, s[12:15], s20 offen offset:3072 lds\n\t"
  "s_add_u32 s20, s20, 0x4000\n\t"
  "v_cvt_pk_bf16_f32 v205, v206, v207\n\t"
  "v_mfma_f32_16x16x32_bf16 v[140:143], v[236:239], a[188:191], v[140:143]\n\t"
  "v_fma_f32 v208, v20, v208, v224\n\t"
  "v_fma_f32 v210, v20, v210, v226\n\t"
  "v_mfma_f32_16x16x32_bf16 v[144:147], v[240:243], a[160:163], v[144:147]\n\t"
  "v_fma_f32 v209, v20, v209, v225\n\t"
  "v_fma_f32 v211, v20, v211, v227\n\t"
  "v_mfma_f32_16x16x32_bf16 v[144:147], v[244:247], a[176:179], v[144:147]\n\t"
  "ds_read_b64_tr_b16 v[232:233], v16 offset:32776\n\t"
  "ds_read_b64_tr_b16 v[234:235], v16 offset:36872\n\t"
  "v_mfma_f32_16x16x32_bf16 v[148:151], v[240:243], a[164:167], v[148:151]\n\t"
  "v_mul_f32_e32 v28, v208, v208\n\t"
  "v_mul_f32_e32 v14, v210, v210\n\t"
  "v_mfma_f32_16x16x32_bf16 v[148:151], v[244:247], a[180:183], v[148:151]\n\t"
  "ds_read_b64_tr_b16 v[236:237], v16 offset:40968\n\t"
  "ds_read_b64_tr_b16 v[238:239], v16 offset:45064\n\t"
  "v_mfma_f32_16x16x32_bf16 v[152:155], v[240:243], a[168:171], v[152:155]\n\t"
  "v_mul_f32_e32 v29, v209, v209\n\t"
  "v_mul_f32_e32 v15, v211, v211\n\t"
  "v_mfma_f32_16x16x32_bf16 v[152:155], v[244:247], a[184:187], v[152:155]\n\t"
  "v_fma_f32 v28, v28, s49, v31\n\t"
  "v_fma_f32 v14, v14, s49, v31\n\t"
  "v_mfma_f32_16x16x32_bf16 v[156:159], v[240:243], a[172:175], v[156:159]\n\t"
  "v_fma_f32 v29, v29, s49, v31\n\t"
  "v_fma_f32 v15, v15, s49, v31\n\t"
  "v_mfma_f32_16x16x32_bf16 v[156:159], v[244:247], a[188:191], v[156:159]\n\t"
  "s_waitcnt lgkmcnt(2)\n\t"
  "v_mul_f32_e32 v28, v208, v28\n\t"
  "v_mul_f32_e64 v14, v210, v14\n\t"
  "v_mfma_f32_16x16x32_bf16 v[32:35], v[248:251], a[192:195], v[32:35]\n\t"
  "v_mul_f32_e32 v29, v209, v29\n\t"
  "v_mul_f32_e32 v15, v211, v15\n\t"
  "v_mfma_f32_16x16x32_bf16 v[32:35], v[252:255], a[208:211], v[32:35]\n\t"
  "ds_read_b64_tr_b16 v[240:241], v16 offset:33280\n\t"
  "ds_read_b64_tr_b16 v[242:243], v16 offset:37376\n\t"
  "v_mfma_f32_16x16x32_bf16 v[36:39], v[248:251], a[196:199], v[36:39]\n\t"
  "v_exp_f32_e64 v28, v28\n\t"
  "v_mfma_f32_16x16x32_bf16 v[36:39], v[252:255], a[212:215], v[36:39]\n\t"
  "ds_read_b64_tr_b16 v[244:245], v16 offset:41472\n\t"
  "ds_read_b64_tr_b16 v[246:247], v16 offset:45568\n\t"
  "v_mfma_f32_16x16x32_bf16 v[40:43], v[248:251], a[200:203], v[40:43]\n\t"
  "v_exp_f32_e64 v14, v14\n\t"
  "v_mfma_f32_16x16x32_bf16 v[40:43], v[252:255], a[216:219], v[40:43]\n\t"
  "v_exp_f32_e64 v29, v29\n\t"
  "v_mfma_f32_16x16x32_bf16 v[44:47], v[248:251], a[204:207], v[44:47]\n\t"
  "v_exp_f32_e64 v15, v15\n\t"
  "v_mfma_f32_16x16x32_bf16 v[44:47], v[252:255], a[220:223], v[44:47]\n\t"
  "v_add_f32_e32 v28, 1.0, v28\n\t"
  "v_add_f32_e32 v14, 1.0, v14\n\t"
  "v_mfma_f32_16x16x32_bf16 v[48:51], v[232:235], a[192:195], v[48:51]\n\t"
  "s_waitcnt lgkmcnt(0)\n\t"
  "v_add_f32_e32 v29, 1.0, v29\n\t"
  "v_add_f32_e64 v15, 1.0, v15\n\t"
  "v_mfma_f32_16x16x32_bf16 v[48:51], v[236:239], a[208:211], v[48:51]\n\t"
  "ds_read_b64_tr_b16 v[248:249], v16 offset:33288\n\t"
  "ds_read_b64_tr_b16 v[250:251], v16 offset:37384\n\t"
  "v_mfma_f32_16x16x32_bf16 v[52:55], v[232:235], a[196:199], v[52:55]\n\t"
  "v_rcp_f32_e64 v28, v28\n\t"
  "v_mfma_f32_16x16x32_bf16 v[52:55], v[236:239], a[212:215], v[52:55]\n\t"
  "ds_read_b64_tr_b16 v[252:253], v16 offset:41480\n\t"
  "ds_read_b64_tr_b16 v[254:255], v16 offset:45576\n\t"
  "v_mfma_f32_16x16x32_bf16 v[56:59], v[232:235], a[200:203], v[56:59]\n\t"
  "v_rcp_f32_e64 v14, v14\n\t"
  "v_mfma_f32_16x16x32_bf16 v[56:59], v[236:239], a[216:219], v[56:59]\n\t"
  "v_rcp_f32_e64 v29, v29\n\t"
  "v_mfma_f32_16x16x32_bf16 v[60:63], v[232:235], a[204:207], v[60:63]\n\t"
  "v_rcp_f32_e64 v15, v15\n\t"
  "v_mfma_f32_16x16x32_bf16 v[60:63], v[236:239], a[220:223], v[60:63]\n\t"
  "v_mul_f32_e32 v208, v208, v28\n\t"
  "v_mul_f32_e32 v210, v210, v14\n\t"
  "v_mfma_f32_16x16x32_bf16 v[64:67], v[240:243], a[192:195], v[64:67]\n\t"
  "v_mul_f32_e32 v209, v209, v29\n\t"
  "v_mul_f32_e32 v211, v211, v15\n\t"
  "v_mfma_f32_16x16x32_bf16 v[64:67], v[244:247], a[208:211], v[64:67]\n\t"
  "ds_read_b64_tr_b16 v[232:233], v16 offset:33792\n\t"
  "ds_read_b64_tr_b16 v[234:235], v16 offset:37888\n\t"
  "v_mfma_f32_16x16x32_bf16 v[68:71], v[240:243], a[196:199], v[68:71]\n\t"
  "v_cvt_pk_bf16_f32 v194, v208, v209\n\t"
  "v_cvt_pk_bf16_f32 v195, v210, v211\n\t"
  "v_mfma_f32_16x16x32_bf16 v[68:71], v[244:247], a[212:215], v[68:71]\n\t"
  "ds_read_b64_tr_b16 v[236:237], v16 offset:41984\n\t"
  "ds_read_b64_tr_b16 v[238:239], v16 offset:46080\n\t"
  "v_mfma_f32_16x16x32_bf16 v[72:75], v[240:243], a[200:203], v[72:75]\n\t"
  "s_add_u32 s40, s46, 1024\n\t"
  "buffer_store_dwordx4 v[192:195], v1, s[32:35], s40 offen sc1\n\t"
  "v_fma_f32 v212, v21, v212, v224\n\t"
  "v_mfma_f32_16x16x32_bf16 v[72:75], v[244:247], a[216:219], v[72:75]\n\t"
  "v_fma_f32 v214, v21, v214, v226\n\t"
  "v_fma_f32 v213, v21, v213, v225\n\t"
  "v_mfma_f32_16x16x32_bf16 v[76:79], v[240:243], a[204:207], v[76:79]\n\t"
  "v_fma_f32 v215, v21, v215, v227\n\t"
  "v_mul_f32_e64 v28, v212, v212\n\t"
  "v_mfma_f32_16x16x32_bf16 v[76:79], v[244:247], a[220:223], v[76:79]\n\t"
  "s_waitcnt lgkmcnt(2)\n\t"
  "v_mul_f32_e32 v14, v214, v214\n\t"
  "v_mul_f32_e64 v29, v213, v213\n\t"
  "v_mfma_f32_16x16x32_bf16 v[80:83], v[248:251], a[192:195], v[80:83]\n\t"
  "v_mul_f32_e64 v15, v215, v215\n\t"
  "v_fma_f32 v28, v28, s49, v31\n\t"
  "v_mfma_f32_16x16x32_bf16 v[80:83], v[252:255], a[208:211], v[80:83]\n\t"
  "ds_read_b64_tr_b16 v[240:241], v16 offset:33800\n\t"
  "ds_read_b64_tr_b16 v[242:243], v16 offset:37896\n\t"
  "v_mfma_f32_16x16x32_bf16 v[84:87], v[248:251], a[196:199], v[84:87]\n\t"
  "v_fma_f32 v14, v14, s49, v31\n\t"
  "v_fma_f32 v29, v29, s49, v31\n\t"
  "v_mfma_f32_16x16x32_bf16 v[84:87], v[252:255], a[212:215], v[84:87]\n\t"
  "ds_read_b64_tr_b16 v[244:245], v16 offset:41992\n\t"
  "ds_read_b64_tr_b16 v[246:247], v16 offset:46088\n\t"
  "v_mfma_f32_16x16x32_bf16 v[88:91], v[248:251], a[200:203], v[88:91]\n\t"
  "v_fma_f32 v15, v15, s49, v31\n\t"
  "v_mul_f32_e64 v28, v212, v28\n\t"
  "v_mfma_f32_16x16x32_bf16 v[88:91], v[252:255], a[216:219], v[88:91]\n\t"
  "v_mul_f32_e32 v14, v214, v14\n\t"
  "v_mul_f32_e32 v29, v213, v29\n\t"
  "v_mfma_f32_16x16x32_bf16 v[92:95], v[248:251], a[204:207], v[92:95]\n\t"
  "v_mul_f32_e64 v15, v215, v15\n\t"
  "v_mfma_f32_16x16x32_bf16 v[92:95], v[252:255], a[220:223], v[92:95]\n\t"
  "v_exp_f32_e64 v28, v28\n\t"
  "v_mfma_f32_16x16x32_bf16 v[96:99], v[232:235], a[192:195], v[96:99]\n\t"
  "s_waitcnt lgkmcnt(0)\n\t"
  "v_exp_f32_e32 v14, v14\n\t"
  "v_mfma_f32_16x16x32_bf16 v[96:99], v[236:239], a[208:211], v[96:99]\n\t"
  "ds_read_b64_tr_b16 v[248:249], v16 offset:34304\n\t"
  "ds_read_b64_tr_b16 v[250:251], v16 offset:38400\n\t"
  "v_mfma_f32_16x16x32_bf16 v[100:103], v[232:235], a[196:199], v[100:103]\n\t"
  "v_exp_f32_e64 v29, v29\n\t"
  "v_mfma_f32_16x16x32_bf16 v[100:103], v[236:239], a[212:215], v[100:103]\n\t"
  "ds_read_b64_tr_b16 v[252:253], v16 offset:42496\n\t"
  "ds_read_b64_tr_b16 v[254:255], v16 offset:46592\n\t"
  "v_mfma_f32_16x16x32_bf16 v[104:107], v[232:235], a[200:203], v[104:107]\n\t"
  "v_exp_f32_e64 v15, v15\n\t"
  "v_mfma_f32_16x16x32_bf16 v[104:107], v[236:239], a[216:219], v[104:107]\n\t"
  "v_add_f32_e32 v28, 1.0, v28\n\t"
  "v_add_f32_e32 v14, 1.0, v14\n\t"
  "v_mfma_f32_16x16x32_bf16 v[108:111], v[232:235], a[204:207], v[108:111]\n\t"
  "v_add_f32_e32 v29, 1.0, v29\n\t"
  "v_add_f32_e32 v15, 1.0, v15\n\t"
  "v_mfma_f32_16x16x32_bf16 v[108:111], v[236:239], a[220:223], v[108:111]\n\t"
  "v_rcp_f32_e64 v28, v28\n\t"
  "v_mfma_f32_16x16x32_bf16 v[112:115], v[240:243], a[192:195], v[112:115]\n\t"
  "v_rcp_f32_e64 v14, v14\n\t"
  "v_mfma_f32_16x16x32_bf16 v[112:115], v[244:247], a[208:211], v[112:115]\n\t"
  "ds_read_b64_tr_b16 v[232:233], v16 offset:34312\n\t"
  "ds_read_b64_tr_b16 v[234:235], v16 offset:38408\n\t"
  "v_mfma_f32_16x16x32_bf16 v[116:119], v[240:243], a[196:199], v[116:119]\n\t"
  "v_rcp_f32_e64 v29, v29\n\t"
  "v_mfma_f32_16x16x32_bf16 v[116:119], v[244:247], a[212:215], v[116:119]\n\t"
  "ds_read_b64_tr_b16 v[236:237], v16 offset:42504\n\t"
  "ds_read_b64_tr_b16 v[238:239], v16 offset:46600\n\t"
  "v_mfma_f32_16x16x32_bf16 v[120:123], v[240:243], a[200:203], v[120:123]\n\t"
  "v_rcp_f32_e64 v15, v15\n\t"
  "v_mfma_f32_16x16x32_bf16 v[120:123], v[244:247], a[216:219], v[120:123]\n\t"
  "v_mul_f32_e32 v212, v212, v28\n\t"
  "v_mul_f32_e32 v214, v214, v14\n\t"
  "v_mfma_f32_16x16x32_bf16 v[124:127], v[240:243], a[204:207], v[124:127]\n\t"
  "v_mul_f32_e32 v213, v213, v29\n\t"
  "v_mul_f32_e32 v215, v215, v15\n\t"
  "v_mfma_f32_16x16x32_bf16 v[124:127], v[244:247], a[220:223], v[124:127]\n\t"
  "v_cvt_pk_bf16_f32 v198, v212, v213\n\t"
  "v_cvt_pk_bf16_f32 v199, v214, v215\n\t"
  "s_waitcnt vmcnt(8)\n\t"
  "s_barrier\n\t"
  "s_waitcnt lgkmcnt(2)\n\t"
  "s_nop 0\n\t"
  "v_mfma_f32_16x16x32_bf16 v[128:131], v[248:251], a[192:195], v[128:131]\n\t"
  "s_add_u32 m0, s18, 49152\n\t"
  "s_nop 0\n\t"
  "s_nop 0\n\t"
  "buffer_load_dwordx4 v8, s[8:11], s16 offen offset:0 lds\n\t"
  "s_add_u32 s40, s46, 189440\n\t"
  "buffer_store_dwordx4 v[196:199], v1, s[32:35], s40 offen sc1\n\t"
  "v_mfma_f32_16x16x32_bf16 v[128:131], v[252:255], a[208:211], v[128:131]\n\t"
  "ds_read_b64_tr_b16 v[240:241], v16 offset:49152\n\t"
  "ds_read_b64_tr_b16 v[242:243], v16 offset:53248\n\t"
  "v_mfma_f32_16x16x32_bf16 v[132:135], v[248:251], a[196:199], v[132:135]\n\t"
  "buffer_load_dwordx4 v9, s[8:11], s16 offen offset:1024 lds\n\t"
  "v_fma_f32 v216, v22, v216, v224\n\t"
  "v_mfma_f32_16x16x32_bf16 v[132:135], v[252:255], a[212:215], v[132:135]\n\t"
  "ds_read_b64_tr_b16 v[244:245], v16 offset:57344\n\t"
  "ds_read_b64_tr_b16 v[246:247], v16 offset:61440\n\t"
  "v_mfma_f32_16x16x32_bf16 v[136:139], v[248:251], a[200:203], v[136:139]\n\t"
  "buffer_load_dwordx4 v10, s[8:11], s16 offen offset:2048 lds\n\t"
  "v_fma_f32 v218, v22, v218, v226\n\t"
  "v_mfma_f32_16x16x32_bf16 v[136:139], v[252:255], a[216:219], v[136:139]\n\t"
  "v_fma_f32 v217, v22, v217, v225\n\t"
  "v_fma_f32 v219, v22, v219, v227\n\t"
  "v_mfma_f32_16x16x32_bf16 v[140:143], v[248:251], a[204:207], v[140:143]\n\t"
  "buffer_load_dwordx4 v11, s[8:11], s16 offen offset:3072 lds\n\t"
  "v_mul_f32_e64 v28, v216, v216\n\t"
  "v_mfma_f32_16x16x32_bf16 v[140:143], v[252:255], a[220:223], v[140:143]\n\t"
  "v_mul_f32_e32 v14, v218, v218\n\t"
  "v_mul_f32_e32 v29, v217, v217\n\t"
  "v_mfma_f32_16x16x32_bf16 v[144:147], v[232:235], a[192:195], v[144:147]\n\t"
  "s_waitcnt lgkmcnt(0)\n\t"
  "s_add_u32 s16, s16, 0x4000\n\t"
  "s_add_u32 m0, s19, 16384\n\t"
  "s_nop 0\n\t"
  "buffer_load_dwordx4 v12, s[12:15], s20 offen offset:0 lds\n\t"
  "v_mul_f32_e64 v15, v219, v219\n\t"
  "v_mfma_f32_16x16x32_bf16 v[144:147], v[236:239], a[208:211], v[144:147]\n\t"
  "ds_read_b64_tr_b16 v[248:249], v16 offset:49160\n\t"
  "ds_read_b64_tr_b16 v[250:251], v16 offset:53256\n\t"
  "v_mfma_f32_16x16x32_bf16 v[148:151], v[232:235], a[196:199], v[148:151]\n\t"
  "buffer_load_dwordx4 v13, s[12:15], s20 offen offset:1024 lds\n\t"
  "v_fma_f32 v28, v28, s49, v31\n\t"
  "v_mfma_f32_16x16x32_bf16 v[148:151], v[236:239], a[212:215], v[148:151]\n\t"
  "ds_read_b64_tr_b16 v[252:253], v16 offset:57352\n\t"
  "ds_read_b64_tr_b16 v[254:255], v16 offset:61448\n\t"
  "v_mfma_f32_16x16x32_bf16 v[152:155], v[232:235], a[200:203], v[152:155]\n\t"
  "buffer_load_dwordx4 v12, s[12:15], s20 offen offset:2048 lds\n\t"
  "v_fma_f32 v14, v14, s49, v31\n\t"
  "v_mfma_f32_16x16x32_bf16 v[152:155], v[236:239], a[216:219], v[152:155]\n\t"
  "v_fma_f32 v29, v29, s49, v31\n\t"
  "v_fma_f32 v15, v15, s49, v31\n\t"
  "v_mfma_f32_16x16x32_bf16 v[156:159], v[232:235], a[204:207], v[156:159]\n\t"
  "buffer_load_dwordx4 v13, s[12:15], s20 offen offset:3072 lds\n\t"
  "s_add_u32 s20, s20, 0x4000\n\t"
  "v_mul_f32_e64 v28, v216, v28\n\t"
  "v_mfma_f32_16x16x32_bf16 v[156:159], v[236:239], a[220:223], v[156:159]\n\t"
  "v_mul_f32_e32 v14, v218, v14\n\t"
  "v_mul_f32_e32 v29, v217, v29\n\t"
  "v_mfma_f32_16x16x32_bf16 v[32:35], v[240:243], a[224:227], v[32:35]\n\t"
  "v_mul_f32_e64 v15, v219, v15\n\t"
  "v_mfma_f32_16x16x32_bf16 v[32:35], v[244:247], a[240:243], v[32:35]\n\t"
  "ds_read_b64_tr_b16 v[232:233], v16 offset:49664\n\t"
  "ds_read_b64_tr_b16 v[234:235], v16 offset:53760\n\t"
  "v_mfma_f32_16x16x32_bf16 v[36:39], v[240:243], a[228:231], v[36:39]\n\t"
  "v_exp_f32_e64 v28, v28\n\t"
  "v_mfma_f32_16x16x32_bf16 v[36:39], v[244:247], a[244:247], v[36:39]\n\t"
  "ds_read_b64_tr_b16 v[236:237], v16 offset:57856\n\t"
  "ds_read_b64_tr_b16 v[238:239], v16 offset:61952\n\t"
  "v_mfma_f32_16x16x32_bf16 v[40:43], v[240:243], a[232:235], v[40:43]\n\t"
  "v_exp_f32_e64 v14, v14\n\t"
  "v_mfma_f32_16x16x32_bf16 v[40:43], v[244:247], a[248:251], v[40:43]\n\t"
  "v_exp_f32_e64 v29, v29\n\t"
  "v_mfma_f32_16x16x32_bf16 v[44:47], v[240:243], a[236:239], v[44:47]\n\t"
  "s_waitcnt lgkmcnt(2)\n\t"
  "v_exp_f32_e32 v15, v15\n\t"
  "v_mfma_f32_16x16x32_bf16 v[44:47], v[244:247], a[252:255], v[44:47]\n\t"
  "v_add_f32_e32 v28, 1.0, v28\n\t"
  "v_add_f32_e32 v14, 1.0, v14\n\t"
  "v_mfma_f32_16x16x32_bf16 v[48:51], v[248:251], a[224:227], v[48:51]\n\t"
  "v_add_f32_e32 v29, 1.0, v29\n\t"
  "v_add_f32_e32 v15, 1.0, v15\n\t"
  "v_mfma_f32_16x16x32_bf16 v[48:51], v[252:255], a[240:243], v[48:51]\n\t"
  "ds_read_b64_tr_b16 v[240:241], v16 offset:49672\n\t"
  "ds_read_b64_tr_b16 v[242:243], v16 offset:53768\n\t"
  "v_mfma_f32_16x16x32_bf16 v[52:55], v[248:251], a[228:231], v[52:55]\n\t"
  "v_rcp_f32_e64 v28, v28\n\t"
  "v_mfma_f32_16x16x32_bf16 v[52:55], v[252:255], a[244:247], v[52:55]\n\t"
  "ds_read_b64_tr_b16 v[244:245], v16 offset:57864\n\t"
  "ds_read_b64_tr_b16 v[246:247], v16 offset:61960\n\t"
  "v_mfma_f32_16x16x32_bf16 v[56:59], v[248:251], a[232:235], v[56:59]\n\t"
  "v_rcp_f32_e64 v14, v14\n\t"
  "v_mfma_f32_16x16x32_bf16 v[56:59], v[252:255], a[248:251], v[56:59]\n\t"
  "v_rcp_f32_e64 v29, v29\n\t"
  "v_mfma_f32_16x16x32_bf16 v[60:63], v[248:251], a[236:239], v[60:63]\n\t"
  "v_rcp_f32_e64 v15, v15\n\t"
  "v_mfma_f32_16x16x32_bf16 v[60:63], v[252:255], a[252:255], v[60:63]\n\t"
  "v_mul_f32_e32 v216, v216, v28\n\t"
  "v_mul_f32_e32 v218, v218, v14\n\t"
  "v_mfma_f32_16x16x32_bf16 v[64:67], v[232:235], a[224:227], v[64:67]\n\t"
  "s_waitcnt lgkmcnt(0)\n\t"
  "v_mul_f32_e32 v217, v217, v29\n\t"
  "v_mul_f32_e64 v219, v219, v15\n\t"
  "v_mfma_f32_16x16x32_bf16 v[64:67], v[236:239], a[240:243], v[64:67]\n\t"
  "ds_read_b64_tr_b16 v[248:249], v16 offset:50176\n\t"
  "ds_read_b64_tr_b16 v[250:251], v16 offset:54272\n\t"
  "v_mfma_f32_16x16x32_bf16 v[68:71], v[232:235], a[228:231], v[68:71]\n\t"
  "v_cvt_pk_bf16_f32 v202, v216, v217\n\t"
  "v_cvt_pk_bf16_f32 v203, v218, v219\n\t"
  "v_mfma_f32_16x16x32_bf16 v[68:71], v[236:239], a[244:247], v[68:71]\n\t"
  "ds_read_b64_tr_b16 v[252:253], v16 offset:58368\n\t"
  "ds_read_b64_tr_b16 v[254:255], v16 offset:62464\n\t"
  "v_mfma_f32_16x16x32_bf16 v[72:75], v[232:235], a[232:235], v[72:75]\n\t"
  "s_add_u32 s40, s46, 377856\n\t"
  "buffer_store_dwordx4 v[200:203], v1, s[32:35], s40 offen sc1\n\t"
  "v_fma_f32 v220, v23, v220, v224\n\t"
  "v_mfma_f32_16x16x32_bf16 v[72:75], v[236:239], a[248:251], v[72:75]\n\t"
  "v_fma_f32 v222, v23, v222, v226\n\t"
  "v_fma_f32 v221, v23, v221, v225\n\t"
  "v_mfma_f32_16x16x32_bf16 v[76:79], v[232:235], a[236:239], v[76:79]\n\t"
  "v_fma_f32 v223, v23, v223, v227\n\t"
  "v_mul_f32_e64 v28, v220, v220\n\t"
  "v_mfma_f32_16x16x32_bf16 v[76:79], v[236:239], a[252:255], v[76:79]\n\t"
  "v_mul_f32_e32 v14, v222, v222\n\t"
  "v_mul_f32_e32 v29, v221, v221\n\t"
  "v_mfma_f32_16x16x32_bf16 v[80:83], v[240:243], a[224:227], v[80:83]\n\t"
  "v_mul_f32_e64 v15, v223, v223\n\t"
  "v_fma_f32 v28, v28, s49, v31\n\t"
  "v_mfma_f32_16x16x32_bf16 v[80:83], v[244:247], a[240:243], v[80:83]\n\t"
  "ds_read_b64_tr_b16 v[232:233], v16 offset:50184\n\t"
  "ds_read_b64_tr_b16 v[234:235], v16 offset:54280\n\t"
  "v_mfma_f32_16x16x32_bf16 v[84:87], v[240:243], a[228:231], v[84:87]\n\t"
  "v_fma_f32 v14, v14, s49, v31\n\t"
  "v_fma_f32 v29, v29, s49, v31\n\t"
  "v_mfma_f32_16x16x32_bf16 v[84:87], v[244:247], a[244:247], v[84:87]\n\t"
  "ds_read_b64_tr_b16 v[236:237], v16 offset:58376\n\t"
  "ds_read_b64_tr_b16 v[238:239], v16 offset:62472\n\t"
  "v_mfma_f32_16x16x32_bf16 v[88:91], v[240:243], a[232:235], v[88:91]\n\t"
  "v_fma_f32 v15, v15, s49, v31\n\t"
  "v_mul_f32_e64 v28, v220, v28\n\t"
  "v_mfma_f32_16x16x32_bf16 v[88:91], v[244:247], a[248:251], v[88:91]\n\t"
  "v_mul_f32_e32 v14, v222, v14\n\t"
  "v_mul_f32_e32 v29, v221, v29\n\t"
  "v_mfma_f32_16x16x32_bf16 v[92:95], v[240:243], a[236:239], v[92:95]\n\t"
  "v_mul_f32_e64 v15, v223, v15\n\t"
  "v_mfma_f32_16x16x32_bf16 v[92:95], v[244:247], a[252:255], v[92:95]\n\t"
  "s_waitcnt lgkmcnt(2)\n\t"
  "v_exp_f32_e32 v28, v28\n\t"
  "v_mfma_f32_16x16x32_bf16 v[96:99], v[248:251], a[224:227], v[96:99]\n\t"
  "v_exp_f32_e64 v14, v14\n\t"
  "v_mfma_f32_16x16x32_bf16 v[96:99], v[252:255], a[240:243], v[96:99]\n\t"
  "ds_read_b64_tr_b16 v[240:241], v16 offset:50688\n\t"
  "ds_read_b64_tr_b16 v[242:243], v16 offset:54784\n\t"
  "v_mfma_f32_16x16x32_bf16 v[100:103], v[248:251], a[228:231], v[100:103]\n\t"
  "v_exp_f32_e64 v29, v29\n\t"
  "v_mfma_f32_16x16x32_bf16 v[100:103], v[252:255], a[244:247], v[100:103]\n\t"
  "ds_read_b64_tr_b16 v[244:245], v16 offset:58880\n\t"
  "ds_read_b64_tr_b16 v[246:247], v16 offset:62976\n\t"
  "v_mfma_f32_16x16x32_bf16 v[104:107], v[248:251], a[232:235], v[104:107]\n\t"
  "v_exp_f32_e64 v15, v15\n\t"
  "v_mfma_f32_16x16x32_bf16 v[104:107], v[252:255], a[248:251], v[104:107]\n\t"
  "v_add_f32_e32 v28, 1.0, v28\n\t"
  "v_add_f32_e32 v14, 1.0, v14\n\t"
  "v_mfma_f32_16x16x32_bf16 v[108:111], v[248:251], a[236:239], v[108:111]\n\t"
  "v_add_f32_e32 v29, 1.0, v29\n\t"
  "v_add_f32_e32 v15, 1.0, v15\n\t"
  "v_mfma_f32_16x16x32_bf16 v[108:111], v[252:255], a[252:255], v[108:111]\n\t"
  "v_rcp_f32_e64 v28, v28\n\t"
  "v_mfma_f32_16x16x32_bf16 v[112:115], v[232:235], a[224:227], v[112:115]\n\t"
  "s_waitcnt lgkmcnt(0)\n\t"
  "v_rcp_f32_e32 v14, v14\n\t"
  "v_mfma_f32_16x16x32_bf16 v[112:115], v[236:239], a[240:243], v[112:115]\n\t"
  "ds_read_b64_tr_b16 v[248:249], v16 offset:50696\n\t"
  "ds_read_b64_tr_b16 v[250:251], v16 offset:54792\n\t"
  "v_mfma_f32_16x16x32_bf16 v[116:119], v[232:235], a[228:231], v[116:119]\n\t"
  "v_rcp_f32_e64 v29, v29\n\t"
  "v_mfma_f32_16x16x32_bf16 v[116:119], v[236:239], a[244:247], v[116:119]\n\t"
  "ds_read_b64_tr_b16 v[252:253], v16 offset:58888\n\t"
  "ds_read_b64_tr_b16 v[254:255], v16 offset:62984\n\t"
  "v_mfma_f32_16x16x32_bf16 v[120:123], v[232:235], a[232:235], v[120:123]\n\t"
  "v_rcp_f32_e64 v15, v15\n\t"
  "v_mfma_f32_16x16x32_bf16 v[120:123], v[236:239], a[248:251], v[120:123]\n\t"
  "v_mul_f32_e32 v220, v220, v28\n\t"
  "v_mul_f32_e32 v222, v222, v14\n\t"
  "v_mfma_f32_16x16x32_bf16 v[124:127], v[232:235], a[236:239], v[124:127]\n\t"
  "v_mul_f32_e32 v221, v221, v29\n\t"
  "v_mul_f32_e32 v223, v223, v15\n\t"
  "v_mfma_f32_16x16x32_bf16 v[124:127], v[236:239], a[252:255], v[124:127]\n\t"
  "v_cvt_pk_bf16_f32 v206, v220, v221\n\t"
  "v_cvt_pk_bf16_f32 v207, v222, v223\n\t"
  "v_mfma_f32_16x16x32_bf16 v[128:131], v[240:243], a[224:227], v[128:131]\n\t"
  "s_add_u32 s40, s46, 566272\n\t"
  "buffer_store_dwordx4 v[204:207], v1, s[32:35], s40 offen sc1\n\t"
  "v_mfma_f32_16x16x32_bf16 v[128:131], v[244:247], a[240:243], v[128:131]\n\t"
  "v_mfma_f32_16x16x32_bf16 v[132:135], v[240:243], a[228:231], v[132:135]\n\t"
  "v_cvt_pk_bf16_f32 v160, v32, v33\n\t"
  "v_cvt_pk_bf16_f32 v161, v34, v35\n\t"
  "v_mfma_f32_16x16x32_bf16 v[132:135], v[244:247], a[244:247], v[132:135]\n\t"
  "v_cvt_pk_bf16_f32 v176, v36, v37\n\t"
  "v_cvt_pk_bf16_f32 v177, v38, v39\n\t"
  "v_mfma_f32_16x16x32_bf16 v[136:139], v[240:243], a[232:235], v[136:139]\n\t"
  "v_cvt_pk_bf16_f32 v192, v40, v41\n\t"
  "v_cvt_pk_bf16_f32 v193, v42, v43\n\t"
  "v_mfma_f32_16x16x32_bf16 v[136:139], v[244:247], a[248:251], v[136:139]\n\t"
  "v_cvt_pk_bf16_f32 v208, v44, v45\n\t"
  "v_cvt_pk_bf16_f32 v209, v46, v47\n\t"
  "v_mfma_f32_16x16x32_bf16 v[140:143], v[240:243], a[236:239], v[140:143]\n\t"
  "v_cvt_pk_bf16_f32 v162, v48, v49\n\t"
  "v_cvt_pk_bf16_f32 v163, v50, v51\n\t"
  "v_mfma_f32_16x16x32_bf16 v[140:143], v[244:247], a[252:255], v[140:143]\n\t"
  "s_waitcnt lgkmcnt(0)\n\t"
  "s_nop 0\n\t"
  "v_cvt_pk_bf16_f32 v178, v52, v53\n\t"
  "v_cvt_pk_bf16_f32 v179, v54, v55\n\t"
  "v_mfma_f32_16x16x32_bf16 v[144:147], v[248:251], a[224:227], v[144:147]\n\t"
  "v_cvt_pk_bf16_f32 v194, v56, v57\n\t"
  "v_cvt_pk_bf16_f32 v195, v58, v59\n\t"
  "v_mfma_f32_16x16x32_bf16 v[144:147], v[252:255], a[240:243], v[144:147]\n\t"
  "v_cvt_pk_bf16_f32 v210, v60, v61\n\t"
  "v_cvt_pk_bf16_f32 v211, v62, v63\n\t"
  "v_mfma_f32_16x16x32_bf16 v[148:151], v[248:251], a[228:231], v[148:151]\n\t"
  "v_cvt_pk_bf16_f32 v164, v64, v65\n\t"
  "v_cvt_pk_bf16_f32 v165, v66, v67\n\t"
  "v_mfma_f32_16x16x32_bf16 v[148:151], v[252:255], a[244:247], v[148:151]\n\t"
  "v_cvt_pk_bf16_f32 v180, v68, v69\n\t"
  "v_cvt_pk_bf16_f32 v181, v70, v71\n\t"
  "v_mfma_f32_16x16x32_bf16 v[152:155], v[248:251], a[232:235], v[152:155]\n\t"
  "v_cvt_pk_bf16_f32 v196, v72, v73\n\t"
  "v_cvt_pk_bf16_f32 v197, v74, v75\n\t"
  "v_mfma_f32_16x16x32_bf16 v[152:155], v[252:255], a[248:251], v[152:155]\n\t"
  "v_cvt_pk_bf16_f32 v212, v76, v77\n\t"
  "v_cvt_pk_bf16_f32 v213, v78, v79\n\t"
  "v_mfma_f32_16x16x32_bf16 v[156:159], v[248:251], a[236:239], v[156:159]\n\t"
  "v_cvt_pk_bf16_f32 v166, v80, v81\n\t"
  "v_cvt_pk_bf16_f32 v167, v82, v83\n\t"
  "v_mfma_f32_16x16x32_bf16 v[156:159], v[252:255], a[252:255], v[156:159]\n\t"
  "v_cvt_pk_bf16_f32 v182, v84, v85\n\t"
  "v_cvt_pk_bf16_f32 v183, v86, v87\n\t"
  "s_waitcnt vmcnt(37)\n\t"
  "s_barrier\n\t"
  "v_mov_b32_e32 v28, 0x3f803f80\n\t"
  "ds_read_b128 a[128:131], v2 offset:0\n\t"
  "ds_read_b128 a[132:135], v3 offset:0\n\t"
  "ds_read_b128 a[136:139], v4 offset:0\n\t"
  "ds_read_b128 a[140:143], v5 offset:0\n\t"
  "v_cvt_pk_bf16_f32 v198, v88, v89\n\t"
  "v_cvt_pk_bf16_f32 v199, v90, v91\n\t"
  "v_cvt_pk_bf16_f32 v214, v92, v93\n\t"
  "v_cvt_pk_bf16_f32 v215, v94, v95\n\t"
  "v_cvt_pk_bf16_f32 v168, v96, v97\n\t"
  "v_cvt_pk_bf16_f32 v169, v98, v99\n\t"
  "v_cvt_pk_bf16_f32 v184, v100, v101\n\t"
  "v_cvt_pk_bf16_f32 v185, v102, v103\n\t"
  "v_cvt_pk_bf16_f32 v200, v104, v105\n\t"
  "v_cvt_pk_bf16_f32 v201, v106, v107\n\t"
  "v_cvt_pk_bf16_f32 v216, v108, v109\n\t"
  "v_cvt_pk_bf16_f32 v217, v110, v111\n\t"
  "v_cvt_pk_bf16_f32 v170, v112, v113\n\t"
  "v_cvt_pk_bf16_f32 v171, v114, v115\n\t"
  "v_cvt_pk_bf16_f32 v186, v116, v117\n\t"
  "v_cvt_pk_bf16_f32 v187, v118, v119\n\t"
  "v_cvt_pk_bf16_f32 v202, v120, v121\n\t"
  "v_cvt_pk_bf16_f32 v203, v122, v123\n\t"
  "v_cvt_pk_bf16_f32 v218, v124, v125\n\t"
  "v_cvt_pk_bf16_f32 v219, v126, v127\n\t"
  "v_cvt_pk_bf16_f32 v172, v128, v129\n\t"
  "v_cvt_pk_bf16_f32 v173, v130, v131\n\t"
  "v_cvt_pk_bf16_f32 v188, v132, v133\n\t"
  "v_cvt_pk_bf16_f32 v189, v134, v135\n\t"
  "v_cvt_pk_bf16_f32 v204, v136, v137\n\t"
  "v_cvt_pk_bf16_f32 v205, v138, v139\n\t"
  "v_cvt_pk_bf16_f32 v220, v140, v141\n\t"
  "v_cvt_pk_bf16_f32 v221, v142, v143\n\t"
  "v_cvt_pk_bf16_f32 v174, v144, v145\n\t"
  "v_cvt_pk_bf16_f32 v175, v146, v147\n\t"
  "v_cvt_pk_bf16_f32 v190, v148, v149\n\t"
  "v_cvt_pk_bf16_f32 v191, v150, v151\n\t"
  "v_cvt_pk_bf16_f32 v206, v152, v153\n\t"
  "v_cvt_pk_bf16_f32 v207, v154, v155\n\t"
  "v_cvt_pk_bf16_f32 v222, v156, v157\n\t"
  "v_cvt_pk_bf16_f32 v223, v158, v159\n\t"
  "s_waitcnt lgkmcnt(3)\n\t"
  "s_nop 0\n\t"
  "v_mfma_f32_16x16x32_bf16 v[32:35], a[128:131], v[160:163], 0\n\t"
  "s_waitcnt lgkmcnt(2)\n\t"
  "s_nop 0\n\t"
  "v_accvgpr_write_b32 a208, v28\n\t"
  "v_accvgpr_write_b32 a209, v28\n\t"
  "v_mfma_f32_16x16x32_bf16 v[32:35], a[132:135], v[164:167], v[32:35]\n\t"
  "s_waitcnt lgkmcnt(1)\n\t"
  "s_nop 0\n\t"
  "ds_read_b128 a[144:147], v2 offset:4096\n\t"
  "v_accvgpr_write_b32 a210, v28\n\t"
  "v_mfma_f32_16x16x32_bf16 v[32:35], a[136:139], v[168:171], v[32:35]\n\t"
  "s_waitcnt lgkmcnt(1)\n\t"
  "s_nop 0\n\t"
  "v_accvgpr_write_b32 a211, v28\n\t"
  "v_mfma_f32_16x16x32_bf16 v[32:35], a[140:143], v[172:175], v[32:35]\n\t"
  "ds_read_b128 a[148:151], v3 offset:4096\n\t"
  "v_mfma_f32_16x16x32_bf16 v[36:39], a[128:131], v[176:179], 0\n\t"
  "v_mfma_f32_16x16x32_bf16 v[36:39], a[132:135], v[180:183], v[36:39]\n\t"
  "ds_read_b128 a[152:155], v4 offset:4096\n\t"
  "v_mfma_f32_16x16x32_bf16 v[36:39], a[136:139], v[184:187], v[36:39]\n\t"
  "v_mfma_f32_16x16x32_bf16 v[36:39], a[140:143], v[188:191], v[36:39]\n\t"
  "ds_read_b128 a[156:159], v5 offset:4096\n\t"
  "v_mfma_f32_16x16x32_bf16 v[40:43], a[128:131], v[192:195], 0\n\t"
  "v_mfma_f32_16x16x32_bf16 v[40:43], a[132:135], v[196:199], v[40:43]\n\t"
  "v_mfma_f32_16x16x32_bf16 v[40:43], a[136:139], v[200:203], v[40:43]\n\t"
  "v_mfma_f32_16x16x32_bf16 v[40:43], a[140:143], v[204:207], v[40:43]\n\t"
  "v_mfma_f32_16x16x32_bf16 v[44:47], a[128:131], v[208:211], 0\n\t"
  "v_mfma_f32_16x16x32_bf16 v[44:47], a[132:135], v[212:215], v[44:47]\n\t"
  "v_mfma_f32_16x16x32_bf16 v[44:47], a[136:139], v[216:219], v[44:47]\n\t"
  "v_mfma_f32_16x16x32_bf16 v[44:47], a[140:143], v[220:223], v[44:47]\n\t"
  "s_waitcnt lgkmcnt(0)\n\t"
  "s_nop 0\n\t"
  "v_max3_f32 v224, v32, v33, v34\n\t"
  "v_max_f32_e64 v224, v224, v35\n\t"
  "v_max3_f32 v228, v36, v37, v38\n\t"
  "v_max_f32_e64 v228, v228, v39\n\t"
  "v_max3_f32 v232, v40, v41, v42\n\t"
  "v_max_f32_e32 v232, v232, v43\n\t"
  "s_nop 0\n\t"
  "v_max3_f32 v236, v44, v45, v46\n\t"
  "v_max_f32_e32 v236, v236, v47\n\t"
  "v_mov_b32_e32 v225, v224\n\t"
  "v_mov_b32_e32 v229, v228\n\t"
  "v_mov_b32_e32 v233, v232\n\t"
  "v_mov_b32_e32 v237, v236\n\t"
  "v_permlane16_swap_b32_e32 v224, v225\n\t"
  "v_permlane16_swap_b32_e32 v228, v229\n\t"
  "v_permlane16_swap_b32_e32 v232, v233\n\t"
  "v_permlane16_swap_b32_e32 v236, v237\n\t"
  "v_max_f32_e32 v224, v224, v225\n\t"
  "v_max_f32_e32 v228, v228, v229\n\t"
  "v_max_f32_e32 v232, v232, v233\n\t"
  "v_max_f32_e32 v236, v236, v237\n\t"
  "v_mov_b32_e32 v225, v224\n\t"
  "v_mov_b32_e32 v229, v228\n\t"
  "v_mov_b32_e32 v233, v232\n\t"
  "v_mov_b32_e32 v237, v236\n\t"
  "v_permlane32_swap_b32_e32 v224, v225\n\t"
  "v_permlane32_swap_b32_e32 v228, v229\n\t"
  "v_permlane32_swap_b32_e32 v232, v233\n\t"
  "v_permlane32_swap_b32_e32 v236, v237\n\t"
  "v_max_f32_e32 v225, v224, v225\n\t"
  "v_max_f32_e32 v229, v228, v229\n\t"
  "v_max_f32_e32 v233, v232, v233\n\t"
  "v_max_f32_e32 v237, v236, v237\n\t"
  "v_sub_f32_e32 v32, v32, v225\n\t"
  "v_sub_f32_e32 v33, v33, v225\n\t"
  "v_sub_f32_e32 v34, v34, v225\n\t"
  "v_sub_f32_e32 v35, v35, v225\n\t"
  "v_sub_f32_e32 v36, v36, v229\n\t"
  "v_sub_f32_e32 v37, v37, v229\n\t"
  "v_sub_f32_e32 v38, v38, v229\n\t"
  "v_sub_f32_e32 v39, v39, v229\n\t"
  "v_sub_f32_e32 v40, v40, v233\n\t"
  "v_sub_f32_e32 v41, v41, v233\n\t"
  "v_sub_f32_e32 v42, v42, v233\n\t"
  "v_sub_f32_e32 v43, v43, v233\n\t"
  "v_sub_f32_e32 v44, v44, v237\n\t"
  "v_sub_f32_e32 v45, v45, v237\n\t"
  "v_sub_f32_e32 v46, v46, v237\n\t"
  "v_sub_f32_e32 v47, v47, v237\n\t"
  "v_sub_f32_e32 v224, 0, v225\n\t"
  "v_sub_f32_e32 v228, 0, v229\n\t"
  "v_sub_f32_e32 v232, 0, v233\n\t"
  "v_sub_f32_e32 v236, 0, v237\n\t"
  "v_mov_b32_e32 v225, v224\n\t"
  "v_mov_b32_e32 v226, v224\n\t"
  "v_mov_b32_e32 v227, v224\n\t"
  "v_mov_b32_e32 v229, v228\n\t"
  "v_mov_b32_e32 v230, v228\n\t"
  "v_mov_b32_e32 v231, v228\n\t"
  "v_mov_b32_e32 v233, v232\n\t"
  "v_mov_b32_e32 v234, v232\n\t"
  "v_mov_b32_e32 v235, v232\n\t"
  "v_mov_b32_e32 v237, v236\n\t"
  "v_mov_b32_e32 v238, v236\n\t"
  "v_mov_b32_e32 v239, v236\n\t"
  "v_exp_f32_e32 v32, v32\n\t"
  "v_exp_f32_e32 v33, v33\n\t"
  "s_nop 0\n\t"
  "v_cvt_pk_bf16_f32 v96, v32, v33\n\t"
  "v_exp_f32_e32 v34, v34\n\t"
  "v_exp_f32_e64 v35, v35\n\t"
  "s_nop 0\n\t"
  "v_cvt_pk_bf16_f32 v97, v34, v35\n\t"
  "v_mfma_f32_16x16x32_bf16 v[48:51], a[144:147], v[160:163], v[224:227]\n\t"
  "v_exp_f32_e64 v36, v36\n\t"
  "v_mfma_f32_16x16x32_bf16 v[48:51], a[148:151], v[164:167], v[48:51]\n\t"
  "ds_read_b128 a[128:131], v2 offset:8192\n\t"
  "v_mfma_f32_16x16x32_bf16 v[48:51], a[152:155], v[168:171], v[48:51]\n\t"
  "v_exp_f32_e64 v37, v37\n\t"
  "v_mfma_f32_16x16x32_bf16 v[48:51], a[156:159], v[172:175], v[48:51]\n\t"
  "ds_read_b128 a[132:135], v3 offset:8192\n\t"
  "v_cvt_pk_bf16_f32 v104, v36, v37\n\t"
  "v_mfma_f32_16x16x32_bf16 v[52:55], a[144:147], v[176:179], v[228:231]\n\t"
  "v_exp_f32_e64 v38, v38\n\t"
  "v_mfma_f32_16x16x32_bf16 v[52:55], a[148:151], v[180:183], v[52:55]\n\t"
  "ds_read_b128 a[136:139], v4 offset:8192\n\t"
  "v_mfma_f32_16x16x32_bf16 v[52:55], a[152:155], v[184:187], v[52:55]\n\t"
  "v_exp_f32_e64 v39, v39\n\t"
  "v_mfma_f32_16x16x32_bf16 v[52:55], a[156:159], v[188:191], v[52:55]\n\t"
  "ds_read_b128 a[140:143], v5 offset:8192\n\t"
  "v_cvt_pk_bf16_f32 v105, v38, v39\n\t"
  "v_mfma_f32_16x16x32_bf16 v[56:59], a[144:147], v[192:195], v[232:235]\n\t"
  "v_exp_f32_e64 v40, v40\n\t"
  "v_mfma_f32_16x16x32_bf16 v[56:59], a[148:151], v[196:199], v[56:59]\n\t"
  "v_mfma_f32_16x16x32_bf16 v[56:59], a[152:155], v[200:203], v[56:59]\n\t"
  "v_exp_f32_e64 v41, v41\n\t"
  "v_mfma_f32_16x16x32_bf16 v[56:59], a[156:159], v[204:207], v[56:59]\n\t"
  "v_cvt_pk_bf16_f32 v112, v40, v41\n\t"
  "v_mfma_f32_16x16x32_bf16 v[60:63], a[144:147], v[208:211], v[236:239]\n\t"
  "v_exp_f32_e64 v42, v42\n\t"
  "v_mfma_f32_16x16x32_bf16 v[60:63], a[148:151], v[212:215], v[60:63]\n\t"
  "v_mfma_f32_16x16x32_bf16 v[60:63], a[152:155], v[216:219], v[60:63]\n\t"
  "s_waitcnt lgkmcnt(0)\n\t"
  "v_exp_f32_e32 v43, v43\n\t"
  "v_mfma_f32_16x16x32_bf16 v[60:63], a[156:159], v[220:223], v[60:63]\n\t"
  "v_cvt_pk_bf16_f32 v113, v42, v43\n\t"
  "v_mfma_f32_16x16x32_bf16 v[64:67], a[128:131], v[160:163], v[224:227]\n\t"
  "v_exp_f32_e64 v44, v44\n\t"
  "v_mfma_f32_16x16x32_bf16 v[64:67], a[132:135], v[164:167], v[64:67]\n\t"
  "ds_read_b128 a[144:147], v2 offset:12288\n\t"
  "v_mfma_f32_16x16x32_bf16 v[64:67], a[136:139], v[168:171], v[64:67]\n\t"
  "v_exp_f32_e64 v45, v45\n\t"
  "v_mfma_f32_16x16x32_bf16 v[64:67], a[140:143], v[172:175], v[64:67]\n\t"
  "ds_read_b128 a[148:151], v3 offset:12288\n\t"
  "v_cvt_pk_bf16_f32 v120, v44, v45\n\t"
  "v_mfma_f32_16x16x32_bf16 v[68:71], a[128:131], v[176:179], v[228:231]\n\t"
  "v_exp_f32_e64 v46, v46\n\t"
  "v_mfma_f32_16x16x32_bf16 v[68:71], a[132:135], v[180:183], v[68:71]\n\t"
  "ds_read_b128 a[152:155], v4 offset:12288\n\t"
  "v_mfma_f32_16x16x32_bf16 v[68:71], a[136:139], v[184:187], v[68:71]\n\t"
  "v_exp_f32_e64 v47, v47\n\t"
  "v_mfma_f32_16x16x32_bf16 v[68:71], a[140:143], v[188:191], v[68:71]\n\t"
  "ds_read_b128 a[156:159], v5 offset:12288\n\t"
  "v_cvt_pk_bf16_f32 v121, v46, v47\n\t"
  "v_mfma_f32_16x16x32_bf16 v[72:75], a[128:131], v[192:195], v[232:235]\n\t"
  "v_exp_f32_e64 v48, v48\n\t"
  "v_mfma_f32_16x16x32_bf16 v[72:75], a[132:135], v[196:199], v[72:75]\n\t"
  "v_mfma_f32_16x16x32_bf16 v[72:75], a[136:139], v[200:203], v[72:75]\n\t"
  "v_exp_f32_e64 v49, v49\n\t"
  "v_mfma_f32_16x16x32_bf16 v[72:75], a[140:143], v[204:207], v[72:75]\n\t"
  "v_cvt_pk_bf16_f32 v98, v48, v49\n\t"
  "v_mfma_f32_16x16x32_bf16 v[76:79], a[128:131], v[208:211], v[236:239]\n\t"
  "v_exp_f32_e64 v50, v50\n\t"
  "v_mfma_f32_16x16x32_bf16 v[76:79], a[132:135], v[212:215], v[76:79]\n\t"
  "v_mfma_f32_16x16x32_bf16 v[76:79], a[136:139], v[216:219], v[76:79]\n\t"
  "s_waitcnt lgkmcnt(0)\n\t"
  "v_exp_f32_e32 v51, v51\n\t"
  "v_mfma_f32_16x16x32_bf16 v[76:79], a[140:143], v[220:223], v[76:79]\n\t"
  "v_cvt_pk_bf16_f32 v99, v50, v51\n\t"
  "v_mfma_f32_16x16x32_bf16 v[80:83], a[144:147], v[160:163], v[224:227]\n\t"
  "v_exp_f32_e64 v52, v52\n\t"
  "v_mfma_f32_16x16x32_bf16 v[80:83], a[148:151], v[164:167], v[80:83]\n\t"
  "v_mfma_f32_16x16x32_bf16 v[80:83], a[152:155], v[168:171], v[80:83]\n\t"
  "v_exp_f32_e64 v53, v53\n\t"
  "v_mfma_f32_16x16x32_bf16 v[80:83], a[156:159], v[172:175], v[80:83]\n\t"
  "v_cvt_pk_bf16_f32 v106, v52, v53\n\t"
  "v_mfma_f32_16x16x32_bf16 v[84:87], a[144:147], v[176:179], v[228:231]\n\t"
  "v_exp_f32_e64 v54, v54\n\t"
  "v_mfma_f32_16x16x32_bf16 v[84:87], a[148:151], v[180:183], v[84:87]\n\t"
  "v_mfma_f32_16x16x32_bf16 v[84:87], a[152:155], v[184:187], v[84:87]\n\t"
  "v_exp_f32_e64 v55, v55\n\t"
  "v_mfma_f32_16x16x32_bf16 v[84:87], a[156:159], v[188:191], v[84:87]\n\t"
  "v_cvt_pk_bf16_f32 v107, v54, v55\n\t"
  "v_mfma_f32_16x16x32_bf16 v[88:91], a[144:147], v[192:195], v[232:235]\n\t"
  "v_exp_f32_e64 v56, v56\n\t"
  "v_mfma_f32_16x16x32_bf16 v[88:91], a[148:151], v[196:199], v[88:91]\n\t"
  "v_mfma_f32_16x16x32_bf16 v[88:91], a[152:155], v[200:203], v[88:91]\n\t"
  "v_exp_f32_e64 v57, v57\n\t"
  "v_mfma_f32_16x16x32_bf16 v[88:91], a[156:159], v[204:207], v[88:91]\n\t"
  "v_cvt_pk_bf16_f32 v114, v56, v57\n\t"
  "v_mfma_f32_16x16x32_bf16 v[92:95], a[144:147], v[208:211], v[236:239]\n\t"
  "v_exp_f32_e64 v58, v58\n\t"
  "v_mfma_f32_16x16x32_bf16 v[92:95], a[148:151], v[212:215], v[92:95]\n\t"
  "v_mfma_f32_16x16x32_bf16 v[92:95], a[152:155], v[216:219], v[92:95]\n\t"
  "v_exp_f32_e64 v59, v59\n\t"
  "v_mfma_f32_16x16x32_bf16 v[92:95], a[156:159], v[220:223], v[92:95]\n\t"
  "v_cvt_pk_bf16_f32 v115, v58, v59\n\t"
  "s_waitcnt vmcnt(33)\n\t"
  "s_barrier\n\t"
  "ds_read_b128 a[128:131], v2 offset:16384\n\t"
  "ds_read_b128 a[132:135], v3 offset:16384\n\t"
  "ds_read_b128 a[136:139], v4 offset:16384\n\t"
  "ds_read_b128 a[140:143], v5 offset:16384\n\t"
  "v_exp_f32_e32 v60, v60\n\t"
  "v_exp_f32_e64 v61, v61\n\t"
  "s_nop 0\n\t"
  "v_cvt_pk_bf16_f32 v122, v60, v61\n\t"
  "v_exp_f32_e32 v62, v62\n\t"
  "v_exp_f32_e64 v63, v63\n\t"
  "s_nop 0\n\t"
  "v_cvt_pk_bf16_f32 v123, v62, v63\n\t"
  "v_exp_f32_e32 v64, v64\n\t"
  "v_exp_f32_e64 v65, v65\n\t"
  "s_nop 0\n\t"
  "v_cvt_pk_bf16_f32 v100, v64, v65\n\t"
  "v_exp_f32_e32 v66, v66\n\t"
  "v_exp_f32_e64 v67, v67\n\t"
  "s_nop 0\n\t"
  "v_cvt_pk_bf16_f32 v101, v66, v67\n\t"
  "v_exp_f32_e32 v68, v68\n\t"
  "v_exp_f32_e64 v69, v69\n\t"
  "s_nop 0\n\t"
  "v_cvt_pk_bf16_f32 v108, v68, v69\n\t"
  "v_exp_f32_e32 v70, v70\n\t"
  "v_exp_f32_e64 v71, v71\n\t"
  "s_nop 0\n\t"
  "v_cvt_pk_bf16_f32 v109, v70, v71\n\t"
  "v_exp_f32_e32 v72, v72\n\t"
  "v_exp_f32_e64 v73, v73\n\t"
  "s_nop 0\n\t"
  "v_cvt_pk_bf16_f32 v116, v72, v73\n\t"
  "v_exp_f32_e32 v74, v74\n\t"
  "v_exp_f32_e64 v75, v75\n\t"
  "s_nop 0\n\t"
  "v_cvt_pk_bf16_f32 v117, v74, v75\n\t"
  "v_exp_f32_e32 v76, v76\n\t"
  "v_exp_f32_e64 v77, v77\n\t"
  "s_nop 0\n\t"
  "v_cvt_pk_bf16_f32 v124, v76, v77\n\t"
  "v_exp_f32_e32 v78, v78\n\t"
  "v_exp_f32_e64 v79, v79\n\t"
  "s_nop 0\n\t"
  "v_cvt_pk_bf16_f32 v125, v78, v79\n\t"
  "v_exp_f32_e32 v80, v80\n\t"
  "v_exp_f32_e64 v81, v81\n\t"
  "s_nop 0\n\t"
  "v_cvt_pk_bf16_f32 v102, v80, v81\n\t"
  "v_exp_f32_e32 v82, v82\n\t"
  "v_exp_f32_e64 v83, v83\n\t"
  "s_nop 0\n\t"
  "v_cvt_pk_bf16_f32 v103, v82, v83\n\t"
  "v_exp_f32_e32 v84, v84\n\t"
  "v_exp_f32_e64 v85, v85\n\t"
  "s_nop 0\n\t"
  "v_cvt_pk_bf16_f32 v110, v84, v85\n\t"
  "v_exp_f32_e32 v86, v86\n\t"
  "v_exp_f32_e64 v87, v87\n\t"
  "s_nop 0\n\t"
  "v_cvt_pk_bf16_f32 v111, v86, v87\n\t"
  "v_exp_f32_e32 v88, v88\n\t"
  "v_exp_f32_e64 v89, v89\n\t"
  "s_nop 0\n\t"
  "v_cvt_pk_bf16_f32 v118, v88, v89\n\t"
  "v_exp_f32_e32 v90, v90\n\t"
  "v_exp_f32_e64 v91, v91\n\t"
  "s_nop 0\n\t"
  "v_cvt_pk_bf16_f32 v119, v90, v91\n\t"
  "v_exp_f32_e32 v92, v92\n\t"
  "v_exp_f32_e64 v93, v93\n\t"
  "s_nop 0\n\t"
  "v_cvt_pk_bf16_f32 v126, v92, v93\n\t"
  "v_exp_f32_e32 v94, v94\n\t"
  "v_exp_f32_e64 v95, v95\n\t"
  "s_nop 0\n\t"
  "v_cvt_pk_bf16_f32 v127, v94, v95\n\t"
  "s_waitcnt lgkmcnt(0)\n\t"
  "s_lshr_b32 s41, %12, 1\n\t"
  "s_lshl_b32 s41, s41, 2\n\t"
  "s_add_u32 s41, s41, %11\n\t"
  "s_lshl_b32 s40, s41, 8\n\t"
  "s_add_u32 s40, s40, 2244608\n\t"
  "s_add_u32 s50, s64, s40\n\t"
  "s_addc_u32 s51, s65, 0\n\t"
  "v_mov_b32_e64 v21, 0\n\t"
  "s_lshr_b32 s40, %10, 8\n\t"
  "s_and_b32 s41, %10, 0xff\n\t"
  "s_mul_i32 s42, s40, 458752\n\t"
  "s_mul_i32 s43, s41, 3584\n\t"
  "s_add_u32 s42, s42, s43\n\t"
  "s_add_u32 s24, s74, s42\n\t"
  "s_addc_u32 s25, s75, 0\n\t"
  "s_lshl_b32 s42, s40, 20\n\t"
  "s_lshl_b32 s43, s41, 13\n\t"
  "s_add_u32 s42, s42, s43\n\t"
  "s_add_u32 s26, s76, s42\n\t"
  "s_addc_u32 s27, s77, 0\n\t"
  "s_add_u32 s28, s26, 0x1000\n\t"
  "s_addc_u32 s29, s27, 0\n\t"
  "s_mov_b32 s17, 7\n\t"
  "s_waitcnt vmcnt(8)\n\t"
  "s_barrier\n\t"
  "v_mfma_f32_16x16x32_bf16 v[32:35], a[128:131], v[160:163], v[224:227]\n\t"
  "v_mfma_f32_16x16x32_bf16 v[32:35], a[132:135], v[164:167], v[32:35]\n\t"
  "ds_read_b128 a[144:147], v2 offset:20480\n\t"
  "v_mfma_f32_16x16x32_bf16 v[32:35], a[136:139], v[168:171], v[32:35]\n\t"
  "v_mfma_f32_16x16x32_bf16 v[32:35], a[140:143], v[172:175], v[32:35]\n\t"
  "ds_read_b128 a[148:151], v3 offset:20480\n\t"
  "v_mfma_f32_16x16x32_bf16 v[36:39], a[128:131], v[176:179], v[228:231]\n\t"
  "v_mfma_f32_16x16x32_bf16 v[36:39], a[132:135], v[180:183], v[36:39]\n\t"
  "ds_read_b128 a[152:155], v4 offset:20480\n\t"
  "v_mfma_f32_16x16x32_bf16 v[36:39], a[136:139], v[184:187], v[36:39]\n\t"
  "v_mfma_f32_16x16x32_bf16 v[36:39], a[140:143], v[188:191], v[36:39]\n\t"
  "ds_read_b128 a[156:159], v5 offset:20480\n\t"
  "v_mfma_f32_16x16x32_bf16 v[40:43], a[128:131], v[192:195], v[232:235]\n\t"
  "s_add_u32 m0, s18, 0\n\t"
  "v_exp_f32_e32 v32, v32\n\t"
  "v_mfma_f32_16x16x32_bf16 v[40:43], a[132:135], v[196:199], v[40:43]\n\t"
  "buffer_load_dwordx4 v8, s[8:11], s16 offen offset:0 lds\n\t"
  "v_mfma_f32_16x16x32_bf16 v[40:43], a[136:139], v[200:203], v[40:43]\n\t"
  "v_exp_f32_e64 v33, v33\n\t"
  "v_mfma_f32_16x16x32_bf16 v[40:43], a[140:143], v[204:207], v[40:43]\n\t"
  "v_cvt_pk_bf16_f32 v128, v32, v33\n\t"
  "v_mfma_f32_16x16x32_bf16 v[44:47], a[128:131], v[208:211], v[236:239]\n\t"
  "v_exp_f32_e64 v34, v34\n\t"
  "v_mfma_f32_16x16x32_bf16 v[44:47], a[132:135], v[212:215], v[44:47]\n\t"
  "buffer_load_dwordx4 v9, s[8:11], s16 offen offset:1024 lds\n\t"
  "v_mfma_f32_16x16x32_bf16 v[44:47], a[136:139], v[216:219], v[44:47]\n\t"
  "s_waitcnt lgkmcnt(0)\n\t"
  "v_exp_f32_e32 v35, v35\n\t"
  "v_mfma_f32_16x16x32_bf16 v[44:47], a[140:143], v[220:223], v[44:47]\n\t"
  "v_cvt_pk_bf16_f32 v129, v34, v35\n\t"
  "v_mfma_f32_16x16x32_bf16 v[48:51], a[144:147], v[160:163], v[224:227]\n\t"
  "v_exp_f32_e64 v36, v36\n\t"
  "v_mfma_f32_16x16x32_bf16 v[48:51], a[148:151], v[164:167], v[48:51]\n\t"
  "ds_read_b128 a[128:131], v2 offset:24576\n\t"
  "v_mfma_f32_16x16x32_bf16 v[48:51], a[152:155], v[168:171], v[48:51]\n\t"
  "v_exp_f32_e64 v37, v37\n\t"
  "v_mfma_f32_16x16x32_bf16 v[48:51], a[156:159], v[172:175], v[48:51]\n\t"
  "ds_read_b128 a[132:135], v3 offset:24576\n\t"
  "v_cvt_pk_bf16_f32 v136, v36, v37\n\t"
  "v_mfma_f32_16x16x32_bf16 v[52:55], a[144:147], v[176:179], v[228:231]\n\t"
  "v_exp_f32_e64 v38, v38\n\t"
  "v_mfma_f32_16x16x32_bf16 v[52:55], a[148:151], v[180:183], v[52:55]\n\t"
  "ds_read_b128 a[136:139], v4 offset:24576\n\t"
  "v_mfma_f32_16x16x32_bf16 v[52:55], a[152:155], v[184:187], v[52:55]\n\t"
  "v_exp_f32_e64 v39, v39\n\t"
  "v_mfma_f32_16x16x32_bf16 v[52:55], a[156:159], v[188:191], v[52:55]\n\t"
  "ds_read_b128 a[140:143], v5 offset:24576\n\t"
  "v_cvt_pk_bf16_f32 v137, v38, v39\n\t"
  "v_mfma_f32_16x16x32_bf16 v[56:59], a[144:147], v[192:195], v[232:235]\n\t"
  "v_exp_f32_e64 v40, v40\n\t"
  "v_mfma_f32_16x16x32_bf16 v[56:59], a[148:151], v[196:199], v[56:59]\n\t"
  "buffer_load_dwordx4 v10, s[8:11], s16 offen offset:2048 lds\n\t"
  "v_mfma_f32_16x16x32_bf16 v[56:59], a[152:155], v[200:203], v[56:59]\n\t"
  "v_exp_f32_e64 v41, v41\n\t"
  "v_mfma_f32_16x16x32_bf16 v[56:59], a[156:159], v[204:207], v[56:59]\n\t"
  "v_cvt_pk_bf16_f32 v144, v40, v41\n\t"
  "v_mfma_f32_16x16x32_bf16 v[60:63], a[144:147], v[208:211], v[236:239]\n\t"
  "v_exp_f32_e64 v42, v42\n\t"
  "v_mfma_f32_16x16x32_bf16 v[60:63], a[148:151], v[212:215], v[60:63]\n\t"
  "buffer_load_dwordx4 v11, s[8:11], s16 offen offset:3072 lds\n\t"
  "v_mfma_f32_16x16x32_bf16 v[60:63], a[152:155], v[216:219], v[60:63]\n\t"
  "s_add_u32 s16, s16, 0x4000\n\t"
  "v_exp_f32_e64 v43, v43\n\t"
  "v_mfma_f32_16x16x32_bf16 v[60:63], a[156:159], v[220:223], v[60:63]\n\t"
  "s_waitcnt lgkmcnt(0)\n\t"
  "s_nop 0\n\t"
  "v_cvt_pk_bf16_f32 v145, v42, v43\n\t"
  "v_mfma_f32_16x16x32_bf16 v[64:67], a[128:131], v[160:163], v[224:227]\n\t"
  "v_exp_f32_e64 v44, v44\n\t"
  "v_mfma_f32_16x16x32_bf16 v[64:67], a[132:135], v[164:167], v[64:67]\n\t"
  "ds_read_b128 a[144:147], v2 offset:28672\n\t"
  "v_mfma_f32_16x16x32_bf16 v[64:67], a[136:139], v[168:171], v[64:67]\n\t"
  "v_exp_f32_e64 v45, v45\n\t"
  "v_mfma_f32_16x16x32_bf16 v[64:67], a[140:143], v[172:175], v[64:67]\n\t"
  "ds_read_b128 a[148:151], v3 offset:28672\n\t"
  "v_cvt_pk_bf16_f32 v152, v44, v45\n\t"
  "v_mfma_f32_16x16x32_bf16 v[68:71], a[128:131], v[176:179], v[228:231]\n\t"
  "v_exp_f32_e64 v46, v46\n\t"
  "v_mfma_f32_16x16x32_bf16 v[68:71], a[132:135], v[180:183], v[68:71]\n\t"
  "ds_read_b128 a[152:155], v4 offset:28672\n\t"
  "v_mfma_f32_16x16x32_bf16 v[68:71], a[136:139], v[184:187], v[68:71]\n\t"
  "v_exp_f32_e64 v47, v47\n\t"
  "v_mfma_f32_16x16x32_bf16 v[68:71], a[140:143], v[188:191], v[68:71]\n\t"
  "ds_read_b128 a[156:159], v5 offset:28672\n\t"
  "v_cvt_pk_bf16_f32 v153, v46, v47\n\t"
  "v_mfma_f32_16x16x32_bf16 v[72:75], a[128:131], v[192:195], v[232:235]\n\t"
  "s_add_u32 m0, s19, 32768\n\t"
  "v_exp_f32_e64 v48, v48\n\t"
  "v_mfma_f32_16x16x32_bf16 v[72:75], a[132:135], v[196:199], v[72:75]\n\t"
  "buffer_load_dwordx4 v12, s[12:15], s20 offen offset:0 lds\n\t"
  "v_mfma_f32_16x16x32_bf16 v[72:75], a[136:139], v[200:203], v[72:75]\n\t"
  "v_exp_f32_e64 v49, v49\n\t"
  "v_mfma_f32_16x16x32_bf16 v[72:75], a[140:143], v[204:207], v[72:75]\n\t"
  "v_cvt_pk_bf16_f32 v130, v48, v49\n\t"
  "v_mfma_f32_16x16x32_bf16 v[76:79], a[128:131], v[208:211], v[236:239]\n\t"
  "v_exp_f32_e64 v50, v50\n\t"
  "v_mfma_f32_16x16x32_bf16 v[76:79], a[132:135], v[212:215], v[76:79]\n\t"
  "buffer_load_dwordx4 v13, s[12:15], s20 offen offset:1024 lds\n\t"
  "v_mfma_f32_16x16x32_bf16 v[76:79], a[136:139], v[216:219], v[76:79]\n\t"
  "s_waitcnt lgkmcnt(0)\n\t"
  "v_exp_f32_e32 v51, v51\n\t"
  "v_mfma_f32_16x16x32_bf16 v[76:79], a[140:143], v[220:223], v[76:79]\n\t"
  "v_cvt_pk_bf16_f32 v131, v50, v51\n\t"
  "v_mfma_f32_16x16x32_bf16 v[80:83], a[144:147], v[160:163], v[224:227]\n\t"
  "v_exp_f32_e64 v52, v52\n\t"
  "v_mfma_f32_16x16x32_bf16 v[80:83], a[148:151], v[164:167], v[80:83]\n\t"
  "ds_read_b64_tr_b16 a[160:161], v6 offset:0\n\t"
  "v_mfma_f32_16x16x32_bf16 v[80:83], a[152:155], v[168:171], v[80:83]\n\t"
  "v_exp_f32_e64 v53, v53\n\t"
  "v_mfma_f32_16x16x32_bf16 v[80:83], a[156:159], v[172:175], v[80:83]\n\t"
  "ds_read_b64_tr_b16 a[162:163], v6 offset:4096\n\t"
  "v_cvt_pk_bf16_f32 v138, v52, v53\n\t"
  "v_mfma_f32_16x16x32_bf16 v[84:87], a[144:147], v[176:179], v[228:231]\n\t"
  "v_exp_f32_e64 v54, v54\n\t"
  "v_mfma_f32_16x16x32_bf16 v[84:87], a[148:151], v[180:183], v[84:87]\n\t"
  "ds_read_b64_tr_b16 a[164:165], v6 offset:8192\n\t"
  "v_mfma_f32_16x16x32_bf16 v[84:87], a[152:155], v[184:187], v[84:87]\n\t"
  "v_exp_f32_e64 v55, v55\n\t"
  "v_mfma_f32_16x16x32_bf16 v[84:87], a[156:159], v[188:191], v[84:87]\n\t"
  "ds_read_b64_tr_b16 a[166:167], v6 offset:12288\n\t"
  "v_cvt_pk_bf16_f32 v139, v54, v55\n\t"
  "v_mfma_f32_16x16x32_bf16 v[88:91], a[144:147], v[192:195], v[232:235]\n\t"
  "v_exp_f32_e64 v56, v56\n\t"
  "v_mfma_f32_16x16x32_bf16 v[88:91], a[148:151], v[196:199], v[88:91]\n\t"
  "ds_read_b64_tr_b16 a[168:169], v7 offset:0\n\t"
  "v_mfma_f32_16x16x32_bf16 v[88:91], a[152:155], v[200:203], v[88:91]\n\t"
  "v_exp_f32_e64 v57, v57\n\t"
  "v_mfma_f32_16x16x32_bf16 v[88:91], a[156:159], v[204:207], v[88:91]\n\t"
  "ds_read_b64_tr_b16 a[170:171], v7 offset:4096\n\t"
  "v_cvt_pk_bf16_f32 v146, v56, v57\n\t"
  "v_mfma_f32_16x16x32_bf16 v[92:95], a[144:147], v[208:211], v[236:239]\n\t"
  "v_exp_f32_e64 v58, v58\n\t"
  "v_mfma_f32_16x16x32_bf16 v[92:95], a[148:151], v[212:215], v[92:95]\n\t"
  "ds_read_b64_tr_b16 a[172:173], v7 offset:8192\n\t"
  "v_mfma_f32_16x16x32_bf16 v[92:95], a[152:155], v[216:219], v[92:95]\n\t"
  "s_waitcnt lgkmcnt(2)\n\t"
  "v_exp_f32_e32 v59, v59\n\t"
  "v_mfma_f32_16x16x32_bf16 v[92:95], a[156:159], v[220:223], v[92:95]\n\t"
  "ds_read_b64_tr_b16 a[174:175], v7 offset:12288\n\t"
  "v_cvt_pk_bf16_f32 v147, v58, v59\n\t"
  "v_mfma_f32_16x16x32_bf16 a[0:3], a[160:163], v[96:99], 0\n\t"
  "v_exp_f32_e64 v60, v60\n\t"
  "v_mfma_f32_16x16x32_bf16 a[0:3], a[164:167], v[100:103], a[0:3]\n\t"
  "ds_read_b64_tr_b16 a[176:177], v6 offset:512\n\t"
  "v_mfma_f32_16x16x32_bf16 a[32:35], a[160:163], v[104:107], 0\n\t"
  "v_exp_f32_e64 v61, v61\n\t"
  "v_mfma_f32_16x16x32_bf16 a[32:35], a[164:167], v[108:111], a[32:35]\n\t"
  "ds_read_b64_tr_b16 a[178:179], v6 offset:4608\n\t"
  "v_cvt_pk_bf16_f32 v154, v60, v61\n\t"
  "v_mfma_f32_16x16x32_bf16 a[64:67], a[160:163], v[112:115], 0\n\t"
  "v_exp_f32_e64 v62, v62\n\t"
  "v_mfma_f32_16x16x32_bf16 a[64:67], a[164:167], v[116:119], a[64:67]\n\t"
  "ds_read_b64_tr_b16 a[180:181], v6 offset:8704\n\t"
  "v_mfma_f32_16x16x32_bf16 a[96:99], a[160:163], v[120:123], 0\n\t"
  "s_waitcnt lgkmcnt(2)\n\t"
  "v_exp_f32_e32 v63, v63\n\t"
  "v_mfma_f32_16x16x32_bf16 a[96:99], a[164:167], v[124:127], a[96:99]\n\t"
  "ds_read_b64_tr_b16 a[182:183], v6 offset:12800\n\t"
  "v_cvt_pk_bf16_f32 v155, v62, v63\n\t"
  "v_mfma_f32_16x16x32_bf16 a[4:7], a[168:171], v[96:99], 0\n\t"
  "v_exp_f32_e64 v64, v64\n\t"
  "v_mfma_f32_16x16x32_bf16 a[4:7], a[172:175], v[100:103], a[4:7]\n\t"
  "ds_read_b64_tr_b16 a[184:185], v7 offset:512\n\t"
  "v_mfma_f32_16x16x32_bf16 a[36:39], a[168:171], v[104:107], 0\n\t"
  "v_exp_f32_e64 v65, v65\n\t"
  "v_mfma_f32_16x16x32_bf16 a[36:39], a[172:175], v[108:111], a[36:39]\n\t"
  "ds_read_b64_tr_b16 a[186:187], v7 offset:4608\n\t"
  "v_cvt_pk_bf16_f32 v132, v64, v65\n\t"
  "v_mfma_f32_16x16x32_bf16 a[68:71], a[168:171], v[112:115], 0\n\t"
  "v_exp_f32_e64 v66, v66\n\t"
  "v_mfma_f32_16x16x32_bf16 a[68:71], a[172:175], v[116:119], a[68:71]\n\t"
  "ds_read_b64_tr_b16 a[188:189], v7 offset:8704\n\t"
  "v_mfma_f32_16x16x32_bf16 a[100:103], a[168:171], v[120:123], 0\n\t"
  "s_waitcnt lgkmcnt(2)\n\t"
  "v_exp_f32_e32 v67, v67\n\t"
  "v_mfma_f32_16x16x32_bf16 a[100:103], a[172:175], v[124:127], a[100:103]\n\t"
  "ds_read_b64_tr_b16 a[190:191], v7 offset:12800\n\t"
  "v_cvt_pk_bf16_f32 v133, v66, v67\n\t"
  "v_mfma_f32_16x16x32_bf16 a[8:11], a[176:179], v[96:99], 0\n\t"
  "v_exp_f32_e64 v68, v68\n\t"
  "v_mfma_f32_16x16x32_bf16 a[8:11], a[180:183], v[100:103], a[8:11]\n\t"
  "ds_read_b64_tr_b16 a[160:161], v6 offset:1024\n\t"
  "v_mfma_f32_16x16x32_bf16 a[40:43], a[176:179], v[104:107], 0\n\t"
  "v_exp_f32_e64 v69, v69\n\t"
  "v_mfma_f32_16x16x32_bf16 a[40:43], a[180:183], v[108:111], a[40:43]\n\t"
  "ds_read_b64_tr_b16 a[162:163], v6 offset:5120\n\t"
  "v_cvt_pk_bf16_f32 v140, v68, v69\n\t"
  "v_mfma_f32_16x16x32_bf16 a[72:75], a[176:179], v[112:115], 0\n\t"
  "v_exp_f32_e64 v70, v70\n\t"
  "v_mfma_f32_16x16x32_bf16 a[72:75], a[180:183], v[116:119], a[72:75]\n\t"
  "ds_read_b64_tr_b16 a[164:165], v6 offset:9216\n\t"
  "v_mfma_f32_16x16x32_bf16 a[104:107], a[176:179], v[120:123], 0\n\t"
  "s_waitcnt lgkmcnt(2)\n\t"
  "v_exp_f32_e32 v71, v71\n\t"
  "v_mfma_f32_16x16x32_bf16 a[104:107], a[180:183], v[124:127], a[104:107]\n\t"
  "ds_read_b64_tr_b16 a[166:167], v6 offset:13312\n\t"
  "v_cvt_pk_bf16_f32 v141, v70, v71\n\t"
  "v_mfma_f32_16x16x32_bf16 a[12:15], a[184:187], v[96:99], 0\n\t"
  "v_exp_f32_e64 v72, v72\n\t"
  "v_mfma_f32_16x16x32_bf16 a[12:15], a[188:191], v[100:103], a[12:15]\n\t"
  "ds_read_b64_tr_b16 a[168:169], v7 offset:1024\n\t"
  "v_mfma_f32_16x16x32_bf16 a[44:47], a[184:187], v[104:107], 0\n\t"
  "v_exp_f32_e64 v73, v73\n\t"
  "v_mfma_f32_16x16x32_bf16 a[44:47], a[188:191], v[108:111], a[44:47]\n\t"
  "ds_read_b64_tr_b16 a[170:171], v7 offset:5120\n\t"
  "v_cvt_pk_bf16_f32 v148, v72, v73\n\t"
  "v_mfma_f32_16x16x32_bf16 a[76:79], a[184:187], v[112:115], 0\n\t"
  "v_exp_f32_e64 v74, v74\n\t"
  "v_mfma_f32_16x16x32_bf16 a[76:79], a[188:191], v[116:119], a[76:79]\n\t"
  "ds_read_b64_tr_b16 a[172:173], v7 offset:9216\n\t"
  "v_mfma_f32_16x16x32_bf16 a[108:111], a[184:187], v[120:123], 0\n\t"
  "s_waitcnt lgkmcnt(2)\n\t"
  "v_exp_f32_e32 v75, v75\n\t"
  "v_mfma_f32_16x16x32_bf16 a[108:111], a[188:191], v[124:127], a[108:111]\n\t"
  "ds_read_b64_tr_b16 a[174:175], v7 offset:13312\n\t"
  "v_cvt_pk_bf16_f32 v149, v74, v75\n\t"
  "v_mfma_f32_16x16x32_bf16 a[16:19], a[160:163], v[96:99], 0\n\t"
  "v_exp_f32_e64 v76, v76\n\t"
  "v_mfma_f32_16x16x32_bf16 a[16:19], a[164:167], v[100:103], a[16:19]\n\t"
  "ds_read_b64_tr_b16 a[176:177], v6 offset:1536\n\t"
  "v_mfma_f32_16x16x32_bf16 a[48:51], a[160:163], v[104:107], 0\n\t"
  "v_exp_f32_e64 v77, v77\n\t"
  "v_mfma_f32_16x16x32_bf16 a[48:51], a[164:167], v[108:111], a[48:51]\n\t"
  "ds_read_b64_tr_b16 a[178:179], v6 offset:5632\n\t"
  "v_cvt_pk_bf16_f32 v156, v76, v77\n\t"
  "v_mfma_f32_16x16x32_bf16 a[80:83], a[160:163], v[112:115], 0\n\t"
  "v_exp_f32_e64 v78, v78\n\t"
  "v_mfma_f32_16x16x32_bf16 a[80:83], a[164:167], v[116:119], a[80:83]\n\t"
  "ds_read_b64_tr_b16 a[180:181], v6 offset:9728\n\t"
  "v_mfma_f32_16x16x32_bf16 a[112:115], a[160:163], v[120:123], 0\n\t"
  "s_waitcnt lgkmcnt(2)\n\t"
  "v_exp_f32_e32 v79, v79\n\t"
  "v_mfma_f32_16x16x32_bf16 a[112:115], a[164:167], v[124:127], a[112:115]\n\t"
  "ds_read_b64_tr_b16 a[182:183], v6 offset:13824\n\t"
  "v_cvt_pk_bf16_f32 v157, v78, v79\n\t"
  "v_mfma_f32_16x16x32_bf16 a[20:23], a[168:171], v[96:99], 0\n\t"
  "v_exp_f32_e64 v80, v80\n\t"
  "v_mfma_f32_16x16x32_bf16 a[20:23], a[172:175], v[100:103], a[20:23]\n\t"
  "ds_read_b64_tr_b16 a[184:185], v7 offset:1536\n\t"
  "v_mfma_f32_16x16x32_bf16 a[52:55], a[168:171], v[104:107], 0\n\t"
  "v_exp_f32_e64 v81, v81\n\t"
  "v_mfma_f32_16x16x32_bf16 a[52:55], a[172:175], v[108:111], a[52:55]\n\t"
  "ds_read_b64_tr_b16 a[186:187], v7 offset:5632\n\t"
  "v_cvt_pk_bf16_f32 v134, v80, v81\n\t"
  "v_mfma_f32_16x16x32_bf16 a[84:87], a[168:171], v[112:115], 0\n\t"
  "v_exp_f32_e64 v82, v82\n\t"
  "v_mfma_f32_16x16x32_bf16 a[84:87], a[172:175], v[116:119], a[84:87]\n\t"
  "ds_read_b64_tr_b16 a[188:189], v7 offset:9728\n\t"
  "v_mfma_f32_16x16x32_bf16 a[116:119], a[168:171], v[120:123], 0\n\t"
  "s_waitcnt lgkmcnt(2)\n\t"
  "v_exp_f32_e32 v83, v83\n\t"
  "v_mfma_f32_16x16x32_bf16 a[116:119], a[172:175], v[124:127], a[116:119]\n\t"
  "ds_read_b64_tr_b16 a[190:191], v7 offset:13824\n\t"
  "v_cvt_pk_bf16_f32 v135, v82, v83\n\t"
  "v_mfma_f32_16x16x32_bf16 a[24:27], a[176:179], v[96:99], 0\n\t"
  "v_exp_f32_e64 v84, v84\n\t"
  "v_mfma_f32_16x16x32_bf16 a[24:27], a[180:183], v[100:103], a[24:27]\n\t"
  "ds_read_b128 a[128:131], v2 offset:32768\n\t"
  "v_mfma_f32_16x16x32_bf16 a[56:59], a[176:179], v[104:107], 0\n\t"
  "v_exp_f32_e64 v85, v85\n\t"
  "v_mfma_f32_16x16x32_bf16 a[56:59], a[180:183], v[108:111], a[56:59]\n\t"
  "ds_read_b128 a[132:135], v3 offset:32768\n\t"
  "v_cvt_pk_bf16_f32 v142, v84, v85\n\t"
  "v_mfma_f32_16x16x32_bf16 a[88:91], a[176:179], v[112:115], 0\n\t"
  "v_exp_f32_e64 v86, v86\n\t"
  "v_mfma_f32_16x16x32_bf16 a[88:91], a[180:183], v[116:119], a[88:91]\n\t"
  "ds_read_b128 a[136:139], v4 offset:32768\n\t"
  "v_mfma_f32_16x16x32_bf16 a[120:123], a[176:179], v[120:123], 0\n\t"
  "s_waitcnt lgkmcnt(2)\n\t"
  "v_exp_f32_e32 v87, v87\n\t"
  "v_mfma_f32_16x16x32_bf16 a[120:123], a[180:183], v[124:127], a[120:123]\n\t"
  "ds_read_b128 a[140:143], v5 offset:32768\n\t"
  "v_cvt_pk_bf16_f32 v143, v86, v87\n\t"
  "v_mfma_f32_16x16x32_bf16 a[28:31], a[184:187], v[96:99], 0\n\t"
  "v_exp_f32_e64 v88, v88\n\t"
  "v_mfma_f32_16x16x32_bf16 a[28:31], a[188:191], v[100:103], a[28:31]\n\t"
  "buffer_load_dwordx4 v12, s[12:15], s20 offen offset:2048 lds\n\t"
  "v_mfma_f32_16x16x32_bf16 a[60:63], a[184:187], v[104:107], 0\n\t"
  "v_exp_f32_e64 v89, v89\n\t"
  "v_mfma_f32_16x16x32_bf16 a[60:63], a[188:191], v[108:111], a[60:63]\n\t"
  "v_cvt_pk_bf16_f32 v150, v88, v89\n\t"
  "v_mfma_f32_16x16x32_bf16 a[92:95], a[184:187], v[112:115], 0\n\t"
  "v_exp_f32_e64 v90, v90\n\t"
  "v_mfma_f32_16x16x32_bf16 a[92:95], a[188:191], v[116:119], a[92:95]\n\t"
  "buffer_load_dwordx4 v13, s[12:15], s20 offen offset:3072 lds\n\t"
  "v_mfma_f32_16x16x32_bf16 a[124:127], a[184:187], v[120:123], 0\n\t"
  "s_add_u32 s20, s20, 0x4000\n\t"
  "v_exp_f32_e64 v91, v91\n\t"
  "v_mfma_f32_16x16x32_bf16 a[124:127], a[188:191], v[124:127], a[124:127]\n\t"
  "v_cvt_pk_bf16_f32 v151, v90, v91\n\t"
  "v_mfma_f32_16x16x32_bf16 a[192:195], a[208:211], v[96:99], 0\n\t"
  "v_exp_f32_e64 v92, v92\n\t"
  "v_mfma_f32_16x16x32_bf16 a[192:195], a[208:211], v[100:103], a[192:195]\n\t"
  "v_mfma_f32_16x16x32_bf16 a[196:199], a[208:211], v[104:107], 0\n\t"
  "v_exp_f32_e64 v93, v93\n\t"
  "v_mfma_f32_16x16x32_bf16 a[196:199], a[208:211], v[108:111], a[196:199]\n\t"
  "v_cvt_pk_bf16_f32 v158, v92, v93\n\t"
  "v_mfma_f32_16x16x32_bf16 a[200:203], a[208:211], v[112:115], 0\n\t"
  "v_exp_f32_e64 v94, v94\n\t"
  "v_mfma_f32_16x16x32_bf16 a[200:203], a[208:211], v[116:119], a[200:203]\n\t"
  "v_mfma_f32_16x16x32_bf16 a[204:207], a[208:211], v[120:123], 0\n\t"
  "v_exp_f32_e64 v95, v95\n\t"
  "v_mfma_f32_16x16x32_bf16 a[204:207], a[208:211], v[124:127], a[204:207]\n\t"
  "v_cvt_pk_bf16_f32 v159, v94, v95\n\t"
  "s_branch L_loop1_%=\n\t"
  ".p2align 6\n\t"
  "L_loop_%=:\n\t"
  "s_waitcnt vmcnt(8)\n\t"
  "s_barrier\n\t"
  "v_mfma_f32_16x16x32_bf16 v[32:35], a[128:131], v[160:163], v[224:227]\n\t"
  "s_waitcnt lgkmcnt(0)\n\t"
  "s_nop 0\n\t"
  "v_mfma_f32_16x16x32_bf16 v[32:35], a[132:135], v[164:167], v[32:35]\n\t"
  "ds_read_b128 a[144:147], v2 offset:20480\n\t"
  "v_mfma_f32_16x16x32_bf16 v[32:35], a[136:139], v[168:171], v[32:35]\n\t"
  "v_mfma_f32_16x16x32_bf16 v[32:35], a[140:143], v[172:175], v[32:35]\n\t"
  "ds_read_b128 a[148:151], v3 offset:20480\n\t"
  "v_mfma_f32_16x16x32_bf16 v[36:39], a[128:131], v[176:179], v[228:231]\n\t"
  "v_mfma_f32_16x16x32_bf16 v[36:39], a[132:135], v[180:183], v[36:39]\n\t"
  "ds_read_b128 a[152:155], v4 offset:20480\n\t"
  "v_mfma_f32_16x16x32_bf16 v[36:39], a[136:139], v[184:187], v[36:39]\n\t"
  "v_mfma_f32_16x16x32_bf16 v[36:39], a[140:143], v[188:191], v[36:39]\n\t"
  "ds_read_b128 a[156:159], v5 offset:20480\n\t"
  "v_mfma_f32_16x16x32_bf16 v[40:43], a[128:131], v[192:195], v[232:235]\n\t"
  "s_add_u32 m0, s18, 0\n\t"
  "v_exp_f32_e32 v32, v32\n\t"
  "v_mfma_f32_16x16x32_bf16 v[40:43], a[132:135], v[196:199], v[40:43]\n\t"
  "buffer_load_dwordx4 v8, s[8:11], s16 offen offset:0 lds\n\t"
  "v_mfma_f32_16x16x32_bf16 v[40:43], a[136:139], v[200:203], v[40:43]\n\t"
  "v_exp_f32_e64 v33, v33\n\t"
  "v_mfma_f32_16x16x32_bf16 v[40:43], a[140:143], v[204:207], v[40:43]\n\t"
  "v_cvt_pk_bf16_f32 v128, v32, v33\n\t"
  "v_mfma_f32_16x16x32_bf16 v[44:47], a[128:131], v[208:211], v[236:239]\n\t"
  "v_exp_f32_e64 v34, v34\n\t"
  "v_mfma_f32_16x16x32_bf16 v[44:47], a[132:135], v[212:215], v[44:47]\n\t"
  "buffer_load_dwordx4 v9, s[8:11], s16 offen offset:1024 lds\n\t"
  "v_mfma_f32_16x16x32_bf16 v[44:47], a[136:139], v[216:219], v[44:47]\n\t"
  "s_waitcnt lgkmcnt(0)\n\t"
  "v_exp_f32_e32 v35, v35\n\t"
  "v_mfma_f32_16x16x32_bf16 v[44:47], a[140:143], v[220:223], v[44:47]\n\t"
  "v_cvt_pk_bf16_f32 v129, v34, v35\n\t"
  "v_mfma_f32_16x16x32_bf16 v[48:51], a[144:147], v[160:163], v[224:227]\n\t"
  "v_exp_f32_e64 v36, v36\n\t"
  "v_mfma_f32_16x16x32_bf16 v[48:51], a[148:151], v[164:167], v[48:51]\n\t"
  "ds_read_b128 a[128:131], v2 offset:24576\n\t"
  "v_mfma_f32_16x16x32_bf16 v[48:51], a[152:155], v[168:171], v[48:51]\n\t"
  "v_exp_f32_e64 v37, v37\n\t"
  "v_mfma_f32_16x16x32_bf16 v[48:51], a[156:159], v[172:175], v[48:51]\n\t"
  "ds_read_b128 a[132:135], v3 offset:24576\n\t"
  "v_cvt_pk_bf16_f32 v136, v36, v37\n\t"
  "v_mfma_f32_16x16x32_bf16 v[52:55], a[144:147], v[176:179], v[228:231]\n\t"
  "v_exp_f32_e64 v38, v38\n\t"
  "v_mfma_f32_16x16x32_bf16 v[52:55], a[148:151], v[180:183], v[52:55]\n\t"
  "ds_read_b128 a[136:139], v4 offset:24576\n\t"
  "v_mfma_f32_16x16x32_bf16 v[52:55], a[152:155], v[184:187], v[52:55]\n\t"
  "v_exp_f32_e64 v39, v39\n\t"
  "v_mfma_f32_16x16x32_bf16 v[52:55], a[156:159], v[188:191], v[52:55]\n\t"
  "ds_read_b128 a[140:143], v5 offset:24576\n\t"
  "v_cvt_pk_bf16_f32 v137, v38, v39\n\t"
  "v_mfma_f32_16x16x32_bf16 v[56:59], a[144:147], v[192:195], v[232:235]\n\t"
  "v_exp_f32_e64 v40, v40\n\t"
  "v_mfma_f32_16x16x32_bf16 v[56:59], a[148:151], v[196:199], v[56:59]\n\t"
  "buffer_load_dwordx4 v10, s[8:11], s16 offen offset:2048 lds\n\t"
  "v_mfma_f32_16x16x32_bf16 v[56:59], a[152:155], v[200:203], v[56:59]\n\t"
  "v_exp_f32_e64 v41, v41\n\t"
  "v_mfma_f32_16x16x32_bf16 v[56:59], a[156:159], v[204:207], v[56:59]\n\t"
  "v_cvt_pk_bf16_f32 v144, v40, v41\n\t"
  "v_mfma_f32_16x16x32_bf16 v[60:63], a[144:147], v[208:211], v[236:239]\n\t"
  "v_exp_f32_e64 v42, v42\n\t"
  "v_mfma_f32_16x16x32_bf16 v[60:63], a[148:151], v[212:215], v[60:63]\n\t"
  "buffer_load_dwordx4 v11, s[8:11], s16 offen offset:3072 lds\n\t"
  "v_mfma_f32_16x16x32_bf16 v[60:63], a[152:155], v[216:219], v[60:63]\n\t"
  "s_add_u32 s16, s16, 0x4000\n\t"
  "v_exp_f32_e64 v43, v43\n\t"
  "v_mfma_f32_16x16x32_bf16 v[60:63], a[156:159], v[220:223], v[60:63]\n\t"
  "s_waitcnt lgkmcnt(0)\n\t"
  "s_nop 0\n\t"
  "v_cvt_pk_bf16_f32 v145, v42, v43\n\t"
  "v_mfma_f32_16x16x32_bf16 v[64:67], a[128:131], v[160:163], v[224:227]\n\t"
  "v_exp_f32_e64 v44, v44\n\t"
  "v_mfma_f32_16x16x32_bf16 v[64:67], a[132:135], v[164:167], v[64:67]\n\t"
  "ds_read_b128 a[144:147], v2 offset:28672\n\t"
  "v_mfma_f32_16x16x32_bf16 v[64:67], a[136:139], v[168:171], v[64:67]\n\t"
  "v_exp_f32_e64 v45, v45\n\t"
  "v_mfma_f32_16x16x32_bf16 v[64:67], a[140:143], v[172:175], v[64:67]\n\t"
  "ds_read_b128 a[148:151], v3 offset:28672\n\t"
  "v_cvt_pk_bf16_f32 v152, v44, v45\n\t"
  "v_mfma_f32_16x16x32_bf16 v[68:71], a[128:131], v[176:179], v[228:231]\n\t"
  "v_exp_f32_e64 v46, v46\n\t"
  "v_mfma_f32_16x16x32_bf16 v[68:71], a[132:135], v[180:183], v[68:71]\n\t"
  "ds_read_b128 a[152:155], v4 offset:28672\n\t"
  "v_mfma_f32_16x16x32_bf16 v[68:71], a[136:139], v[184:187], v[68:71]\n\t"
  "v_exp_f32_e64 v47, v47\n\t"
  "v_mfma_f32_16x16x32_bf16 v[68:71], a[140:143], v[188:191], v[68:71]\n\t"
  "ds_read_b128 a[156:159], v5 offset:28672\n\t"
  "v_cvt_pk_bf16_f32 v153, v46, v47\n\t"
  "v_mfma_f32_16x16x32_bf16 v[72:75], a[128:131], v[192:195], v[232:235]\n\t"
  "s_add_u32 m0, s19, 32768\n\t"
  "v_exp_f32_e64 v48, v48\n\t"
  "v_mfma_f32_16x16x32_bf16 v[72:75], a[132:135], v[196:199], v[72:75]\n\t"
  "buffer_load_dwordx4 v12, s[12:15], s20 offen offset:0 lds\n\t"
  "v_mfma_f32_16x16x32_bf16 v[72:75], a[136:139], v[200:203], v[72:75]\n\t"
  "v_exp_f32_e64 v49, v49\n\t"
  "v_mfma_f32_16x16x32_bf16 v[72:75], a[140:143], v[204:207], v[72:75]\n\t"
  "v_cvt_pk_bf16_f32 v130, v48, v49\n\t"
  "v_mfma_f32_16x16x32_bf16 v[76:79], a[128:131], v[208:211], v[236:239]\n\t"
  "v_exp_f32_e64 v50, v50\n\t"
  "v_mfma_f32_16x16x32_bf16 v[76:79], a[132:135], v[212:215], v[76:79]\n\t"
  "buffer_load_dwordx4 v13, s[12:15], s20 offen offset:1024 lds\n\t"
  "v_mfma_f32_16x16x32_bf16 v[76:79], a[136:139], v[216:219], v[76:79]\n\t"
  "s_waitcnt lgkmcnt(0)\n\t"
  "v_exp_f32_e32 v51, v51\n\t"
  "v_mfma_f32_16x16x32_bf16 v[76:79], a[140:143], v[220:223], v[76:79]\n\t"
  "v_cvt_pk_bf16_f32 v131, v50, v51\n\t"
  "v_mfma_f32_16x16x32_bf16 v[80:83], a[144:147], v[160:163], v[224:227]\n\t"
  "v_exp_f32_e64 v52, v52\n\t"
  "v_mfma_f32_16x16x32_bf16 v[80:83], a[148:151], v[164:167], v[80:83]\n\t"
  "ds_read_b64_tr_b16 a[160:161], v6 offset:0\n\t"
  "v_mfma_f32_16x16x32_bf16 v[80:83], a[152:155], v[168:171], v[80:83]\n\t"
  "v_exp_f32_e64 v53, v53\n\t"
  "v_mfma_f32_16x16x32_bf16 v[80:83], a[156:159], v[172:175], v[80:83]\n\t"
  "ds_read_b64_tr_b16 a[162:163], v6 offset:4096\n\t"
  "v_cvt_pk_bf16_f32 v138, v52, v53\n\t"
  "v_mfma_f32_16x16x32_bf16 v[84:87], a[144:147], v[176:179], v[228:231]\n\t"
  "v_exp_f32_e64 v54, v54\n\t"
  "v_mfma_f32_16x16x32_bf16 v[84:87], a[148:151], v[180:183], v[84:87]\n\t"
  "ds_read_b64_tr_b16 a[164:165], v6 offset:8192\n\t"
  "v_mfma_f32_16x16x32_bf16 v[84:87], a[152:155], v[184:187], v[84:87]\n\t"
  "v_exp_f32_e64 v55, v55\n\t"
  "v_mfma_f32_16x16x32_bf16 v[84:87], a[156:159], v[188:191], v[84:87]\n\t"
  "ds_read_b64_tr_b16 a[166:167], v6 offset:12288\n\t"
  "v_cvt_pk_bf16_f32 v139, v54, v55\n\t"
  "v_mfma_f32_16x16x32_bf16 v[88:91], a[144:147], v[192:195], v[232:235]\n\t"
  "v_exp_f32_e64 v56, v56\n\t"
  "v_mfma_f32_16x16x32_bf16 v[88:91], a[148:151], v[196:199], v[88:91]\n\t"
  "ds_read_b64_tr_b16 a[168:169], v7 offset:0\n\t"
  "v_mfma_f32_16x16x32_bf16 v[88:91], a[152:155], v[200:203], v[88:91]\n\t"
  "v_exp_f32_e64 v57, v57\n\t"
  "v_mfma_f32_16x16x32_bf16 v[88:91], a[156:159], v[204:207], v[88:91]\n\t"
  "ds_read_b64_tr_b16 a[170:171], v7 offset:4096\n\t"
  "v_cvt_pk_bf16_f32 v146, v56, v57\n\t"
  "v_mfma_f32_16x16x32_bf16 v[92:95], a[144:147], v[208:211], v[236:239]\n\t"
  "v_exp_f32_e64 v58, v58\n\t"
  "v_mfma_f32_16x16x32_bf16 v[92:95], a[148:151], v[212:215], v[92:95]\n\t"
  "ds_read_b64_tr_b16 a[172:173], v7 offset:8192\n\t"
  "v_mfma_f32_16x16x32_bf16 v[92:95], a[152:155], v[216:219], v[92:95]\n\t"
  "s_waitcnt lgkmcnt(2)\n\t"
  "v_exp_f32_e32 v59, v59\n\t"
  "v_mfma_f32_16x16x32_bf16 v[92:95], a[156:159], v[220:223], v[92:95]\n\t"
  "ds_read_b64_tr_b16 a[174:175], v7 offset:12288\n\t"
  "v_cvt_pk_bf16_f32 v147, v58, v59\n\t"
  "v_mfma_f32_16x16x32_bf16 a[0:3], a[160:163], v[96:99], a[0:3]\n\t"
  "v_exp_f32_e64 v60, v60\n\t"
  "v_mfma_f32_16x16x32_bf16 a[0:3], a[164:167], v[100:103], a[0:3]\n\t"
  "ds_read_b64_tr_b16 a[176:177], v6 offset:512\n\t"
  "v_mfma_f32_16x16x32_bf16 a[32:35], a[160:163], v[104:107], a[32:35]\n\t"
  "v_exp_f32_e64 v61, v61\n\t"
  "v_mfma_f32_16x16x32_bf16 a[32:35], a[164:167], v[108:111], a[32:35]\n\t"
  "ds_read_b64_tr_b16 a[178:179], v6 offset:4608\n\t"
  "v_cvt_pk_bf16_f32 v154, v60, v61\n\t"
  "v_mfma_f32_16x16x32_bf16 a[64:67], a[160:163], v[112:115], a[64:67]\n\t"
  "v_exp_f32_e64 v62, v62\n\t"
  "v_mfma_f32_16x16x32_bf16 a[64:67], a[164:167], v[116:119], a[64:67]\n\t"
  "ds_read_b64_tr_b16 a[180:181], v6 offset:8704\n\t"
  "v_mfma_f32_16x16x32_bf16 a[96:99], a[160:163], v[120:123], a[96:99]\n\t"
  "s_waitcnt lgkmcnt(2)\n\t"
  "v_exp_f32_e32 v63, v63\n\t"
  "v_mfma_f32_16x16x32_bf16 a[96:99], a[164:167], v[124:127], a[96:99]\n\t"
  "ds_read_b64_tr_b16 a[182:183], v6 offset:12800\n\t"
  "v_cvt_pk_bf16_f32 v155, v62, v63\n\t"
  "v_mfma_f32_16x16x32_bf16 a[4:7], a[168:171], v[96:99], a[4:7]\n\t"
  "v_exp_f32_e64 v64, v64\n\t"
  "v_mfma_f32_16x16x32_bf16 a[4:7], a[172:175], v[100:103], a[4:7]\n\t"
  "ds_read_b64_tr_b16 a[184:185], v7 offset:512\n\t"
  "v_mfma_f32_16x16x32_bf16 a[36:39], a[168:171], v[104:107], a[36:39]\n\t"
  "v_exp_f32_e64 v65, v65\n\t"
  "v_mfma_f32_16x16x32_bf16 a[36:39], a[172:175], v[108:111], a[36:39]\n\t"
  "ds_read_b64_tr_b16 a[186:187], v7 offset:4608\n\t"
  "v_cvt_pk_bf16_f32 v132, v64, v65\n\t"
  "v_mfma_f32_16x16x32_bf16 a[68:71], a[168:171], v[112:115], a[68:71]\n\t"
  "v_exp_f32_e64 v66, v66\n\t"
  "v_mfma_f32_16x16x32_bf16 a[68:71], a[172:175], v[116:119], a[68:71]\n\t"
  "ds_read_b64_tr_b16 a[188:189], v7 offset:8704\n\t"
  "v_mfma_f32_16x16x32_bf16 a[100:103], a[168:171], v[120:123], a[100:103]\n\t"
  "s_waitcnt lgkmcnt(2)\n\t"
  "v_exp_f32_e32 v67, v67\n\t"
  "v_mfma_f32_16x16x32_bf16 a[100:103], a[172:175], v[124:127], a[100:103]\n\t"
  "ds_read_b64_tr_b16 a[190:191], v7 offset:12800\n\t"
  "v_cvt_pk_bf16_f32 v133, v66, v67\n\t"
  "v_mfma_f32_16x16x32_bf16 a[8:11], a[176:179], v[96:99], a[8:11]\n\t"
  "v_exp_f32_e64 v68, v68\n\t"
  "v_mfma_f32_16x16x32_bf16 a[8:11], a[180:183], v[100:103], a[8:11]\n\t"
  "ds_read_b64_tr_b16 a[160:161], v6 offset:1024\n\t"
  "v_mfma_f32_16x16x32_bf16 a[40:43], a[176:179], v[104:107], a[40:43]\n\t"
  "v_exp_f32_e64 v69, v69\n\t"
  "v_mfma_f32_16x16x32_bf16 a[40:43], a[180:183], v[108:111], a[40:43]\n\t"
  "ds_read_b64_tr_b16 a[162:163], v6 offset:5120\n\t"
  "v_cvt_pk_bf16_f32 v140, v68, v69\n\t"
  "v_mfma_f32_16x16x32_bf16 a[72:75], a[176:179], v[112:115], a[72:75]\n\t"
  "v_exp_f32_e64 v70, v70\n\t"
  "v_mfma_f32_16x16x32_bf16 a[72:75], a[180:183], v[116:119], a[72:75]\n\t"
  "ds_read_b64_tr_b16 a[164:165], v6 offset:9216\n\t"
  "v_mfma_f32_16x16x32_bf16 a[104:107], a[176:179], v[120:123], a[104:107]\n\t"
  "s_waitcnt lgkmcnt(2)\n\t"
  "v_exp_f32_e32 v71, v71\n\t"
  "v_mfma_f32_16x16x32_bf16 a[104:107], a[180:183], v[124:127], a[104:107]\n\t"
  "ds_read_b64_tr_b16 a[166:167], v6 offset:13312\n\t"
  "v_cvt_pk_bf16_f32 v141, v70, v71\n\t"
  "v_mfma_f32_16x16x32_bf16 a[12:15], a[184:187], v[96:99], a[12:15]\n\t"
  "v_exp_f32_e64 v72, v72\n\t"
  "v_mfma_f32_16x16x32_bf16 a[12:15], a[188:191], v[100:103], a[12:15]\n\t"
  "ds_read_b64_tr_b16 a[168:169], v7 offset:1024\n\t"
  "v_mfma_f32_16x16x32_bf16 a[44:47], a[184:187], v[104:107], a[44:47]\n\t"
  "v_exp_f32_e64 v73, v73\n\t"
  "v_mfma_f32_16x16x32_bf16 a[44:47], a[188:191], v[108:111], a[44:47]\n\t"
  "ds_read_b64_tr_b16 a[170:171], v7 offset:5120\n\t"
  "v_cvt_pk_bf16_f32 v148, v72, v73\n\t"
  "v_mfma_f32_16x16x32_bf16 a[76:79], a[184:187], v[112:115], a[76:79]\n\t"
  "v_exp_f32_e64 v74, v74\n\t"
  "v_mfma_f32_16x16x32_bf16 a[76:79], a[188:191], v[116:119], a[76:79]\n\t"
  "ds_read_b64_tr_b16 a[172:173], v7 offset:9216\n\t"
  "v_mfma_f32_16x16x32_bf16 a[108:111], a[184:187], v[120:123], a[108:111]\n\t"
  "s_waitcnt lgkmcnt(2)\n\t"
  "v_exp_f32_e32 v75, v75\n\t"
  "v_mfma_f32_16x16x32_bf16 a[108:111], a[188:191], v[124:127], a[108:111]\n\t"
  "ds_read_b64_tr_b16 a[174:175], v7 offset:13312\n\t"
  "v_cvt_pk_bf16_f32 v149, v74, v75\n\t"
  "v_mfma_f32_16x16x32_bf16 a[16:19], a[160:163], v[96:99], a[16:19]\n\t"
  "v_exp_f32_e64 v76, v76\n\t"
  "v_mfma_f32_16x16x32_bf16 a[16:19], a[164:167], v[100:103], a[16:19]\n\t"
  "ds_read_b64_tr_b16 a[176:177], v6 offset:1536\n\t"
  "v_mfma_f32_16x16x32_bf16 a[48:51], a[160:163], v[104:107], a[48:51]\n\t"
  "v_exp_f32_e64 v77, v77\n\t"
  "v_mfma_f32_16x16x32_bf16 a[48:51], a[164:167], v[108:111], a[48:51]\n\t"
  "ds_read_b64_tr_b16 a[178:179], v6 offset:5632\n\t"
  "v_cvt_pk_bf16_f32 v156, v76, v77\n\t"
  "v_mfma_f32_16x16x32_bf16 a[80:83], a[160:163], v[112:115], a[80:83]\n\t"
  "v_exp_f32_e64 v78, v78\n\t"
  "v_mfma_f32_16x16x32_bf16 a[80:83], a[164:167], v[116:119], a[80:83]\n\t"
  "ds_read_b64_tr_b16 a[180:181], v6 offset:9728\n\t"
  "v_mfma_f32_16x16x32_bf16 a[112:115], a[160:163], v[120:123], a[112:115]\n\t"
  "s_waitcnt lgkmcnt(2)\n\t"
  "v_exp_f32_e32 v79, v79\n\t"
  "v_mfma_f32_16x16x32_bf16 a[112:115], a[164:167], v[124:127], a[112:115]\n\t"
  "ds_read_b64_tr_b16 a[182:183], v6 offset:13824\n\t"
  "v_cvt_pk_bf16_f32 v157, v78, v79\n\t"
  "v_mfma_f32_16x16x32_bf16 a[20:23], a[168:171], v[96:99], a[20:23]\n\t"
  "v_exp_f32_e64 v80, v80\n\t"
  "v_mfma_f32_16x16x32_bf16 a[20:23], a[172:175], v[100:103], a[20:23]\n\t"
  "ds_read_b64_tr_b16 a[184:185], v7 offset:1536\n\t"
  "v_mfma_f32_16x16x32_bf16 a[52:55], a[168:171], v[104:107], a[52:55]\n\t"
  "v_exp_f32_e64 v81, v81\n\t"
  "v_mfma_f32_16x16x32_bf16 a[52:55], a[172:175], v[108:111], a[52:55]\n\t"
  "ds_read_b64_tr_b16 a[186:187], v7 offset:5632\n\t"
  "v_cvt_pk_bf16_f32 v134, v80, v81\n\t"
  "v_mfma_f32_16x16x32_bf16 a[84:87], a[168:171], v[112:115], a[84:87]\n\t"
  "v_exp_f32_e64 v82, v82\n\t"
  "v_mfma_f32_16x16x32_bf16 a[84:87], a[172:175], v[116:119], a[84:87]\n\t"
  "ds_read_b64_tr_b16 a[188:189], v7 offset:9728\n\t"
  "v_mfma_f32_16x16x32_bf16 a[116:119], a[168:171], v[120:123], a[116:119]\n\t"
  "s_waitcnt lgkmcnt(2)\n\t"
  "v_exp_f32_e32 v83, v83\n\t"
  "v_mfma_f32_16x16x32_bf16 a[116:119], a[172:175], v[124:127], a[116:119]\n\t"
  "ds_read_b64_tr_b16 a[190:191], v7 offset:13824\n\t"
  "v_cvt_pk_bf16_f32 v135, v82, v83\n\t"
  "v_mfma_f32_16x16x32_bf16 a[24:27], a[176:179], v[96:99], a[24:27]\n\t"
  "v_exp_f32_e64 v84, v84\n\t"
  "v_mfma_f32_16x16x32_bf16 a[24:27], a[180:183], v[100:103], a[24:27]\n\t"
  "ds_read_b128 a[128:131], v2 offset:32768\n\t"
  "v_mfma_f32_16x16x32_bf16 a[56:59], a[176:179], v[104:107], a[56:59]\n\t"
  "v_exp_f32_e64 v85, v85\n\t"
  "v_mfma_f32_16x16x32_bf16 a[56:59], a[180:183], v[108:111], a[56:59]\n\t"
  "ds_read_b128 a[132:135], v3 offset:32768\n\t"
  "v_cvt_pk_bf16_f32 v142, v84, v85\n\t"
  "v_mfma_f32_16x16x32_bf16 a[88:91], a[176:179], v[112:115], a[88:91]\n\t"
  "v_exp_f32_e64 v86, v86\n\t"
  "v_mfma_f32_16x16x32_bf16 a[88:91], a[180:183], v[116:119], a[88:91]\n\t"
  "ds_read_b128 a[136:139], v4 offset:32768\n\t"
  "v_mfma_f32_16x16x32_bf16 a[120:123], a[176:179], v[120:123], a[120:123]\n\t"
  "s_waitcnt lgkmcnt(2)\n\t"
  "v_exp_f32_e32 v87, v87\n\t"
  "v_mfma_f32_16x16x32_bf16 a[120:123], a[180:183], v[124:127], a[120:123]\n\t"
  "ds_read_b128 a[140:143], v5 offset:32768\n\t"
  "v_cvt_pk_bf16_f32 v143, v86, v87\n\t"
  "v_mfma_f32_16x16x32_bf16 a[28:31], a[184:187], v[96:99], a[28:31]\n\t"
  "v_exp_f32_e64 v88, v88\n\t"
  "v_mfma_f32_16x16x32_bf16 a[28:31], a[188:191], v[100:103], a[28:31]\n\t"
  "buffer_load_dwordx4 v12, s[12:15], s20 offen offset:2048 lds\n\t"
  "v_mfma_f32_16x16x32_bf16 a[60:63], a[184:187], v[104:107], a[60:63]\n\t"
  "v_exp_f32_e64 v89, v89\n\t"
  "v_mfma_f32_16x16x32_bf16 a[60:63], a[188:191], v[108:111], a[60:63]\n\t"
  "v_cvt_pk_bf16_f32 v150, v88, v89\n\t"
  "v_mfma_f32_16x16x32_bf16 a[92:95], a[184:187], v[112:115], a[92:95]\n\t"
  "v_exp_f32_e64 v90, v90\n\t"
  "v_mfma_f32_16x16x32_bf16 a[92:95], a[188:191], v[116:119], a[92:95]\n\t"
  "buffer_load_dwordx4 v13, s[12:15], s20 offen offset:3072 lds\n\t"
  "v_mfma_f32_16x16x32_bf16 a[124:127], a[184:187], v[120:123], a[124:127]\n\t"
  "s_add_u32 s20, s20, 0x4000\n\t"
  "v_exp_f32_e64 v91, v91\n\t"
  "v_mfma_f32_16x16x32_bf16 a[124:127], a[188:191], v[124:127], a[124:127]\n\t"
  "v_cvt_pk_bf16_f32 v151, v90, v91\n\t"
  "v_mfma_f32_16x16x32_bf16 a[192:195], a[208:211], v[96:99], a[192:195]\n\t"
  "v_exp_f32_e64 v92, v92\n\t"
  "v_mfma_f32_16x16x32_bf16 a[192:195], a[208:211], v[100:103], a[192:195]\n\t"
  "v_mfma_f32_16x16x32_bf16 a[196:199], a[208:211], v[104:107], a[196:199]\n\t"
  "v_exp_f32_e64 v93, v93\n\t"
  "v_mfma_f32_16x16x32_bf16 a[196:199], a[208:211], v[108:111], a[196:199]\n\t"
  "v_cvt_pk_bf16_f32 v158, v92, v93\n\t"
  "v_mfma_f32_16x16x32_bf16 a[200:203], a[208:211], v[112:115], a[200:203]\n\t"
  "v_exp_f32_e64 v94, v94\n\t"
  "v_mfma_f32_16x16x32_bf16 a[200:203], a[208:211], v[116:119], a[200:203]\n\t"
  "v_mfma_f32_16x16x32_bf16 a[204:207], a[208:211], v[120:123], a[204:207]\n\t"
  "v_exp_f32_e64 v95, v95\n\t"
  "v_mfma_f32_16x16x32_bf16 a[204:207], a[208:211], v[124:127], a[204:207]\n\t"
  "v_cvt_pk_bf16_f32 v159, v94, v95\n\t"
  "L_loop1_%=:\n\t"
  "s_waitcnt vmcnt(8)\n\t"
  "s_barrier\n\t"
  "v_mfma_f32_16x16x32_bf16 v[32:35], a[128:131], v[160:163], v[224:227]\n\t"
  "s_waitcnt lgkmcnt(0)\n\t"
  "s_nop 0\n\t"
  "v_mfma_f32_16x16x32_bf16 v[32:35], a[132:135], v[164:167], v[32:35]\n\t"
  "ds_read_b128 a[144:147], v2 offset:36864\n\t"
  "v_mfma_f32_16x16x32_bf16 v[32:35], a[136:139], v[168:171], v[32:35]\n\t"
  "v_mfma_f32_16x16x32_bf16 v[32:35], a[140:143], v[172:175], v[32:35]\n\t"
  "ds_read_b128 a[148:151], v3 offset:36864\n\t"
  "v_mfma_f32_16x16x32_bf16 v[36:39], a[128:131], v[176:179], v[228:231]\n\t"
  "v_mfma_f32_16x16x32_bf16 v[36:39], a[132:135], v[180:183], v[36:39]\n\t"
  "ds_read_b128 a[152:155], v4 offset:36864\n\t"
  "v_mfma_f32_16x16x32_bf16 v[36:39], a[136:139], v[184:187], v[36:39]\n\t"
  "v_mfma_f32_16x16x32_bf16 v[36:39], a[140:143], v[188:191], v[36:39]\n\t"
  "ds_read_b128 a[156:159], v5 offset:36864\n\t"
  "v_mfma_f32_16x16x32_bf16 v[40:43], a[128:131], v[192:195], v[232:235]\n\t"
  "s_add_u32 m0, s18, 16384\n\t"
  "v_exp_f32_e64 v32, v32\n\t"
  "v_mfma_f32_16x16x32_bf16 v[40:43], a[132:135], v[196:199], v[40:43]\n\t"
  "buffer_load_dwordx4 v8, s[8:11], s16 offen offset:0 lds\n\t"
  "v_mfma_f32_16x16x32_bf16 v[40:43], a[136:139], v[200:203], v[40:43]\n\t"
  "v_exp_f32_e64 v33, v33\n\t"
  "v_mfma_f32_16x16x32_bf16 v[40:43], a[140:143], v[204:207], v[40:43]\n\t"
  "v_cvt_pk_bf16_f32 v96, v32, v33\n\t"
  "v_mfma_f32_16x16x32_bf16 v[44:47], a[128:131], v[208:211], v[236:239]\n\t"
  "v_exp_f32_e64 v34, v34\n\t"
  "v_mfma_f32_16x16x32_bf16 v[44:47], a[132:135], v[212:215], v[44:47]\n\t"
  "buffer_load_dwordx4 v9, s[8:11], s16 offen offset:1024 lds\n\t"
  "v_mfma_f32_16x16x32_bf16 v[44:47], a[136:139], v[216:219], v[44:47]\n\t"
  "s_waitcnt lgkmcnt(0)\n\t"
  "v_exp_f32_e32 v35, v35\n\t"
  "v_mfma_f32_16x16x32_bf16 v[44:47], a[140:143], v[220:223], v[44:47]\n\t"
  "v_cvt_pk_bf16_f32 v97, v34, v35\n\t"
  "v_mfma_f32_16x16x32_bf16 v[48:51], a[144:147], v[160:163], v[224:227]\n\t"
  "v_exp_f32_e64 v36, v36\n\t"
  "v_mfma_f32_16x16x32_bf16 v[48:51], a[148:151], v[164:167], v[48:51]\n\t"
  "ds_read_b128 a[128:131], v2 offset:40960\n\t"
  "v_mfma_f32_16x16x32_bf16 v[48:51], a[152:155], v[168:171], v[48:51]\n\t"
  "v_exp_f32_e64 v37, v37\n\t"
  "v_mfma_f32_16x16x32_bf16 v[48:51], a[156:159], v[172:175], v[48:51]\n\t"
  "ds_read_b128 a[132:135], v3 offset:40960\n\t"
  "v_cvt_pk_bf16_f32 v104, v36, v37\n\t"
  "v_mfma_f32_16x16x32_bf16 v[52:55], a[144:147], v[176:179], v[228:231]\n\t"
  "v_exp_f32_e64 v38, v38\n\t"
  "v_mfma_f32_16x16x32_bf16 v[52:55], a[148:151], v[180:183], v[52:55]\n\t"
  "ds_read_b128 a[136:139], v4 offset:40960\n\t"
  "v_mfma_f32_16x16x32_bf16 v[52:55], a[152:155], v[184:187], v[52:55]\n\t"
  "v_exp_f32_e64 v39, v39\n\t"
  "v_mfma_f32_16x16x32_bf16 v[52:55], a[156:159], v[188:191], v[52:55]\n\t"
  "ds_read_b128 a[140:143], v5 offset:40960\n\t"
  "v_cvt_pk_bf16_f32 v105, v38, v39\n\t"
  "v_mfma_f32_16x16x32_bf16 v[56:59], a[144:147], v[192:195], v[232:235]\n\t"
  "v_exp_f32_e64 v40, v40\n\t"
  "v_mfma_f32_16x16x32_bf16 v[56:59], a[148:151], v[196:199], v[56:59]\n\t"
  "buffer_load_dwordx4 v10, s[8:11], s16 offen offset:2048 lds\n\t"
  "v_mfma_f32_16x16x32_bf16 v[56:59], a[152:155], v[200:203], v[56:59]\n\t"
  "v_exp_f32_e64 v41, v41\n\t"
  "v_mfma_f32_16x16x32_bf16 v[56:59], a[156:159], v[204:207], v[56:59]\n\t"
  "v_cvt_pk_bf16_f32 v112, v40, v41\n\t"
  "v_mfma_f32_16x16x32_bf16 v[60:63], a[144:147], v[208:211], v[236:239]\n\t"
  "v_exp_f32_e64 v42, v42\n\t"
  "v_mfma_f32_16x16x32_bf16 v[60:63], a[148:151], v[212:215], v[60:63]\n\t"
  "buffer_load_dwordx4 v11, s[8:11], s16 offen offset:3072 lds\n\t"
  "v_mfma_f32_16x16x32_bf16 v[60:63], a[152:155], v[216:219], v[60:63]\n\t"
  "s_add_u32 s16, s16, 0x4000\n\t"
  "v_exp_f32_e64 v43, v43\n\t"
  "v_mfma_f32_16x16x32_bf16 v[60:63], a[156:159], v[220:223], v[60:63]\n\t"
  "s_waitcnt lgkmcnt(0)\n\t"
  "s_nop 0\n\t"
  "v_cvt_pk_bf16_f32 v113, v42, v43\n\t"
  "v_mfma_f32_16x16x32_bf16 v[64:67], a[128:131], v[160:163], v[224:227]\n\t"
  "v_exp_f32_e64 v44, v44\n\t"
  "v_mfma_f32_16x16x32_bf16 v[64:67], a[132:135], v[164:167], v[64:67]\n\t"
  "ds_read_b128 a[144:147], v2 offset:45056\n\t"
  "v_mfma_f32_16x16x32_bf16 v[64:67], a[136:139], v[168:171], v[64:67]\n\t"
  "v_exp_f32_e64 v45, v45\n\t"
  "v_mfma_f32_16x16x32_bf16 v[64:67], a[140:143], v[172:175], v[64:67]\n\t"
  "ds_read_b128 a[148:151], v3 offset:45056\n\t"
  "v_cvt_pk_bf16_f32 v120, v44, v45\n\t"
  "v_mfma_f32_16x16x32_bf16 v[68:71], a[128:131], v[176:179], v[228:231]\n\t"
  "v_exp_f32_e64 v46, v46\n\t"
  "v_mfma_f32_16x16x32_bf16 v[68:71], a[132:135], v[180:183], v[68:71]\n\t"
  "ds_read_b128 a[152:155], v4 offset:45056\n\t"
  "v_mfma_f32_16x16x32_bf16 v[68:71], a[136:139], v[184:187], v[68:71]\n\t"
  "v_exp_f32_e64 v47, v47\n\t"
  "v_mfma_f32_16x16x32_bf16 v[68:71], a[140:143], v[188:191], v[68:71]\n\t"
  "ds_read_b128 a[156:159], v5 offset:45056\n\t"
  "v_cvt_pk_bf16_f32 v121, v46, v47\n\t"
  "v_mfma_f32_16x16x32_bf16 v[72:75], a[128:131], v[192:195], v[232:235]\n\t"
  "s_add_u32 m0, s19, 49152\n\t"
  "v_exp_f32_e64 v48, v48\n\t"
  "v_mfma_f32_16x16x32_bf16 v[72:75], a[132:135], v[196:199], v[72:75]\n\t"
  "buffer_load_dwordx4 v12, s[12:15], s20 offen offset:0 lds\n\t"
  "v_mfma_f32_16x16x32_bf16 v[72:75], a[136:139], v[200:203], v[72:75]\n\t"
  "v_exp_f32_e64 v49, v49\n\t"
  "v_mfma_f32_16x16x32_bf16 v[72:75], a[140:143], v[204:207], v[72:75]\n\t"
  "v_cvt_pk_bf16_f32 v98, v48, v49\n\t"
  "v_mfma_f32_16x16x32_bf16 v[76:79], a[128:131], v[208:211], v[236:239]\n\t"
  "v_exp_f32_e64 v50, v50\n\t"
  "v_mfma_f32_16x16x32_bf16 v[76:79], a[132:135], v[212:215], v[76:79]\n\t"
  "buffer_load_dwordx4 v13, s[12:15], s20 offen offset:1024 lds\n\t"
  "v_mfma_f32_16x16x32_bf16 v[76:79], a[136:139], v[216:219], v[76:79]\n\t"
  "s_waitcnt lgkmcnt(0)\n\t"
  "v_exp_f32_e32 v51, v51\n\t"
  "v_mfma_f32_16x16x32_bf16 v[76:79], a[140:143], v[220:223], v[76:79]\n\t"
  "v_cvt_pk_bf16_f32 v99, v50, v51\n\t"
  "v_mfma_f32_16x16x32_bf16 v[80:83], a[144:147], v[160:163], v[224:227]\n\t"
  "v_exp_f32_e64 v52, v52\n\t"
  "v_mfma_f32_16x16x32_bf16 v[80:83], a[148:151], v[164:167], v[80:83]\n\t"
  "ds_read_b64_tr_b16 a[160:161], v6 offset:16384\n\t"
  "v_mfma_f32_16x16x32_bf16 v[80:83], a[152:155], v[168:171], v[80:83]\n\t"
  "v_exp_f32_e64 v53, v53\n\t"
  "v_mfma_f32_16x16x32_bf16 v[80:83], a[156:159], v[172:175], v[80:83]\n\t"
  "ds_read_b64_tr_b16 a[162:163], v6 offset:20480\n\t"
  "v_cvt_pk_bf16_f32 v106, v52, v53\n\t"
  "v_mfma_f32_16x16x32_bf16 v[84:87], a[144:147], v[176:179], v[228:231]\n\t"
  "v_exp_f32_e64 v54, v54\n\t"
  "v_mfma_f32_16x16x32_bf16 v[84:87], a[148:151], v[180:183], v[84:87]\n\t"
  "ds_read_b64_tr_b16 a[164:165], v6 offset:24576\n\t"
  "v_mfma_f32_16x16x32_bf16 v[84:87], a[152:155], v[184:187], v[84:87]\n\t"
  "v_exp_f32_e64 v55, v55\n\t"
  "v_mfma_f32_16x16x32_bf16 v[84:87], a[156:159], v[188:191], v[84:87]\n\t"
  "ds_read_b64_tr_b16 a[166:167], v6 offset:28672\n\t"
  "v_cvt_pk_bf16_f32 v107, v54, v55\n\t"
  "v_mfma_f32_16x16x32_bf16 v[88:91], a[144:147], v[192:195], v[232:235]\n\t"
  "v_exp_f32_e64 v56, v56\n\t"
  "v_mfma_f32_16x16x32_bf16 v[88:91], a[148:151], v[196:199], v[88:91]\n\t"
  "ds_read_b64_tr_b16 a[168:169], v7 offset:16384\n\t"
  "v_mfma_f32_16x16x32_bf16 v[88:91], a[152:155], v[200:203], v[88:91]\n\t"
  "v_exp_f32_e64 v57, v57\n\t"
  "v_mfma_f32_16x16x32_bf16 v[88:91], a[156:159], v[204:207], v[88:91]\n\t"
  "ds_read_b64_tr_b16 a[170:171], v7 offset:20480\n\t"
  "v_cvt_pk_bf16_f32 v114, v56, v57\n\t"
  "v_mfma_f32_16x16x32_bf16 v[92:95], a[144:147], v[208:211], v[236:239]\n\t"
  "v_exp_f32_e64 v58, v58\n\t"
  "v_mfma_f32_16x16x32_bf16 v[92:95], a[148:151], v[212:215], v[92:95]\n\t"
  "ds_read_b64_tr_b16 a[172:173], v7 offset:24576\n\t"
  "v_mfma_f32_16x16x32_bf16 v[92:95], a[152:155], v[216:219], v[92:95]\n\t"
  "s_waitcnt lgkmcnt(2)\n\t"
  "v_exp_f32_e32 v59, v59\n\t"
  "v_mfma_f32_16x16x32_bf16 v[92:95], a[156:159], v[220:223], v[92:95]\n\t"
  "ds_read_b64_tr_b16 a[174:175], v7 offset:28672\n\t"
  "v_cvt_pk_bf16_f32 v115, v58, v59\n\t"
  "v_mfma_f32_16x16x32_bf16 a[0:3], a[160:163], v[128:131], a[0:3]\n\t"
  "v_exp_f32_e64 v60, v60\n\t"
  "v_mfma_f32_16x16x32_bf16 a[0:3], a[164:167], v[132:135], a[0:3]\n\t"
  "ds_read_b64_tr_b16 a[176:177], v6 offset:16896\n\t"
  "v_mfma_f32_16x16x32_bf16 a[32:35], a[160:163], v[136:139], a[32:35]\n\t"
  "v_exp_f32_e64 v61, v61\n\t"
  "v_mfma_f32_16x16x32_bf16 a[32:35], a[164:167], v[140:143], a[32:35]\n\t"
  "ds_read_b64_tr_b16 a[178:179], v6 offset:20992\n\t"
  "v_cvt_pk_bf16_f32 v122, v60, v61\n\t"
  "v_mfma_f32_16x16x32_bf16 a[64:67], a[160:163], v[144:147], a[64:67]\n\t"
  "v_exp_f32_e64 v62, v62\n\t"
  "v_mfma_f32_16x16x32_bf16 a[64:67], a[164:167], v[148:151], a[64:67]\n\t"
  "ds_read_b64_tr_b16 a[180:181], v6 offset:25088\n\t"
  "v_mfma_f32_16x16x32_bf16 a[96:99], a[160:163], v[152:155], a[96:99]\n\t"
  "s_waitcnt lgkmcnt(2)\n\t"
  "v_exp_f32_e32 v63, v63\n\t"
  "v_mfma_f32_16x16x32_bf16 a[96:99], a[164:167], v[156:159], a[96:99]\n\t"
  "ds_read_b64_tr_b16 a[182:183], v6 offset:29184\n\t"
  "v_cvt_pk_bf16_f32 v123, v62, v63\n\t"
  "v_mfma_f32_16x16x32_bf16 a[4:7], a[168:171], v[128:131], a[4:7]\n\t"
  "v_exp_f32_e64 v64, v64\n\t"
  "v_mfma_f32_16x16x32_bf16 a[4:7], a[172:175], v[132:135], a[4:7]\n\t"
  "ds_read_b64_tr_b16 a[184:185], v7 offset:16896\n\t"
  "v_mfma_f32_16x16x32_bf16 a[36:39], a[168:171], v[136:139], a[36:39]\n\t"
  "v_exp_f32_e64 v65, v65\n\t"
  "v_mfma_f32_16x16x32_bf16 a[36:39], a[172:175], v[140:143], a[36:39]\n\t"
  "ds_read_b64_tr_b16 a[186:187], v7 offset:20992\n\t"
  "v_cvt_pk_bf16_f32 v100, v64, v65\n\t"
  "v_mfma_f32_16x16x32_bf16 a[68:71], a[168:171], v[144:147], a[68:71]\n\t"
  "v_exp_f32_e64 v66, v66\n\t"
  "v_mfma_f32_16x16x32_bf16 a[68:71], a[172:175], v[148:151], a[68:71]\n\t"
  "ds_read_b64_tr_b16 a[188:189], v7 offset:25088\n\t"
  "v_mfma_f32_16x16x32_bf16 a[100:103], a[168:171], v[152:155], a[100:103]\n\t"
  "s_waitcnt lgkmcnt(2)\n\t"
  "v_exp_f32_e32 v67, v67\n\t"
  "v_mfma_f32_16x16x32_bf16 a[100:103], a[172:175], v[156:159], a[100:103]\n\t"
  "ds_read_b64_tr_b16 a[190:191], v7 offset:29184\n\t"
  "v_cvt_pk_bf16_f32 v101, v66, v67\n\t"
  "v_mfma_f32_16x16x32_bf16 a[8:11], a[176:179], v[128:131], a[8:11]\n\t"
  "v_exp_f32_e64 v68, v68\n\t"
  "v_mfma_f32_16x16x32_bf16 a[8:11], a[180:183], v[132:135], a[8:11]\n\t"
  "ds_read_b64_tr_b16 a[160:161], v6 offset:17408\n\t"
  "v_mfma_f32_16x16x32_bf16 a[40:43], a[176:179], v[136:139], a[40:43]\n\t"
  "v_exp_f32_e64 v69, v69\n\t"
  "v_mfma_f32_16x16x32_bf16 a[40:43], a[180:183], v[140:143], a[40:43]\n\t"
  "ds_read_b64_tr_b16 a[162:163], v6 offset:21504\n\t"
  "v_cvt_pk_bf16_f32 v108, v68, v69\n\t"
  "v_mfma_f32_16x16x32_bf16 a[72:75], a[176:179], v[144:147], a[72:75]\n\t"
  "v_exp_f32_e64 v70, v70\n\t"
  "v_mfma_f32_16x16x32_bf16 a[72:75], a[180:183], v[148:151], a[72:75]\n\t"
  "ds_read_b64_tr_b16 a[164:165], v6 offset:25600\n\t"
  "v_mfma_f32_16x16x32_bf16 a[104:107], a[176:179], v[152:155], a[104:107]\n\t"
  "s_waitcnt lgkmcnt(2)\n\t"
  "v_exp_f32_e32 v71, v71\n\t"
  "v_mfma_f32_16x16x32_bf16 a[104:107], a[180:183], v[156:159], a[104:107]\n\t"
  "ds_read_b64_tr_b16 a[166:167], v6 offset:29696\n\t"
  "v_cvt_pk_bf16_f32 v109, v70, v71\n\t"
  "v_mfma_f32_16x16x32_bf16 a[12:15], a[184:187], v[128:131], a[12:15]\n\t"
  "v_exp_f32_e64 v72, v72\n\t"
  "v_mfma_f32_16x16x32_bf16 a[12:15], a[188:191], v[132:135], a[12:15]\n\t"
  "ds_read_b64_tr_b16 a[168:169], v7 offset:17408\n\t"
  "v_mfma_f32_16x16x32_bf16 a[44:47], a[184:187], v[136:139], a[44:47]\n\t"
  "v_exp_f32_e64 v73, v73\n\t"
  "v_mfma_f32_16x16x32_bf16 a[44:47], a[188:191], v[140:143], a[44:47]\n\t"
  "ds_read_b64_tr_b16 a[170:171], v7 offset:21504\n\t"
  "v_cvt_pk_bf16_f32 v116, v72, v73\n\t"
  "v_mfma_f32_16x16x32_bf16 a[76:79], a[184:187], v[144:147], a[76:79]\n\t"
  "v_exp_f32_e64 v74, v74\n\t"
  "v_mfma_f32_16x16x32_bf16 a[76:79], a[188:191], v[148:151], a[76:79]\n\t"
  "ds_read_b64_tr_b16 a[172:173], v7 offset:25600\n\t"
  "v_mfma_f32_16x16x32_bf16 a[108:111], a[184:187], v[152:155], a[108:111]\n\t"
  "s_waitcnt lgkmcnt(2)\n\t"
  "v_exp_f32_e32 v75, v75\n\t"
  "v_mfma_f32_16x16x32_bf16 a[108:111], a[188:191], v[156:159], a[108:111]\n\t"
  "ds_read_b64_tr_b16 a[174:175], v7 offset:29696\n\t"
  "v_cvt_pk_bf16_f32 v117, v74, v75\n\t"
  "v_mfma_f32_16x16x32_bf16 a[16:19], a[160:163], v[128:131], a[16:19]\n\t"
  "v_exp_f32_e64 v76, v76\n\t"
  "v_mfma_f32_16x16x32_bf16 a[16:19], a[164:167], v[132:135], a[16:19]\n\t"
  "ds_read_b64_tr_b16 a[176:177], v6 offset:17920\n\t"
  "v_mfma_f32_16x16x32_bf16 a[48:51], a[160:163], v[136:139], a[48:51]\n\t"
  "v_exp_f32_e64 v77, v77\n\t"
  "v_mfma_f32_16x16x32_bf16 a[48:51], a[164:167], v[140:143], a[48:51]\n\t"
  "ds_read_b64_tr_b16 a[178:179], v6 offset:22016\n\t"
  "v_cvt_pk_bf16_f32 v124, v76, v77\n\t"
  "v_mfma_f32_16x16x32_bf16 a[80:83], a[160:163], v[144:147], a[80:83]\n\t"
  "v_exp_f32_e64 v78, v78\n\t"
  "v_mfma_f32_16x16x32_bf16 a[80:83], a[164:167], v[148:151], a[80:83]\n\t"
  "ds_read_b64_tr_b16 a[180:181], v6 offset:26112\n\t"
  "v_mfma_f32_16x16x32_bf16 a[112:115], a[160:163], v[152:155], a[112:115]\n\t"
  "s_waitcnt lgkmcnt(2)\n\t"
  "v_exp_f32_e32 v79, v79\n\t"
  "v_mfma_f32_16x16x32_bf16 a[112:115], a[164:167], v[156:159], a[112:115]\n\t"
  "ds_read_b64_tr_b16 a[182:183], v6 offset:30208\n\t"
  "v_cvt_pk_bf16_f32 v125, v78, v79\n\t"
  "v_mfma_f32_16x16x32_bf16 a[20:23], a[168:171], v[128:131], a[20:23]\n\t"
  "v_exp_f32_e64 v80, v80\n\t"
  "v_mfma_f32_16x16x32_bf16 a[20:23], a[172:175], v[132:135], a[20:23]\n\t"
  "ds_read_b64_tr_b16 a[184:185], v7 offset:17920\n\t"
  "v_mfma_f32_16x16x32_bf16 a[52:55], a[168:171], v[136:139], a[52:55]\n\t"
  "v_exp_f32_e64 v81, v81\n\t"
  "v_mfma_f32_16x16x32_bf16 a[52:55], a[172:175], v[140:143], a[52:55]\n\t"
  "ds_read_b64_tr_b16 a[186:187], v7 offset:22016\n\t"
  "v_cvt_pk_bf16_f32 v102, v80, v81\n\t"
  "v_mfma_f32_16x16x32_bf16 a[84:87], a[168:171], v[144:147], a[84:87]\n\t"
  "v_exp_f32_e64 v82, v82\n\t"
  "v_mfma_f32_16x16x32_bf16 a[84:87], a[172:175], v[148:151], a[84:87]\n\t"
  "ds_read_b64_tr_b16 a[188:189], v7 offset:26112\n\t"
  "v_mfma_f32_16x16x32_bf16 a[116:119], a[168:171], v[152:155], a[116:119]\n\t"
  "s_waitcnt lgkmcnt(2)\n\t"
  "v_exp_f32_e32 v83, v83\n\t"
  "v_mfma_f32_16x16x32_bf16 a[116:119], a[172:175], v[156:159], a[116:119]\n\t"
  "ds_read_b64_tr_b16 a[190:191], v7 offset:30208\n\t"
  "v_cvt_pk_bf16_f32 v103, v82, v83\n\t"
  "v_mfma_f32_16x16x32_bf16 a[24:27], a[176:179], v[128:131], a[24:27]\n\t"
  "v_exp_f32_e64 v84, v84\n\t"
  "v_mfma_f32_16x16x32_bf16 a[24:27], a[180:183], v[132:135], a[24:27]\n\t"
  "ds_read_b128 a[128:131], v2 offset:49152\n\t"
  "v_mfma_f32_16x16x32_bf16 a[56:59], a[176:179], v[136:139], a[56:59]\n\t"
  "v_exp_f32_e64 v85, v85\n\t"
  "v_mfma_f32_16x16x32_bf16 a[56:59], a[180:183], v[140:143], a[56:59]\n\t"
  "ds_read_b128 a[132:135], v3 offset:49152\n\t"
  "v_cvt_pk_bf16_f32 v110, v84, v85\n\t"
  "v_mfma_f32_16x16x32_bf16 a[88:91], a[176:179], v[144:147], a[88:91]\n\t"
  "v_exp_f32_e64 v86, v86\n\t"
  "v_mfma_f32_16x16x32_bf16 a[88:91], a[180:183], v[148:151], a[88:91]\n\t"
  "ds_read_b128 a[136:139], v4 offset:49152\n\t"
  "v_mfma_f32_16x16x32_bf16 a[120:123], a[176:179], v[152:155], a[120:123]\n\t"
  "s_waitcnt lgkmcnt(2)\n\t"
  "v_exp_f32_e32 v87, v87\n\t"
  "v_mfma_f32_16x16x32_bf16 a[120:123], a[180:183], v[156:159], a[120:123]\n\t"
  "ds_read_b128 a[140:143], v5 offset:49152\n\t"
  "v_cvt_pk_bf16_f32 v111, v86, v87\n\t"
  "v_mfma_f32_16x16x32_bf16 a[28:31], a[184:187], v[128:131], a[28:31]\n\t"
  "v_exp_f32_e64 v88, v88\n\t"
  "v_mfma_f32_16x16x32_bf16 a[28:31], a[188:191], v[132:135], a[28:31]\n\t"
  "buffer_load_dwordx4 v12, s[12:15], s20 offen offset:2048 lds\n\t"
  "v_mfma_f32_16x16x32_bf16 a[60:63], a[184:187], v[136:139], a[60:63]\n\t"
  "v_exp_f32_e64 v89, v89\n\t"
  "v_mfma_f32_16x16x32_bf16 a[60:63], a[188:191], v[140:143], a[60:63]\n\t"
  "v_cvt_pk_bf16_f32 v118, v88, v89\n\t"
  "v_mfma_f32_16x16x32_bf16 a[92:95], a[184:187], v[144:147], a[92:95]\n\t"
  "v_exp_f32_e64 v90, v90\n\t"
  "v_mfma_f32_16x16x32_bf16 a[92:95], a[188:191], v[148:151], a[92:95]\n\t"
  "buffer_load_dwordx4 v13, s[12:15], s20 offen offset:3072 lds\n\t"
  "v_mfma_f32_16x16x32_bf16 a[124:127], a[184:187], v[152:155], a[124:127]\n\t"
  "s_add_u32 s20, s20, 0x4000\n\t"
  "v_exp_f32_e64 v91, v91\n\t"
  "v_mfma_f32_16x16x32_bf16 a[124:127], a[188:191], v[156:159], a[124:127]\n\t"
  "v_cvt_pk_bf16_f32 v119, v90, v91\n\t"
  "v_mfma_f32_16x16x32_bf16 a[192:195], a[208:211], v[128:131], a[192:195]\n\t"
  "v_exp_f32_e64 v92, v92\n\t"
  "v_mfma_f32_16x16x32_bf16 a[192:195], a[208:211], v[132:135], a[192:195]\n\t"
  "v_mfma_f32_16x16x32_bf16 a[196:199], a[208:211], v[136:139], a[196:199]\n\t"
  "v_exp_f32_e64 v93, v93\n\t"
  "v_mfma_f32_16x16x32_bf16 a[196:199], a[208:211], v[140:143], a[196:199]\n\t"
  "v_cvt_pk_bf16_f32 v126, v92, v93\n\t"
  "v_mfma_f32_16x16x32_bf16 a[200:203], a[208:211], v[144:147], a[200:203]\n\t"
  "v_exp_f32_e64 v94, v94\n\t"
  "v_mfma_f32_16x16x32_bf16 a[200:203], a[208:211], v[148:151], a[200:203]\n\t"
  "v_mfma_f32_16x16x32_bf16 a[204:207], a[208:211], v[152:155], a[204:207]\n\t"
  "v_exp_f32_e64 v95, v95\n\t"
  "v_mfma_f32_16x16x32_bf16 a[204:207], a[208:211], v[156:159], a[204:207]\n\t"
  "v_cvt_pk_bf16_f32 v127, v94, v95\n\t"
  "s_waitcnt vmcnt(8)\n\t"
  "s_barrier\n\t"
  "v_mfma_f32_16x16x32_bf16 v[32:35], a[128:131], v[160:163], v[224:227]\n\t"
  "s_waitcnt lgkmcnt(0)\n\t"
  "s_nop 0\n\t"
  "v_mfma_f32_16x16x32_bf16 v[32:35], a[132:135], v[164:167], v[32:35]\n\t"
  "ds_read_b128 a[144:147], v2 offset:53248\n\t"
  "v_mfma_f32_16x16x32_bf16 v[32:35], a[136:139], v[168:171], v[32:35]\n\t"
  "v_mfma_f32_16x16x32_bf16 v[32:35], a[140:143], v[172:175], v[32:35]\n\t"
  "ds_read_b128 a[148:151], v3 offset:53248\n\t"
  "v_mfma_f32_16x16x32_bf16 v[36:39], a[128:131], v[176:179], v[228:231]\n\t"
  "v_mfma_f32_16x16x32_bf16 v[36:39], a[132:135], v[180:183], v[36:39]\n\t"
  "ds_read_b128 a[152:155], v4 offset:53248\n\t"
  "v_mfma_f32_16x16x32_bf16 v[36:39], a[136:139], v[184:187], v[36:39]\n\t"
  "v_mfma_f32_16x16x32_bf16 v[36:39], a[140:143], v[188:191], v[36:39]\n\t"
  "ds_read_b128 a[156:159], v5 offset:53248\n\t"
  "v_mfma_f32_16x16x32_bf16 v[40:43], a[128:131], v[192:195], v[232:235]\n\t"
  "s_add_u32 m0, s18, 32768\n\t"
  "v_exp_f32_e64 v32, v32\n\t"
  "v_mfma_f32_16x16x32_bf16 v[40:43], a[132:135], v[196:199], v[40:43]\n\t"
  "buffer_load_dwordx4 v8, s[8:11], s16 offen offset:0 lds\n\t"
  "v_mfma_f32_16x16x32_bf16 v[40:43], a[136:139], v[200:203], v[40:43]\n\t"
  "v_exp_f32_e64 v33, v33\n\t"
  "v_mfma_f32_16x16x32_bf16 v[40:43], a[140:143], v[204:207], v[40:43]\n\t"
  "v_cvt_pk_bf16_f32 v128, v32, v33\n\t"
  "v_mfma_f32_16x16x32_bf16 v[44:47], a[128:131], v[208:211], v[236:239]\n\t"
  "v_exp_f32_e64 v34, v34\n\t"
  "v_mfma_f32_16x16x32_bf16 v[44:47], a[132:135], v[212:215], v[44:47]\n\t"
  "buffer_load_dwordx4 v9, s[8:11], s16 offen offset:1024 lds\n\t"
  "v_mfma_f32_16x16x32_bf16 v[44:47], a[136:139], v[216:219], v[44:47]\n\t"
  "s_waitcnt lgkmcnt(0)\n\t"
  "v_exp_f32_e32 v35, v35\n\t"
  "v_mfma_f32_16x16x32_bf16 v[44:47], a[140:143], v[220:223], v[44:47]\n\t"
  "v_cvt_pk_bf16_f32 v129, v34, v35\n\t"
  "v_mfma_f32_16x16x32_bf16 v[48:51], a[144:147], v[160:163], v[224:227]\n\t"
  "v_exp_f32_e64 v36, v36\n\t"
  "v_mfma_f32_16x16x32_bf16 v[48:51], a[148:151], v[164:167], v[48:51]\n\t"
  "ds_read_b128 a[128:131], v2 offset:57344\n\t"
  "v_mfma_f32_16x16x32_bf16 v[48:51], a[152:155], v[168:171], v[48:51]\n\t"
  "v_exp_f32_e64 v37, v37\n\t"
  "v_mfma_f32_16x16x32_bf16 v[48:51], a[156:159], v[172:175], v[48:51]\n\t"
  "ds_read_b128 a[132:135], v3 offset:57344\n\t"
  "v_cvt_pk_bf16_f32 v136, v36, v37\n\t"
  "v_mfma_f32_16x16x32_bf16 v[52:55], a[144:147], v[176:179], v[228:231]\n\t"
  "v_exp_f32_e64 v38, v38\n\t"
  "v_mfma_f32_16x16x32_bf16 v[52:55], a[148:151], v[180:183], v[52:55]\n\t"
  "ds_read_b128 a[136:139], v4 offset:57344\n\t"
  "v_mfma_f32_16x16x32_bf16 v[52:55], a[152:155], v[184:187], v[52:55]\n\t"
  "v_exp_f32_e64 v39, v39\n\t"
  "v_mfma_f32_16x16x32_bf16 v[52:55], a[156:159], v[188:191], v[52:55]\n\t"
  "ds_read_b128 a[140:143], v5 offset:57344\n\t"
  "v_cvt_pk_bf16_f32 v137, v38, v39\n\t"
  "v_mfma_f32_16x16x32_bf16 v[56:59], a[144:147], v[192:195], v[232:235]\n\t"
  "v_exp_f32_e64 v40, v40\n\t"
  "v_mfma_f32_16x16x32_bf16 v[56:59], a[148:151], v[196:199], v[56:59]\n\t"
  "buffer_load_dwordx4 v10, s[8:11], s16 offen offset:2048 lds\n\t"
  "v_mfma_f32_16x16x32_bf16 v[56:59], a[152:155], v[200:203], v[56:59]\n\t"
  "v_exp_f32_e64 v41, v41\n\t"
  "v_mfma_f32_16x16x32_bf16 v[56:59], a[156:159], v[204:207], v[56:59]\n\t"
  "v_cvt_pk_bf16_f32 v144, v40, v41\n\t"
  "v_mfma_f32_16x16x32_bf16 v[60:63], a[144:147], v[208:211], v[236:239]\n\t"
  "v_exp_f32_e64 v42, v42\n\t"
  "v_mfma_f32_16x16x32_bf16 v[60:63], a[148:151], v[212:215], v[60:63]\n\t"
  "buffer_load_dwordx4 v11, s[8:11], s16 offen offset:3072 lds\n\t"
  "v_mfma_f32_16x16x32_bf16 v[60:63], a[152:155], v[216:219], v[60:63]\n\t"
  "s_add_u32 s16, s16, 0x4000\n\t"
  "v_exp_f32_e64 v43, v43\n\t"
  "v_mfma_f32_16x16x32_bf16 v[60:63], a[156:159], v[220:223], v[60:63]\n\t"
  "s_waitcnt lgkmcnt(0)\n\t"
  "s_nop 0\n\t"
  "v_cvt_pk_bf16_f32 v145, v42, v43\n\t"
  "v_mfma_f32_16x16x32_bf16 v[64:67], a[128:131], v[160:163], v[224:227]\n\t"
  "v_exp_f32_e64 v44, v44\n\t"
  "v_mfma_f32_16x16x32_bf16 v[64:67], a[132:135], v[164:167], v[64:67]\n\t"
  "ds_read_b128 a[144:147], v2 offset:61440\n\t"
  "v_mfma_f32_16x16x32_bf16 v[64:67], a[136:139], v[168:171], v[64:67]\n\t"
  "v_exp_f32_e64 v45, v45\n\t"
  "v_mfma_f32_16x16x32_bf16 v[64:67], a[140:143], v[172:175], v[64:67]\n\t"
  "ds_read_b128 a[148:151], v3 offset:61440\n\t"
  "v_cvt_pk_bf16_f32 v152, v44, v45\n\t"
  "v_mfma_f32_16x16x32_bf16 v[68:71], a[128:131], v[176:179], v[228:231]\n\t"
  "v_exp_f32_e64 v46, v46\n\t"
  "v_mfma_f32_16x16x32_bf16 v[68:71], a[132:135], v[180:183], v[68:71]\n\t"
  "ds_read_b128 a[152:155], v4 offset:61440\n\t"
  "v_mfma_f32_16x16x32_bf16 v[68:71], a[136:139], v[184:187], v[68:71]\n\t"
  "v_exp_f32_e64 v47, v47\n\t"
  "v_mfma_f32_16x16x32_bf16 v[68:71], a[140:143], v[188:191], v[68:71]\n\t"
  "ds_read_b128 a[156:159], v5 offset:61440\n\t"
  "v_cvt_pk_bf16_f32 v153, v46, v47\n\t"
  "v_mfma_f32_16x16x32_bf16 v[72:75], a[128:131], v[192:195], v[232:235]\n\t"
  "s_add_u32 m0, s19, 0\n\t"
  "v_exp_f32_e32 v48, v48\n\t"
  "v_mfma_f32_16x16x32_bf16 v[72:75], a[132:135], v[196:199], v[72:75]\n\t"
  "buffer_load_dwordx4 v12, s[12:15], s20 offen offset:0 lds\n\t"
  "v_mfma_f32_16x16x32_bf16 v[72:75], a[136:139], v[200:203], v[72:75]\n\t"
  "v_exp_f32_e64 v49, v49\n\t"
  "v_mfma_f32_16x16x32_bf16 v[72:75], a[140:143], v[204:207], v[72:75]\n\t"
  "v_cvt_pk_bf16_f32 v130, v48, v49\n\t"
  "v_mfma_f32_16x16x32_bf16 v[76:79], a[128:131], v[208:211], v[236:239]\n\t"
  "v_exp_f32_e64 v50, v50\n\t"
  "v_mfma_f32_16x16x32_bf16 v[76:79], a[132:135], v[212:215], v[76:79]\n\t"
  "buffer_load_dwordx4 v13, s[12:15], s20 offen offset:1024 lds\n\t"
  "v_mfma_f32_16x16x32_bf16 v[76:79], a[136:139], v[216:219], v[76:79]\n\t"
  "s_waitcnt lgkmcnt(0)\n\t"
  "v_exp_f32_e32 v51, v51\n\t"
  "v_mfma_f32_16x16x32_bf16 v[76:79], a[140:143], v[220:223], v[76:79]\n\t"
  "v_cvt_pk_bf16_f32 v131, v50, v51\n\t"
  "v_mfma_f32_16x16x32_bf16 v[80:83], a[144:147], v[160:163], v[224:227]\n\t"
  "v_exp_f32_e64 v52, v52\n\t"
  "v_mfma_f32_16x16x32_bf16 v[80:83], a[148:151], v[164:167], v[80:83]\n\t"
  "ds_read_b64_tr_b16 a[160:161], v6 offset:32768\n\t"
  "v_mfma_f32_16x16x32_bf16 v[80:83], a[152:155], v[168:171], v[80:83]\n\t"
  "v_exp_f32_e64 v53, v53\n\t"
  "v_mfma_f32_16x16x32_bf16 v[80:83], a[156:159], v[172:175], v[80:83]\n\t"
  "ds_read_b64_tr_b16 a[162:163], v6 offset:36864\n\t"
  "v_cvt_pk_bf16_f32 v138, v52, v53\n\t"
  "v_mfma_f32_16x16x32_bf16 v[84:87], a[144:147], v[176:179], v[228:231]\n\t"
  "v_exp_f32_e64 v54, v54\n\t"
  "v_mfma_f32_16x16x32_bf16 v[84:87], a[148:151], v[180:183], v[84:87]\n\t"
  "ds_read_b64_tr_b16 a[164:165], v6 offset:40960\n\t"
  "v_mfma_f32_16x16x32_bf16 v[84:87], a[152:155], v[184:187], v[84:87]\n\t"
  "v_exp_f32_e64 v55, v55\n\t"
  "v_mfma_f32_16x16x32_bf16 v[84:87], a[156:159], v[188:191], v[84:87]\n\t"
  "ds_read_b64_tr_b16 a[166:167], v6 offset:45056\n\t"
  "v_cvt_pk_bf16_f32 v139, v54, v55\n\t"
  "v_mfma_f32_16x16x32_bf16 v[88:91], a[144:147], v[192:195], v[232:235]\n\t"
  "v_exp_f32_e64 v56, v56\n\t"
  "v_mfma_f32_16x16x32_bf16 v[88:91], a[148:151], v[196:199], v[88:91]\n\t"
  "ds_read_b64_tr_b16 a[168:169], v7 offset:32768\n\t"
  "v_mfma_f32_16x16x32_bf16 v[88:91], a[152:155], v[200:203], v[88:91]\n\t"
  "v_exp_f32_e64 v57, v57\n\t"
  "v_mfma_f32_16x16x32_bf16 v[88:91], a[156:159], v[204:207], v[88:91]\n\t"
  "ds_read_b64_tr_b16 a[170:171], v7 offset:36864\n\t"
  "v_cvt_pk_bf16_f32 v146, v56, v57\n\t"
  "v_mfma_f32_16x16x32_bf16 v[92:95], a[144:147], v[208:211], v[236:239]\n\t"
  "v_exp_f32_e64 v58, v58\n\t"
  "v_mfma_f32_16x16x32_bf16 v[92:95], a[148:151], v[212:215], v[92:95]\n\t"
  "ds_read_b64_tr_b16 a[172:173], v7 offset:40960\n\t"
  "v_mfma_f32_16x16x32_bf16 v[92:95], a[152:155], v[216:219], v[92:95]\n\t"
  "s_waitcnt lgkmcnt(2)\n\t"
  "v_exp_f32_e32 v59, v59\n\t"
  "v_mfma_f32_16x16x32_bf16 v[92:95], a[156:159], v[220:223], v[92:95]\n\t"
  "ds_read_b64_tr_b16 a[174:175], v7 offset:45056\n\t"
  "v_cvt_pk_bf16_f32 v147, v58, v59\n\t"
  "v_mfma_f32_16x16x32_bf16 a[0:3], a[160:163], v[96:99], a[0:3]\n\t"
  "v_exp_f32_e64 v60, v60\n\t"
  "v_mfma_f32_16x16x32_bf16 a[0:3], a[164:167], v[100:103], a[0:3]\n\t"
  "ds_read_b64_tr_b16 a[176:177], v6 offset:33280\n\t"
  "v_mfma_f32_16x16x32_bf16 a[32:35], a[160:163], v[104:107], a[32:35]\n\t"
  "v_exp_f32_e64 v61, v61\n\t"
  "v_mfma_f32_16x16x32_bf16 a[32:35], a[164:167], v[108:111], a[32:35]\n\t"
  "ds_read_b64_tr_b16 a[178:179], v6 offset:37376\n\t"
  "v_cvt_pk_bf16_f32 v154, v60, v61\n\t"
  "v_mfma_f32_16x16x32_bf16 a[64:67], a[160:163], v[112:115], a[64:67]\n\t"
  "v_exp_f32_e64 v62, v62\n\t"
  "v_mfma_f32_16x16x32_bf16 a[64:67], a[164:167], v[116:119], a[64:67]\n\t"
  "ds_read_b64_tr_b16 a[180:181], v6 offset:41472\n\t"
  "v_mfma_f32_16x16x32_bf16 a[96:99], a[160:163], v[120:123], a[96:99]\n\t"
  "s_waitcnt lgkmcnt(2)\n\t"
  "v_exp_f32_e32 v63, v63\n\t"
  "v_mfma_f32_16x16x32_bf16 a[96:99], a[164:167], v[124:127], a[96:99]\n\t"
  "ds_read_b64_tr_b16 a[182:183], v6 offset:45568\n\t"
  "v_cvt_pk_bf16_f32 v155, v62, v63\n\t"
  "v_mfma_f32_16x16x32_bf16 a[4:7], a[168:171], v[96:99], a[4:7]\n\t"
  "v_exp_f32_e64 v64, v64\n\t"
  "v_mfma_f32_16x16x32_bf16 a[4:7], a[172:175], v[100:103], a[4:7]\n\t"
  "ds_read_b64_tr_b16 a[184:185], v7 offset:33280\n\t"
  "v_mfma_f32_16x16x32_bf16 a[36:39], a[168:171], v[104:107], a[36:39]\n\t"
  "v_exp_f32_e64 v65, v65\n\t"
  "v_mfma_f32_16x16x32_bf16 a[36:39], a[172:175], v[108:111], a[36:39]\n\t"
  "ds_read_b64_tr_b16 a[186:187], v7 offset:37376\n\t"
  "v_cvt_pk_bf16_f32 v132, v64, v65\n\t"
  "v_mfma_f32_16x16x32_bf16 a[68:71], a[168:171], v[112:115], a[68:71]\n\t"
  "v_exp_f32_e64 v66, v66\n\t"
  "v_mfma_f32_16x16x32_bf16 a[68:71], a[172:175], v[116:119], a[68:71]\n\t"
  "ds_read_b64_tr_b16 a[188:189], v7 offset:41472\n\t"
  "v_mfma_f32_16x16x32_bf16 a[100:103], a[168:171], v[120:123], a[100:103]\n\t"
  "s_waitcnt lgkmcnt(2)\n\t"
  "v_exp_f32_e32 v67, v67\n\t"
  "v_mfma_f32_16x16x32_bf16 a[100:103], a[172:175], v[124:127], a[100:103]\n\t"
  "ds_read_b64_tr_b16 a[190:191], v7 offset:45568\n\t"
  "v_cvt_pk_bf16_f32 v133, v66, v67\n\t"
  "v_mfma_f32_16x16x32_bf16 a[8:11], a[176:179], v[96:99], a[8:11]\n\t"
  "v_exp_f32_e64 v68, v68\n\t"
  "v_mfma_f32_16x16x32_bf16 a[8:11], a[180:183], v[100:103], a[8:11]\n\t"
  "ds_read_b64_tr_b16 a[160:161], v6 offset:33792\n\t"
  "v_mfma_f32_16x16x32_bf16 a[40:43], a[176:179], v[104:107], a[40:43]\n\t"
  "v_exp_f32_e64 v69, v69\n\t"
  "v_mfma_f32_16x16x32_bf16 a[40:43], a[180:183], v[108:111], a[40:43]\n\t"
  "ds_read_b64_tr_b16 a[162:163], v6 offset:37888\n\t"
  "v_cvt_pk_bf16_f32 v140, v68, v69\n\t"
  "v_mfma_f32_16x16x32_bf16 a[72:75], a[176:179], v[112:115], a[72:75]\n\t"
  "v_exp_f32_e64 v70, v70\n\t"
  "v_mfma_f32_16x16x32_bf16 a[72:75], a[180:183], v[116:119], a[72:75]\n\t"
  "ds_read_b64_tr_b16 a[164:165], v6 offset:41984\n\t"
  "v_mfma_f32_16x16x32_bf16 a[104:107], a[176:179], v[120:123], a[104:107]\n\t"
  "s_waitcnt lgkmcnt(2)\n\t"
  "v_exp_f32_e32 v71, v71\n\t"
  "v_mfma_f32_16x16x32_bf16 a[104:107], a[180:183], v[124:127], a[104:107]\n\t"
  "ds_read_b64_tr_b16 a[166:167], v6 offset:46080\n\t"
  "v_cvt_pk_bf16_f32 v141, v70, v71\n\t"
  "v_mfma_f32_16x16x32_bf16 a[12:15], a[184:187], v[96:99], a[12:15]\n\t"
  "v_exp_f32_e64 v72, v72\n\t"
  "v_mfma_f32_16x16x32_bf16 a[12:15], a[188:191], v[100:103], a[12:15]\n\t"
  "ds_read_b64_tr_b16 a[168:169], v7 offset:33792\n\t"
  "v_mfma_f32_16x16x32_bf16 a[44:47], a[184:187], v[104:107], a[44:47]\n\t"
  "v_exp_f32_e64 v73, v73\n\t"
  "v_mfma_f32_16x16x32_bf16 a[44:47], a[188:191], v[108:111], a[44:47]\n\t"
  "ds_read_b64_tr_b16 a[170:171], v7 offset:37888\n\t"
  "v_cvt_pk_bf16_f32 v148, v72, v73\n\t"
  "v_mfma_f32_16x16x32_bf16 a[76:79], a[184:187], v[112:115], a[76:79]\n\t"
  "v_exp_f32_e64 v74, v74\n\t"
  "v_mfma_f32_16x16x32_bf16 a[76:79], a[188:191], v[116:119], a[76:79]\n\t"
  "ds_read_b64_tr_b16 a[172:173], v7 offset:41984\n\t"
  "v_mfma_f32_16x16x32_bf16 a[108:111], a[184:187], v[120:123], a[108:111]\n\t"
  "s_waitcnt lgkmcnt(2)\n\t"
  "v_exp_f32_e32 v75, v75\n\t"
  "v_mfma_f32_16x16x32_bf16 a[108:111], a[188:191], v[124:127], a[108:111]\n\t"
  "ds_read_b64_tr_b16 a[174:175], v7 offset:46080\n\t"
  "v_cvt_pk_bf16_f32 v149, v74, v75\n\t"
  "v_mfma_f32_16x16x32_bf16 a[16:19], a[160:163], v[96:99], a[16:19]\n\t"
  "v_exp_f32_e64 v76, v76\n\t"
  "v_mfma_f32_16x16x32_bf16 a[16:19], a[164:167], v[100:103], a[16:19]\n\t"
  "ds_read_b64_tr_b16 a[176:177], v6 offset:34304\n\t"
  "v_mfma_f32_16x16x32_bf16 a[48:51], a[160:163], v[104:107], a[48:51]\n\t"
  "v_exp_f32_e64 v77, v77\n\t"
  "v_mfma_f32_16x16x32_bf16 a[48:51], a[164:167], v[108:111], a[48:51]\n\t"
  "ds_read_b64_tr_b16 a[178:179], v6 offset:38400\n\t"
  "v_cvt_pk_bf16_f32 v156, v76, v77\n\t"
  "v_mfma_f32_16x16x32_bf16 a[80:83], a[160:163], v[112:115], a[80:83]\n\t"
  "v_exp_f32_e64 v78, v78\n\t"
  "v_mfma_f32_16x16x32_bf16 a[80:83], a[164:167], v[116:119], a[80:83]\n\t"
  "ds_read_b64_tr_b16 a[180:181], v6 offset:42496\n\t"
  "v_mfma_f32_16x16x32_bf16 a[112:115], a[160:163], v[120:123], a[112:115]\n\t"
  "s_waitcnt lgkmcnt(2)\n\t"
  "v_exp_f32_e32 v79, v79\n\t"
  "v_mfma_f32_16x16x32_bf16 a[112:115], a[164:167], v[124:127], a[112:115]\n\t"
  "ds_read_b64_tr_b16 a[182:183], v6 offset:46592\n\t"
  "v_cvt_pk_bf16_f32 v157, v78, v79\n\t"
  "v_mfma_f32_16x16x32_bf16 a[20:23], a[168:171], v[96:99], a[20:23]\n\t"
  "v_exp_f32_e64 v80, v80\n\t"
  "v_mfma_f32_16x16x32_bf16 a[20:23], a[172:175], v[100:103], a[20:23]\n\t"
  "ds_read_b64_tr_b16 a[184:185], v7 offset:34304\n\t"
  "v_mfma_f32_16x16x32_bf16 a[52:55], a[168:171], v[104:107], a[52:55]\n\t"
  "v_exp_f32_e64 v81, v81\n\t"
  "v_mfma_f32_16x16x32_bf16 a[52:55], a[172:175], v[108:111], a[52:55]\n\t"
  "ds_read_b64_tr_b16 a[186:187], v7 offset:38400\n\t"
  "v_cvt_pk_bf16_f32 v134, v80, v81\n\t"
  "v_mfma_f32_16x16x32_bf16 a[84:87], a[168:171], v[112:115], a[84:87]\n\t"
  "v_exp_f32_e64 v82, v82\n\t"
  "v_mfma_f32_16x16x32_bf16 a[84:87], a[172:175], v[116:119], a[84:87]\n\t"
  "ds_read_b64_tr_b16 a[188:189], v7 offset:42496\n\t"
  "v_mfma_f32_16x16x32_bf16 a[116:119], a[168:171], v[120:123], a[116:119]\n\t"
  "s_waitcnt lgkmcnt(2)\n\t"
  "v_exp_f32_e32 v83, v83\n\t"
  "v_mfma_f32_16x16x32_bf16 a[116:119], a[172:175], v[124:127], a[116:119]\n\t"
  "ds_read_b64_tr_b16 a[190:191], v7 offset:46592\n\t"
  "v_cvt_pk_bf16_f32 v135, v82, v83\n\t"
  "v_mfma_f32_16x16x32_bf16 a[24:27], a[176:179], v[96:99], a[24:27]\n\t"
  "v_exp_f32_e64 v84, v84\n\t"
  "v_mfma_f32_16x16x32_bf16 a[24:27], a[180:183], v[100:103], a[24:27]\n\t"
  "ds_read_b128 a[128:131], v2 offset:0\n\t"
  "v_mfma_f32_16x16x32_bf16 a[56:59], a[176:179], v[104:107], a[56:59]\n\t"
  "v_exp_f32_e64 v85, v85\n\t"
  "v_mfma_f32_16x16x32_bf16 a[56:59], a[180:183], v[108:111], a[56:59]\n\t"
  "ds_read_b128 a[132:135], v3 offset:0\n\t"
  "v_cvt_pk_bf16_f32 v142, v84, v85\n\t"
  "v_mfma_f32_16x16x32_bf16 a[88:91], a[176:179], v[112:115], a[88:91]\n\t"
  "v_exp_f32_e64 v86, v86\n\t"
  "v_mfma_f32_16x16x32_bf16 a[88:91], a[180:183], v[116:119], a[88:91]\n\t"
  "ds_read_b128 a[136:139], v4 offset:0\n\t"
  "v_mfma_f32_16x16x32_bf16 a[120:123], a[176:179], v[120:123], a[120:123]\n\t"
  "s_waitcnt lgkmcnt(2)\n\t"
  "v_exp_f32_e32 v87, v87\n\t"
  "v_mfma_f32_16x16x32_bf16 a[120:123], a[180:183], v[124:127], a[120:123]\n\t"
  "ds_read_b128 a[140:143], v5 offset:0\n\t"
  "v_cvt_pk_bf16_f32 v143, v86, v87\n\t"
  "v_mfma_f32_16x16x32_bf16 a[28:31], a[184:187], v[96:99], a[28:31]\n\t"
  "v_exp_f32_e64 v88, v88\n\t"
  "v_mfma_f32_16x16x32_bf16 a[28:31], a[188:191], v[100:103], a[28:31]\n\t"
  "buffer_load_dwordx4 v12, s[12:15], s20 offen offset:2048 lds\n\t"
  "v_mfma_f32_16x16x32_bf16 a[60:63], a[184:187], v[104:107], a[60:63]\n\t"
  "v_exp_f32_e64 v89, v89\n\t"
  "v_mfma_f32_16x16x32_bf16 a[60:63], a[188:191], v[108:111], a[60:63]\n\t"
  "v_cvt_pk_bf16_f32 v150, v88, v89\n\t"
  "v_mfma_f32_16x16x32_bf16 a[92:95], a[184:187], v[112:115], a[92:95]\n\t"
  "v_exp_f32_e64 v90, v90\n\t"
  "v_mfma_f32_16x16x32_bf16 a[92:95], a[188:191], v[116:119], a[92:95]\n\t"
  "buffer_load_dwordx4 v13, s[12:15], s20 offen offset:3072 lds\n\t"
  "v_mfma_f32_16x16x32_bf16 a[124:127], a[184:187], v[120:123], a[124:127]\n\t"
  "s_add_u32 s20, s20, 0x4000\n\t"
  "v_exp_f32_e64 v91, v91\n\t"
  "v_mfma_f32_16x16x32_bf16 a[124:127], a[188:191], v[124:127], a[124:127]\n\t"
  "v_cvt_pk_bf16_f32 v151, v90, v91\n\t"
  "v_mfma_f32_16x16x32_bf16 a[192:195], a[208:211], v[96:99], a[192:195]\n\t"
  "v_exp_f32_e64 v92, v92\n\t"
  "v_mfma_f32_16x16x32_bf16 a[192:195], a[208:211], v[100:103], a[192:195]\n\t"
  "v_mfma_f32_16x16x32_bf16 a[196:199], a[208:211], v[104:107], a[196:199]\n\t"
  "v_exp_f32_e64 v93, v93\n\t"
  "v_mfma_f32_16x16x32_bf16 a[196:199], a[208:211], v[108:111], a[196:199]\n\t"
  "v_cvt_pk_bf16_f32 v158, v92, v93\n\t"
  "v_mfma_f32_16x16x32_bf16 a[200:203], a[208:211], v[112:115], a[200:203]\n\t"
  "v_exp_f32_e64 v94, v94\n\t"
  "v_mfma_f32_16x16x32_bf16 a[200:203], a[208:211], v[116:119], a[200:203]\n\t"
  "v_mfma_f32_16x16x32_bf16 a[204:207], a[208:211], v[120:123], a[204:207]\n\t"
  "v_exp_f32_e64 v95, v95\n\t"
  "v_mfma_f32_16x16x32_bf16 a[204:207], a[208:211], v[124:127], a[204:207]\n\t"
  "v_cvt_pk_bf16_f32 v159, v94, v95\n\t"
  "s_waitcnt vmcnt(8)\n\t"
  "s_barrier\n\t"
  "v_mfma_f32_16x16x32_bf16 v[32:35], a[128:131], v[160:163], v[224:227]\n\t"
  "s_waitcnt lgkmcnt(0)\n\t"
  "s_nop 0\n\t"
  "v_mfma_f32_16x16x32_bf16 v[32:35], a[132:135], v[164:167], v[32:35]\n\t"
  "ds_read_b128 a[144:147], v2 offset:4096\n\t"
  "v_mfma_f32_16x16x32_bf16 v[32:35], a[136:139], v[168:171], v[32:35]\n\t"
  "v_mfma_f32_16x16x32_bf16 v[32:35], a[140:143], v[172:175], v[32:35]\n\t"
  "ds_read_b128 a[148:151], v3 offset:4096\n\t"
  "v_mfma_f32_16x16x32_bf16 v[36:39], a[128:131], v[176:179], v[228:231]\n\t"
  "v_mfma_f32_16x16x32_bf16 v[36:39], a[132:135], v[180:183], v[36:39]\n\t"
  "ds_read_b128 a[152:155], v4 offset:4096\n\t"
  "v_mfma_f32_16x16x32_bf16 v[36:39], a[136:139], v[184:187], v[36:39]\n\t"
  "v_mfma_f32_16x16x32_bf16 v[36:39], a[140:143], v[188:191], v[36:39]\n\t"
  "ds_read_b128 a[156:159], v5 offset:4096\n\t"
  "v_mfma_f32_16x16x32_bf16 v[40:43], a[128:131], v[192:195], v[232:235]\n\t"
  "s_add_u32 m0, s18, 49152\n\t"
  "v_exp_f32_e64 v32, v32\n\t"
  "v_mfma_f32_16x16x32_bf16 v[40:43], a[132:135], v[196:199], v[40:43]\n\t"
  "buffer_load_dwordx4 v8, s[8:11], s16 offen offset:0 lds\n\t"
  "v_mfma_f32_16x16x32_bf16 v[40:43], a[136:139], v[200:203], v[40:43]\n\t"
  "v_exp_f32_e64 v33, v33\n\t"
  "v_mfma_f32_16x16x32_bf16 v[40:43], a[140:143], v[204:207], v[40:43]\n\t"
  "v_cvt_pk_bf16_f32 v96, v32, v33\n\t"
  "v_mfma_f32_16x16x32_bf16 v[44:47], a[128:131], v[208:211], v[236:239]\n\t"
  "v_exp_f32_e64 v34, v34\n\t"
  "v_mfma_f32_16x16x32_bf16 v[44:47], a[132:135], v[212:215], v[44:47]\n\t"
  "buffer_load_dwordx4 v9, s[8:11], s16 offen offset:1024 lds\n\t"
  "v_mfma_f32_16x16x32_bf16 v[44:47], a[136:139], v[216:219], v[44:47]\n\t"
  "s_waitcnt lgkmcnt(0)\n\t"
  "v_exp_f32_e32 v35, v35\n\t"
  "v_mfma_f32_16x16x32_bf16 v[44:47], a[140:143], v[220:223], v[44:47]\n\t"
  "v_cvt_pk_bf16_f32 v97, v34, v35\n\t"
  "v_mfma_f32_16x16x32_bf16 v[48:51], a[144:147], v[160:163], v[224:227]\n\t"
  "v_exp_f32_e64 v36, v36\n\t"
  "v_mfma_f32_16x16x32_bf16 v[48:51], a[148:151], v[164:167], v[48:51]\n\t"
  "ds_read_b128 a[128:131], v2 offset:8192\n\t"
  "v_mfma_f32_16x16x32_bf16 v[48:51], a[152:155], v[168:171], v[48:51]\n\t"
  "v_exp_f32_e64 v37, v37\n\t"
  "v_mfma_f32_16x16x32_bf16 v[48:51], a[156:159], v[172:175], v[48:51]\n\t"
  "ds_read_b128 a[132:135], v3 offset:8192\n\t"
  "v_cvt_pk_bf16_f32 v104, v36, v37\n\t"
  "v_mfma_f32_16x16x32_bf16 v[52:55], a[144:147], v[176:179], v[228:231]\n\t"
  "v_exp_f32_e64 v38, v38\n\t"
  "v_mfma_f32_16x16x32_bf16 v[52:55], a[148:151], v[180:183], v[52:55]\n\t"
  "ds_read_b128 a[136:139], v4 offset:8192\n\t"
  "v_mfma_f32_16x16x32_bf16 v[52:55], a[152:155], v[184:187], v[52:55]\n\t"
  "v_exp_f32_e64 v39, v39\n\t"
  "v_mfma_f32_16x16x32_bf16 v[52:55], a[156:159], v[188:191], v[52:55]\n\t"
  "ds_read_b128 a[140:143], v5 offset:8192\n\t"
  "v_cvt_pk_bf16_f32 v105, v38, v39\n\t"
  "v_mfma_f32_16x16x32_bf16 v[56:59], a[144:147], v[192:195], v[232:235]\n\t"
  "v_exp_f32_e64 v40, v40\n\t"
  "v_mfma_f32_16x16x32_bf16 v[56:59], a[148:151], v[196:199], v[56:59]\n\t"
  "buffer_load_dwordx4 v10, s[8:11], s16 offen offset:2048 lds\n\t"
  "v_mfma_f32_16x16x32_bf16 v[56:59], a[152:155], v[200:203], v[56:59]\n\t"
  "v_exp_f32_e64 v41, v41\n\t"
  "v_mfma_f32_16x16x32_bf16 v[56:59], a[156:159], v[204:207], v[56:59]\n\t"
  "v_cvt_pk_bf16_f32 v112, v40, v41\n\t"
  "v_mfma_f32_16x16x32_bf16 v[60:63], a[144:147], v[208:211], v[236:239]\n\t"
  "v_exp_f32_e64 v42, v42\n\t"
  "v_mfma_f32_16x16x32_bf16 v[60:63], a[148:151], v[212:215], v[60:63]\n\t"
  "buffer_load_dwordx4 v11, s[8:11], s16 offen offset:3072 lds\n\t"
  "v_mfma_f32_16x16x32_bf16 v[60:63], a[152:155], v[216:219], v[60:63]\n\t"
  "s_add_u32 s16, s16, 0x4000\n\t"
  "v_exp_f32_e64 v43, v43\n\t"
  "v_mfma_f32_16x16x32_bf16 v[60:63], a[156:159], v[220:223], v[60:63]\n\t"
  "s_waitcnt lgkmcnt(0)\n\t"
  "s_nop 0\n\t"
  "v_cvt_pk_bf16_f32 v113, v42, v43\n\t"
  "v_mfma_f32_16x16x32_bf16 v[64:67], a[128:131], v[160:163], v[224:227]\n\t"
  "v_exp_f32_e64 v44, v44\n\t"
  "v_mfma_f32_16x16x32_bf16 v[64:67], a[132:135], v[164:167], v[64:67]\n\t"
  "ds_read_b128 a[144:147], v2 offset:12288\n\t"
  "v_mfma_f32_16x16x32_bf16 v[64:67], a[136:139], v[168:171], v[64:67]\n\t"
  "v_exp_f32_e64 v45, v45\n\t"
  "v_mfma_f32_16x16x32_bf16 v[64:67], a[140:143], v[172:175], v[64:67]\n\t"
  "ds_read_b128 a[148:151], v3 offset:12288\n\t"
  "v_cvt_pk_bf16_f32 v120, v44, v45\n\t"
  "v_mfma_f32_16x16x32_bf16 v[68:71], a[128:131], v[176:179], v[228:231]\n\t"
  "v_exp_f32_e64 v46, v46\n\t"
  "v_mfma_f32_16x16x32_bf16 v[68:71], a[132:135], v[180:183], v[68:71]\n\t"
  "ds_read_b128 a[152:155], v4 offset:12288\n\t"
  "v_mfma_f32_16x16x32_bf16 v[68:71], a[136:139], v[184:187], v[68:71]\n\t"
  "v_exp_f32_e64 v47, v47\n\t"
  "v_mfma_f32_16x16x32_bf16 v[68:71], a[140:143], v[188:191], v[68:71]\n\t"
  "ds_read_b128 a[156:159], v5 offset:12288\n\t"
  "v_cvt_pk_bf16_f32 v121, v46, v47\n\t"
  "v_mfma_f32_16x16x32_bf16 v[72:75], a[128:131], v[192:195], v[232:235]\n\t"
  "s_add_u32 m0, s19, 16384\n\t"
  "v_exp_f32_e64 v48, v48\n\t"
  "v_mfma_f32_16x16x32_bf16 v[72:75], a[132:135], v[196:199], v[72:75]\n\t"
  "buffer_load_dwordx4 v12, s[12:15], s20 offen offset:0 lds\n\t"
  "v_mfma_f32_16x16x32_bf16 v[72:75], a[136:139], v[200:203], v[72:75]\n\t"
  "v_exp_f32_e64 v49, v49\n\t"
  "v_mfma_f32_16x16x32_bf16 v[72:75], a[140:143], v[204:207], v[72:75]\n\t"
  "v_cvt_pk_bf16_f32 v98, v48, v49\n\t"
  "v_mfma_f32_16x16x32_bf16 v[76:79], a[128:131], v[208:211], v[236:239]\n\t"
  "v_exp_f32_e64 v50, v50\n\t"
  "v_mfma_f32_16x16x32_bf16 v[76:79], a[132:135], v[212:215], v[76:79]\n\t"
  "buffer_load_dwordx4 v13, s[12:15], s20 offen offset:1024 lds\n\t"
  "v_mfma_f32_16x16x32_bf16 v[76:79], a[136:139], v[216:219], v[76:79]\n\t"
  "s_waitcnt lgkmcnt(0)\n\t"
  "v_exp_f32_e32 v51, v51\n\t"
  "v_mfma_f32_16x16x32_bf16 v[76:79], a[140:143], v[220:223], v[76:79]\n\t"
  "v_cvt_pk_bf16_f32 v99, v50, v51\n\t"
  "v_mfma_f32_16x16x32_bf16 v[80:83], a[144:147], v[160:163], v[224:227]\n\t"
  "v_exp_f32_e64 v52, v52\n\t"
  "v_mfma_f32_16x16x32_bf16 v[80:83], a[148:151], v[164:167], v[80:83]\n\t"
  "ds_read_b64_tr_b16 a[160:161], v6 offset:49152\n\t"
  "v_mfma_f32_16x16x32_bf16 v[80:83], a[152:155], v[168:171], v[80:83]\n\t"
  "v_exp_f32_e64 v53, v53\n\t"
  "v_mfma_f32_16x16x32_bf16 v[80:83], a[156:159], v[172:175], v[80:83]\n\t"
  "ds_read_b64_tr_b16 a[162:163], v6 offset:53248\n\t"
  "v_cvt_pk_bf16_f32 v106, v52, v53\n\t"
  "v_mfma_f32_16x16x32_bf16 v[84:87], a[144:147], v[176:179], v[228:231]\n\t"
  "v_exp_f32_e64 v54, v54\n\t"
  "v_mfma_f32_16x16x32_bf16 v[84:87], a[148:151], v[180:183], v[84:87]\n\t"
  "ds_read_b64_tr_b16 a[164:165], v6 offset:57344\n\t"
  "v_mfma_f32_16x16x32_bf16 v[84:87], a[152:155], v[184:187], v[84:87]\n\t"
  "v_exp_f32_e64 v55, v55\n\t"
  "v_mfma_f32_16x16x32_bf16 v[84:87], a[156:159], v[188:191], v[84:87]\n\t"
  "ds_read_b64_tr_b16 a[166:167], v6 offset:61440\n\t"
  "v_cvt_pk_bf16_f32 v107, v54, v55\n\t"
  "v_mfma_f32_16x16x32_bf16 v[88:91], a[144:147], v[192:195], v[232:235]\n\t"
  "v_exp_f32_e64 v56, v56\n\t"
  "v_mfma_f32_16x16x32_bf16 v[88:91], a[148:151], v[196:199], v[88:91]\n\t"
  "ds_read_b64_tr_b16 a[168:169], v7 offset:49152\n\t"
  "v_mfma_f32_16x16x32_bf16 v[88:91], a[152:155], v[200:203], v[88:91]\n\t"
  "v_exp_f32_e64 v57, v57\n\t"
  "v_mfma_f32_16x16x32_bf16 v[88:91], a[156:159], v[204:207], v[88:91]\n\t"
  "ds_read_b64_tr_b16 a[170:171], v7 offset:53248\n\t"
  "v_cvt_pk_bf16_f32 v114, v56, v57\n\t"
  "v_mfma_f32_16x16x32_bf16 v[92:95], a[144:147], v[208:211], v[236:239]\n\t"
  "v_exp_f32_e64 v58, v58\n\t"
  "v_mfma_f32_16x16x32_bf16 v[92:95], a[148:151], v[212:215], v[92:95]\n\t"
  "ds_read_b64_tr_b16 a[172:173], v7 offset:57344\n\t"
  "v_mfma_f32_16x16x32_bf16 v[92:95], a[152:155], v[216:219], v[92:95]\n\t"
  "s_waitcnt lgkmcnt(2)\n\t"
  "v_exp_f32_e32 v59, v59\n\t"
  "v_mfma_f32_16x16x32_bf16 v[92:95], a[156:159], v[220:223], v[92:95]\n\t"
  "ds_read_b64_tr_b16 a[174:175], v7 offset:61440\n\t"
  "v_cvt_pk_bf16_f32 v115, v58, v59\n\t"
  "v_mfma_f32_16x16x32_bf16 a[0:3], a[160:163], v[128:131], a[0:3]\n\t"
  "v_exp_f32_e64 v60, v60\n\t"
  "v_mfma_f32_16x16x32_bf16 a[0:3], a[164:167], v[132:135], a[0:3]\n\t"
  "ds_read_b64_tr_b16 a[176:177], v6 offset:49664\n\t"
  "v_mfma_f32_16x16x32_bf16 a[32:35], a[160:163], v[136:139], a[32:35]\n\t"
  "v_exp_f32_e64 v61, v61\n\t"
  "v_mfma_f32_16x16x32_bf16 a[32:35], a[164:167], v[140:143], a[32:35]\n\t"
  "ds_read_b64_tr_b16 a[178:179], v6 offset:53760\n\t"
  "v_cvt_pk_bf16_f32 v122, v60, v61\n\t"
  "v_mfma_f32_16x16x32_bf16 a[64:67], a[160:163], v[144:147], a[64:67]\n\t"
  "v_exp_f32_e64 v62, v62\n\t"
  "v_mfma_f32_16x16x32_bf16 a[64:67], a[164:167], v[148:151], a[64:67]\n\t"
  "ds_read_b64_tr_b16 a[180:181], v6 offset:57856\n\t"
  "v_mfma_f32_16x16x32_bf16 a[96:99], a[160:163], v[152:155], a[96:99]\n\t"
  "s_waitcnt lgkmcnt(2)\n\t"
  "v_exp_f32_e32 v63, v63\n\t"
  "v_mfma_f32_16x16x32_bf16 a[96:99], a[164:167], v[156:159], a[96:99]\n\t"
  "ds_read_b64_tr_b16 a[182:183], v6 offset:61952\n\t"
  "v_cvt_pk_bf16_f32 v123, v62, v63\n\t"
  "v_mfma_f32_16x16x32_bf16 a[4:7], a[168:171], v[128:131], a[4:7]\n\t"
  "v_exp_f32_e64 v64, v64\n\t"
  "v_mfma_f32_16x16x32_bf16 a[4:7], a[172:175], v[132:135], a[4:7]\n\t"
  "ds_read_b64_tr_b16 a[184:185], v7 offset:49664\n\t"
  "v_mfma_f32_16x16x32_bf16 a[36:39], a[168:171], v[136:139], a[36:39]\n\t"
  "v_exp_f32_e64 v65, v65\n\t"
  "v_mfma_f32_16x16x32_bf16 a[36:39], a[172:175], v[140:143], a[36:39]\n\t"
  "ds_read_b64_tr_b16 a[186:187], v7 offset:53760\n\t"
  "v_cvt_pk_bf16_f32 v100, v64, v65\n\t"
  "v_mfma_f32_16x16x32_bf16 a[68:71], a[168:171], v[144:147], a[68:71]\n\t"
  "v_exp_f32_e64 v66, v66\n\t"
  "v_mfma_f32_16x16x32_bf16 a[68:71], a[172:175], v[148:151], a[68:71]\n\t"
  "ds_read_b64_tr_b16 a[188:189], v7 offset:57856\n\t"
  "v_mfma_f32_16x16x32_bf16 a[100:103], a[168:171], v[152:155], a[100:103]\n\t"
  "s_waitcnt lgkmcnt(2)\n\t"
  "v_exp_f32_e32 v67, v67\n\t"
  "v_mfma_f32_16x16x32_bf16 a[100:103], a[172:175], v[156:159], a[100:103]\n\t"
  "ds_read_b64_tr_b16 a[190:191], v7 offset:61952\n\t"
  "v_cvt_pk_bf16_f32 v101, v66, v67\n\t"
  "v_mfma_f32_16x16x32_bf16 a[8:11], a[176:179], v[128:131], a[8:11]\n\t"
  "v_exp_f32_e64 v68, v68\n\t"
  "v_mfma_f32_16x16x32_bf16 a[8:11], a[180:183], v[132:135], a[8:11]\n\t"
  "ds_read_b64_tr_b16 a[160:161], v6 offset:50176\n\t"
  "v_mfma_f32_16x16x32_bf16 a[40:43], a[176:179], v[136:139], a[40:43]\n\t"
  "v_exp_f32_e64 v69, v69\n\t"
  "v_mfma_f32_16x16x32_bf16 a[40:43], a[180:183], v[140:143], a[40:43]\n\t"
  "ds_read_b64_tr_b16 a[162:163], v6 offset:54272\n\t"
  "v_cvt_pk_bf16_f32 v108, v68, v69\n\t"
  "v_mfma_f32_16x16x32_bf16 a[72:75], a[176:179], v[144:147], a[72:75]\n\t"
  "v_exp_f32_e64 v70, v70\n\t"
  "v_mfma_f32_16x16x32_bf16 a[72:75], a[180:183], v[148:151], a[72:75]\n\t"
  "ds_read_b64_tr_b16 a[164:165], v6 offset:58368\n\t"
  "v_mfma_f32_16x16x32_bf16 a[104:107], a[176:179], v[152:155], a[104:107]\n\t"
  "s_waitcnt lgkmcnt(2)\n\t"
  "v_exp_f32_e32 v71, v71\n\t"
  "v_mfma_f32_16x16x32_bf16 a[104:107], a[180:183], v[156:159], a[104:107]\n\t"
  "ds_read_b64_tr_b16 a[166:167], v6 offset:62464\n\t"
  "v_cvt_pk_bf16_f32 v109, v70, v71\n\t"
  "v_mfma_f32_16x16x32_bf16 a[12:15], a[184:187], v[128:131], a[12:15]\n\t"
  "v_exp_f32_e64 v72, v72\n\t"
  "v_mfma_f32_16x16x32_bf16 a[12:15], a[188:191], v[132:135], a[12:15]\n\t"
  "ds_read_b64_tr_b16 a[168:169], v7 offset:50176\n\t"
  "v_mfma_f32_16x16x32_bf16 a[44:47], a[184:187], v[136:139], a[44:47]\n\t"
  "v_exp_f32_e64 v73, v73\n\t"
  "v_mfma_f32_16x16x32_bf16 a[44:47], a[188:191], v[140:143], a[44:47]\n\t"
  "ds_read_b64_tr_b16 a[170:171], v7 offset:54272\n\t"
  "v_cvt_pk_bf16_f32 v116, v72, v73\n\t"
  "v_mfma_f32_16x16x32_bf16 a[76:79], a[184:187], v[144:147], a[76:79]\n\t"
  "v_exp_f32_e64 v74, v74\n\t"
  "v_mfma_f32_16x16x32_bf16 a[76:79], a[188:191], v[148:151], a[76:79]\n\t"
  "ds_read_b64_tr_b16 a[172:173], v7 offset:58368\n\t"
  "v_mfma_f32_16x16x32_bf16 a[108:111], a[184:187], v[152:155], a[108:111]\n\t"
  "s_waitcnt lgkmcnt(2)\n\t"
  "v_exp_f32_e32 v75, v75\n\t"
  "v_mfma_f32_16x16x32_bf16 a[108:111], a[188:191], v[156:159], a[108:111]\n\t"
  "ds_read_b64_tr_b16 a[174:175], v7 offset:62464\n\t"
  "v_cvt_pk_bf16_f32 v117, v74, v75\n\t"
  "v_mfma_f32_16x16x32_bf16 a[16:19], a[160:163], v[128:131], a[16:19]\n\t"
  "v_exp_f32_e64 v76, v76\n\t"
  "v_mfma_f32_16x16x32_bf16 a[16:19], a[164:167], v[132:135], a[16:19]\n\t"
  "ds_read_b64_tr_b16 a[176:177], v6 offset:50688\n\t"
  "v_mfma_f32_16x16x32_bf16 a[48:51], a[160:163], v[136:139], a[48:51]\n\t"
  "v_exp_f32_e64 v77, v77\n\t"
  "v_mfma_f32_16x16x32_bf16 a[48:51], a[164:167], v[140:143], a[48:51]\n\t"
  "ds_read_b64_tr_b16 a[178:179], v6 offset:54784\n\t"
  "v_cvt_pk_bf16_f32 v124, v76, v77\n\t"
  "v_mfma_f32_16x16x32_bf16 a[80:83], a[160:163], v[144:147], a[80:83]\n\t"
  "v_exp_f32_e64 v78, v78\n\t"
  "v_mfma_f32_16x16x32_bf16 a[80:83], a[164:167], v[148:151], a[80:83]\n\t"
  "ds_read_b64_tr_b16 a[180:181], v6 offset:58880\n\t"
  "v_mfma_f32_16x16x32_bf16 a[112:115], a[160:163], v[152:155], a[112:115]\n\t"
  "s_waitcnt lgkmcnt(2)\n\t"
  "v_exp_f32_e32 v79, v79\n\t"
  "v_mfma_f32_16x16x32_bf16 a[112:115], a[164:167], v[156:159], a[112:115]\n\t"
  "ds_read_b64_tr_b16 a[182:183], v6 offset:62976\n\t"
  "v_cvt_pk_bf16_f32 v125, v78, v79\n\t"
  "v_mfma_f32_16x16x32_bf16 a[20:23], a[168:171], v[128:131], a[20:23]\n\t"
  "v_exp_f32_e64 v80, v80\n\t"
  "v_mfma_f32_16x16x32_bf16 a[20:23], a[172:175], v[132:135], a[20:23]\n\t"
  "ds_read_b64_tr_b16 a[184:185], v7 offset:50688\n\t"
  "v_mfma_f32_16x16x32_bf16 a[52:55], a[168:171], v[136:139], a[52:55]\n\t"
  "v_exp_f32_e64 v81, v81\n\t"
  "v_mfma_f32_16x16x32_bf16 a[52:55], a[172:175], v[140:143], a[52:55]\n\t"
  "ds_read_b64_tr_b16 a[186:187], v7 offset:54784\n\t"
  "v_cvt_pk_bf16_f32 v102, v80, v81\n\t"
  "v_mfma_f32_16x16x32_bf16 a[84:87], a[168:171], v[144:147], a[84:87]\n\t"
  "v_exp_f32_e64 v82, v82\n\t"
  "v_mfma_f32_16x16x32_bf16 a[84:87], a[172:175], v[148:151], a[84:87]\n\t"
  "ds_read_b64_tr_b16 a[188:189], v7 offset:58880\n\t"
  "v_mfma_f32_16x16x32_bf16 a[116:119], a[168:171], v[152:155], a[116:119]\n\t"
  "s_waitcnt lgkmcnt(2)\n\t"
  "v_exp_f32_e32 v83, v83\n\t"
  "v_mfma_f32_16x16x32_bf16 a[116:119], a[172:175], v[156:159], a[116:119]\n\t"
  "ds_read_b64_tr_b16 a[190:191], v7 offset:62976\n\t"
  "v_cvt_pk_bf16_f32 v103, v82, v83\n\t"
  "v_mfma_f32_16x16x32_bf16 a[24:27], a[176:179], v[128:131], a[24:27]\n\t"
  "v_exp_f32_e64 v84, v84\n\t"
  "v_mfma_f32_16x16x32_bf16 a[24:27], a[180:183], v[132:135], a[24:27]\n\t"
  "ds_read_b128 a[128:131], v2 offset:16384\n\t"
  "v_mfma_f32_16x16x32_bf16 a[56:59], a[176:179], v[136:139], a[56:59]\n\t"
  "v_exp_f32_e64 v85, v85\n\t"
  "v_mfma_f32_16x16x32_bf16 a[56:59], a[180:183], v[140:143], a[56:59]\n\t"
  "ds_read_b128 a[132:135], v3 offset:16384\n\t"
  "v_cvt_pk_bf16_f32 v110, v84, v85\n\t"
  "v_mfma_f32_16x16x32_bf16 a[88:91], a[176:179], v[144:147], a[88:91]\n\t"
  "v_exp_f32_e64 v86, v86\n\t"
  "v_mfma_f32_16x16x32_bf16 a[88:91], a[180:183], v[148:151], a[88:91]\n\t"
  "ds_read_b128 a[136:139], v4 offset:16384\n\t"
  "v_mfma_f32_16x16x32_bf16 a[120:123], a[176:179], v[152:155], a[120:123]\n\t"
  "s_waitcnt lgkmcnt(2)\n\t"
  "v_exp_f32_e32 v87, v87\n\t"
  "v_mfma_f32_16x16x32_bf16 a[120:123], a[180:183], v[156:159], a[120:123]\n\t"
  "ds_read_b128 a[140:143], v5 offset:16384\n\t"
  "v_cvt_pk_bf16_f32 v111, v86, v87\n\t"
  "v_mfma_f32_16x16x32_bf16 a[28:31], a[184:187], v[128:131], a[28:31]\n\t"
  "v_exp_f32_e64 v88, v88\n\t"
  "v_mfma_f32_16x16x32_bf16 a[28:31], a[188:191], v[132:135], a[28:31]\n\t"
  "buffer_load_dwordx4 v12, s[12:15], s20 offen offset:2048 lds\n\t"
  "v_mfma_f32_16x16x32_bf16 a[60:63], a[184:187], v[136:139], a[60:63]\n\t"
  "v_exp_f32_e64 v89, v89\n\t"
  "v_mfma_f32_16x16x32_bf16 a[60:63], a[188:191], v[140:143], a[60:63]\n\t"
  "v_cvt_pk_bf16_f32 v118, v88, v89\n\t"
  "v_mfma_f32_16x16x32_bf16 a[92:95], a[184:187], v[144:147], a[92:95]\n\t"
  "v_exp_f32_e64 v90, v90\n\t"
  "v_mfma_f32_16x16x32_bf16 a[92:95], a[188:191], v[148:151], a[92:95]\n\t"
  "buffer_load_dwordx4 v13, s[12:15], s20 offen offset:3072 lds\n\t"
  "v_mfma_f32_16x16x32_bf16 a[124:127], a[184:187], v[152:155], a[124:127]\n\t"
  "s_add_u32 s20, s20, 0x4000\n\t"
  "v_exp_f32_e64 v91, v91\n\t"
  "v_mfma_f32_16x16x32_bf16 a[124:127], a[188:191], v[156:159], a[124:127]\n\t"
  "v_cvt_pk_bf16_f32 v119, v90, v91\n\t"
  "v_mfma_f32_16x16x32_bf16 a[192:195], a[208:211], v[128:131], a[192:195]\n\t"
  "v_exp_f32_e64 v92, v92\n\t"
  "v_mfma_f32_16x16x32_bf16 a[192:195], a[208:211], v[132:135], a[192:195]\n\t"
  "v_mfma_f32_16x16x32_bf16 a[196:199], a[208:211], v[136:139], a[196:199]\n\t"
  "v_exp_f32_e64 v93, v93\n\t"
  "v_mfma_f32_16x16x32_bf16 a[196:199], a[208:211], v[140:143], a[196:199]\n\t"
  "v_cvt_pk_bf16_f32 v126, v92, v93\n\t"
  "v_mfma_f32_16x16x32_bf16 a[200:203], a[208:211], v[144:147], a[200:203]\n\t"
  "v_exp_f32_e64 v94, v94\n\t"
  "v_mfma_f32_16x16x32_bf16 a[200:203], a[208:211], v[148:151], a[200:203]\n\t"
  "v_mfma_f32_16x16x32_bf16 a[204:207], a[208:211], v[152:155], a[204:207]\n\t"
  "v_exp_f32_e64 v95, v95\n\t"
  "v_mfma_f32_16x16x32_bf16 a[204:207], a[208:211], v[156:159], a[204:207]\n\t"
  "v_cvt_pk_bf16_f32 v127, v94, v95\n\t"
  "s_sub_u32 s17, s17, 1\n\t"
  "s_cmp_lg_u32 s17, 0\n\t"
  "s_cbranch_scc1 L_loop_%=\n\t"
  "s_waitcnt vmcnt(8)\n\t"
  "s_barrier\n\t"
  "s_lshr_b32 s41, %9, 4\n\t"
  "s_mul_i32 s42, %8, 0x2493\n\t"
  "s_lshr_b32 s42, s42, 16\n\t"
  "s_lshl_b32 s41, s41, 2\n\t"
  "s_add_u32 s41, s41, s42\n\t"
  "s_lshl_b32 s40, s41, 8\n\t"
  "s_add_u32 s40, s40, 2244608\n\t"
  "s_add_u32 s50, s64, s40\n\t"
  "s_addc_u32 s51, s65, 0\n\t"
  "v_mov_b32_e32 v14, 0\n\t"
  "s_cmp_eq_u32 %7, 0\n\t"
  "s_cbranch_scc0 L_nah_%=\n\t"
  "s_cmp_lt_u32 %8, 28\n\t"
  "s_cbranch_scc0 L_nah_%=\n\t"
  "v_mov_b32_e64 v15, 1\n\t"
  "s_mov_b64 exec, 1\n\t"
  "global_atomic_add v14, v15, s[50:51]\n\t"
  "s_mov_b64 exec, -1\n\t"
  "L_nah_%=:\n\t"
  "s_lshr_b32 s41, %12, 1\n\t"
  "s_lshl_b32 s41, s41, 2\n\t"
  "s_add_u32 s41, s41, %11\n\t"
  "s_lshl_b32 s40, s41, 8\n\t"
  "s_add_u32 s40, s40, 2244608\n\t"
  "s_add_u32 s50, s64, s40\n\t"
  "s_addc_u32 s51, s65, 0\n\t"
  "s_nop 0\n\t"
  "v_mfma_f32_16x16x32_bf16 v[32:35], a[128:131], v[160:163], v[224:227]\n\t"
  "s_waitcnt lgkmcnt(0)\n\t"
  "s_nop 0\n\t"
  "v_mfma_f32_16x16x32_bf16 v[32:35], a[132:135], v[164:167], v[32:35]\n\t"
  "ds_read_b128 a[144:147], v2 offset:20480\n\t"
  "v_mfma_f32_16x16x32_bf16 v[32:35], a[136:139], v[168:171], v[32:35]\n\t"
  "v_mfma_f32_16x16x32_bf16 v[32:35], a[140:143], v[172:175], v[32:35]\n\t"
  "ds_read_b128 a[148:151], v3 offset:20480\n\t"
  "v_mfma_f32_16x16x32_bf16 v[36:39], a[128:131], v[176:179], v[228:231]\n\t"
  "v_mfma_f32_16x16x32_bf16 v[36:39], a[132:135], v[180:183], v[36:39]\n\t"
  "ds_read_b128 a[152:155], v4 offset:20480\n\t"
  "v_mfma_f32_16x16x32_bf16 v[36:39], a[136:139], v[184:187], v[36:39]\n\t"
  "v_mfma_f32_16x16x32_bf16 v[36:39], a[140:143], v[188:191], v[36:39]\n\t"
  "ds_read_b128 a[156:159], v5 offset:20480\n\t"
  "v_mfma_f32_16x16x32_bf16 v[40:43], a[128:131], v[192:195], v[232:235]\n\t"
  "v_exp_f32_e64 v32, v32\n\t"
  "v_mfma_f32_16x16x32_bf16 v[40:43], a[132:135], v[196:199], v[40:43]\n\t"
  "v_mfma_f32_16x16x32_bf16 v[40:43], a[136:139], v[200:203], v[40:43]\n\t"
  "v_exp_f32_e64 v33, v33\n\t"
  "v_mfma_f32_16x16x32_bf16 v[40:43], a[140:143], v[204:207], v[40:43]\n\t"
  "v_cvt_pk_bf16_f32 v128, v32, v33\n\t"
  "v_mfma_f32_16x16x32_bf16 v[44:47], a[128:131], v[208:211], v[236:239]\n\t"
  "v_exp_f32_e64 v34, v34\n\t"
  "v_mfma_f32_16x16x32_bf16 v[44:47], a[132:135], v[212:215], v[44:47]\n\t"
  "v_mfma_f32_16x16x32_bf16 v[44:47], a[136:139], v[216:219], v[44:47]\n\t"
  "s_waitcnt lgkmcnt(0)\n\t"
  "v_exp_f32_e32 v35, v35\n\t"
  "v_mfma_f32_16x16x32_bf16 v[44:47], a[140:143], v[220:223], v[44:47]\n\t"
  "v_cvt_pk_bf16_f32 v129, v34, v35\n\t"
  "v_mfma_f32_16x16x32_bf16 v[48:51], a[144:147], v[160:163], v[224:227]\n\t"
  "v_exp_f32_e64 v36, v36\n\t"
  "v_mfma_f32_16x16x32_bf16 v[48:51], a[148:151], v[164:167], v[48:51]\n\t"
  "ds_read_b128 a[128:131], v2 offset:24576\n\t"
  "global_load_dwordx4 v[240:243], v1, s[24:25] offset:0\n\t"
  "v_mfma_f32_16x16x32_bf16 v[48:51], a[152:155], v[168:171], v[48:51]\n\t"
  "v_exp_f32_e64 v37, v37\n\t"
  "v_mfma_f32_16x16x32_bf16 v[48:51], a[156:159], v[172:175], v[48:51]\n\t"
  "ds_read_b128 a[132:135], v3 offset:24576\n\t"
  "v_cvt_pk_bf16_f32 v136, v36, v37\n\t"
  "v_mfma_f32_16x16x32_bf16 v[52:55], a[144:147], v[176:179], v[228:231]\n\t"
  "v_exp_f32_e64 v38, v38\n\t"
  "v_mfma_f32_16x16x32_bf16 v[52:55], a[148:151], v[180:183], v[52:55]\n\t"
  "ds_read_b128 a[136:139], v4 offset:24576\n\t"
  "global_load_dwordx4 v[244:247], v1, s[24:25] offset:1024\n\t"
  "v_mfma_f32_16x16x32_bf16 v[52:55], a[152:155], v[184:187], v[52:55]\n\t"
  "v_exp_f32_e64 v39, v39\n\t"
  "v_mfma_f32_16x16x32_bf16 v[52:55], a[156:159], v[188:191], v[52:55]\n\t"
  "ds_read_b128 a[140:143], v5 offset:24576\n\t"
  "v_cvt_pk_bf16_f32 v137, v38, v39\n\t"
  "v_mfma_f32_16x16x32_bf16 v[56:59], a[144:147], v[192:195], v[232:235]\n\t"
  "v_exp_f32_e64 v40, v40\n\t"
  "v_mfma_f32_16x16x32_bf16 v[56:59], a[148:151], v[196:199], v[56:59]\n\t"
  "global_load_dwordx4 v[248:251], v1, s[24:25] offset:2048\n\t"
  "v_mfma_f32_16x16x32_bf16 v[56:59], a[152:155], v[200:203], v[56:59]\n\t"
  "v_exp_f32_e64 v41, v41\n\t"
  "v_mfma_f32_16x16x32_bf16 v[56:59], a[156:159], v[204:207], v[56:59]\n\t"
  "v_cvt_pk_bf16_f32 v144, v40, v41\n\t"
  "v_mfma_f32_16x16x32_bf16 v[60:63], a[144:147], v[208:211], v[236:239]\n\t"
  "v_exp_f32_e64 v42, v42\n\t"
  "v_mfma_f32_16x16x32_bf16 v[60:63], a[148:151], v[212:215], v[60:63]\n\t"
  "global_load_dwordx4 v[252:255], v1, s[24:25] offset:2560\n\t"
  "v_mfma_f32_16x16x32_bf16 v[60:63], a[152:155], v[216:219], v[60:63]\n\t"
  "s_waitcnt lgkmcnt(0)\n\t"
  "v_exp_f32_e32 v43, v43\n\t"
  "v_mfma_f32_16x16x32_bf16 v[60:63], a[156:159], v[220:223], v[60:63]\n\t"
  "v_cvt_pk_bf16_f32 v145, v42, v43\n\t"
  "v_mfma_f32_16x16x32_bf16 v[64:67], a[128:131], v[160:163], v[224:227]\n\t"
  "v_exp_f32_e64 v44, v44\n\t"
  "v_mfma_f32_16x16x32_bf16 v[64:67], a[132:135], v[164:167], v[64:67]\n\t"
  "ds_read_b128 a[144:147], v2 offset:28672\n\t"
  "v_mfma_f32_16x16x32_bf16 v[64:67], a[136:139], v[168:171], v[64:67]\n\t"
  "v_exp_f32_e64 v45, v45\n\t"
  "v_mfma_f32_16x16x32_bf16 v[64:67], a[140:143], v[172:175], v[64:67]\n\t"
  "ds_read_b128 a[148:151], v3 offset:28672\n\t"
  "v_cvt_pk_bf16_f32 v152, v44, v45\n\t"
  "v_mfma_f32_16x16x32_bf16 v[68:71], a[128:131], v[176:179], v[228:231]\n\t"
  "v_exp_f32_e64 v46, v46\n\t"
  "v_mfma_f32_16x16x32_bf16 v[68:71], a[132:135], v[180:183], v[68:71]\n\t"
  "ds_read_b128 a[152:155], v4 offset:28672\n\t"
  "v_mfma_f32_16x16x32_bf16 v[68:71], a[136:139], v[184:187], v[68:71]\n\t"
  "v_exp_f32_e64 v47, v47\n\t"
  "v_mfma_f32_16x16x32_bf16 v[68:71], a[140:143], v[188:191], v[68:71]\n\t"
  "ds_read_b128 a[156:159], v5 offset:28672\n\t"
  "v_cvt_pk_bf16_f32 v153, v46, v47\n\t"
  "v_mfma_f32_16x16x32_bf16 v[72:75], a[128:131], v[192:195], v[232:235]\n\t"
  "s_add_u32 m0, s19, 32768\n\t"
  "v_exp_f32_e64 v48, v48\n\t"
  "v_mfma_f32_16x16x32_bf16 v[72:75], a[132:135], v[196:199], v[72:75]\n\t"
  "buffer_load_dwordx4 v12, s[12:15], s20 offen offset:0 lds\n\t"
  "v_mfma_f32_16x16x32_bf16 v[72:75], a[136:139], v[200:203], v[72:75]\n\t"
  "v_exp_f32_e64 v49, v49\n\t"
  "v_mfma_f32_16x16x32_bf16 v[72:75], a[140:143], v[204:207], v[72:75]\n\t"
  "v_cvt_pk_bf16_f32 v130, v48, v49\n\t"
  "v_mfma_f32_16x16x32_bf16 v[76:79], a[128:131], v[208:211], v[236:239]\n\t"
  "v_exp_f32_e64 v50, v50\n\t"
  "v_mfma_f32_16x16x32_bf16 v[76:79], a[132:135], v[212:215], v[76:79]\n\t"
  "buffer_load_dwordx4 v13, s[12:15], s20 offen offset:1024 lds\n\t"
  "v_mfma_f32_16x16x32_bf16 v[76:79], a[136:139], v[216:219], v[76:79]\n\t"
  "s_waitcnt lgkmcnt(0)\n\t"
  "v_exp_f32_e32 v51, v51\n\t"
  "v_mfma_f32_16x16x32_bf16 v[76:79], a[140:143], v[220:223], v[76:79]\n\t"
  "v_cvt_pk_bf16_f32 v131, v50, v51\n\t"
  "v_mfma_f32_16x16x32_bf16 v[80:83], a[144:147], v[160:163], v[224:227]\n\t"
  "v_exp_f32_e64 v52, v52\n\t"
  "v_mfma_f32_16x16x32_bf16 v[80:83], a[148:151], v[164:167], v[80:83]\n\t"
  "ds_read_b64_tr_b16 a[160:161], v6 offset:0\n\t"
  "v_mfma_f32_16x16x32_bf16 v[80:83], a[152:155], v[168:171], v[80:83]\n\t"
  "v_exp_f32_e64 v53, v53\n\t"
  "v_mfma_f32_16x16x32_bf16 v[80:83], a[156:159], v[172:175], v[80:83]\n\t"
  "ds_read_b64_tr_b16 a[162:163], v6 offset:4096\n\t"
  "v_cvt_pk_bf16_f32 v138, v52, v53\n\t"
  "v_mfma_f32_16x16x32_bf16 v[84:87], a[144:147], v[176:179], v[228:231]\n\t"
  "v_exp_f32_e64 v54, v54\n\t"
  "v_mfma_f32_16x16x32_bf16 v[84:87], a[148:151], v[180:183], v[84:87]\n\t"
  "ds_read_b64_tr_b16 a[164:165], v6 offset:8192\n\t"
  "v_mfma_f32_16x16x32_bf16 v[84:87], a[152:155], v[184:187], v[84:87]\n\t"
  "v_exp_f32_e64 v55, v55\n\t"
  "v_mfma_f32_16x16x32_bf16 v[84:87], a[156:159], v[188:191], v[84:87]\n\t"
  "ds_read_b64_tr_b16 a[166:167], v6 offset:12288\n\t"
  "v_cvt_pk_bf16_f32 v139, v54, v55\n\t"
  "v_mfma_f32_16x16x32_bf16 v[88:91], a[144:147], v[192:195], v[232:235]\n\t"
  "v_exp_f32_e64 v56, v56\n\t"
  "v_mfma_f32_16x16x32_bf16 v[88:91], a[148:151], v[196:199], v[88:91]\n\t"
  "ds_read_b64_tr_b16 a[168:169], v7 offset:0\n\t"
  "v_mfma_f32_16x16x32_bf16 v[88:91], a[152:155], v[200:203], v[88:91]\n\t"
  "v_exp_f32_e64 v57, v57\n\t"
  "v_mfma_f32_16x16x32_bf16 v[88:91], a[156:159], v[204:207], v[88:91]\n\t"
  "ds_read_b64_tr_b16 a[170:171], v7 offset:4096\n\t"
  "v_cvt_pk_bf16_f32 v146, v56, v57\n\t"
  "v_mfma_f32_16x16x32_bf16 v[92:95], a[144:147], v[208:211], v[236:239]\n\t"
  "v_exp_f32_e64 v58, v58\n\t"
  "v_mfma_f32_16x16x32_bf16 v[92:95], a[148:151], v[212:215], v[92:95]\n\t"
  "ds_read_b64_tr_b16 a[172:173], v7 offset:8192\n\t"
  "v_mfma_f32_16x16x32_bf16 v[92:95], a[152:155], v[216:219], v[92:95]\n\t"
  "s_waitcnt lgkmcnt(2)\n\t"
  "v_exp_f32_e32 v59, v59\n\t"
  "v_mfma_f32_16x16x32_bf16 v[92:95], a[156:159], v[220:223], v[92:95]\n\t"
  "ds_read_b64_tr_b16 a[174:175], v7 offset:12288\n\t"
  "v_cvt_pk_bf16_f32 v147, v58, v59\n\t"
  "v_mfma_f32_16x16x32_bf16 a[0:3], a[160:163], v[96:99], a[0:3]\n\t"
  "v_exp_f32_e64 v60, v60\n\t"
  "v_mfma_f32_16x16x32_bf16 a[0:3], a[164:167], v[100:103], a[0:3]\n\t"
  "ds_read_b64_tr_b16 a[176:177], v6 offset:512\n\t"
  "v_mfma_f32_16x16x32_bf16 a[32:35], a[160:163], v[104:107], a[32:35]\n\t"
  "v_exp_f32_e64 v61, v61\n\t"
  "v_mfma_f32_16x16x32_bf16 a[32:35], a[164:167], v[108:111], a[32:35]\n\t"
  "ds_read_b64_tr_b16 a[178:179], v6 offset:4608\n\t"
  "v_cvt_pk_bf16_f32 v154, v60, v61\n\t"
  "v_mfma_f32_16x16x32_bf16 a[64:67], a[160:163], v[112:115], a[64:67]\n\t"
  "v_exp_f32_e64 v62, v62\n\t"
  "v_mfma_f32_16x16x32_bf16 a[64:67], a[164:167], v[116:119], a[64:67]\n\t"
  "ds_read_b64_tr_b16 a[180:181], v6 offset:8704\n\t"
  "v_mfma_f32_16x16x32_bf16 a[96:99], a[160:163], v[120:123], a[96:99]\n\t"
  "s_waitcnt lgkmcnt(2)\n\t"
  "v_exp_f32_e32 v63, v63\n\t"
  "v_mfma_f32_16x16x32_bf16 a[96:99], a[164:167], v[124:127], a[96:99]\n\t"
  "ds_read_b64_tr_b16 a[182:183], v6 offset:12800\n\t"
  "v_cvt_pk_bf16_f32 v155, v62, v63\n\t"
  "v_mfma_f32_16x16x32_bf16 a[4:7], a[168:171], v[96:99], a[4:7]\n\t"
  "v_exp_f32_e64 v64, v64\n\t"
  "v_mfma_f32_16x16x32_bf16 a[4:7], a[172:175], v[100:103], a[4:7]\n\t"
  "ds_read_b64_tr_b16 a[184:185], v7 offset:512\n\t"
  "v_mfma_f32_16x16x32_bf16 a[36:39], a[168:171], v[104:107], a[36:39]\n\t"
  "v_exp_f32_e64 v65, v65\n\t"
  "v_mfma_f32_16x16x32_bf16 a[36:39], a[172:175], v[108:111], a[36:39]\n\t"
  "ds_read_b64_tr_b16 a[186:187], v7 offset:4608\n\t"
  "v_cvt_pk_bf16_f32 v132, v64, v65\n\t"
  "v_mfma_f32_16x16x32_bf16 a[68:71], a[168:171], v[112:115], a[68:71]\n\t"
  "v_exp_f32_e64 v66, v66\n\t"
  "v_mfma_f32_16x16x32_bf16 a[68:71], a[172:175], v[116:119], a[68:71]\n\t"
  "ds_read_b64_tr_b16 a[188:189], v7 offset:8704\n\t"
  "v_mfma_f32_16x16x32_bf16 a[100:103], a[168:171], v[120:123], a[100:103]\n\t"
  "s_waitcnt lgkmcnt(2)\n\t"
  "v_exp_f32_e32 v67, v67\n\t"
  "v_mfma_f32_16x16x32_bf16 a[100:103], a[172:175], v[124:127], a[100:103]\n\t"
  "ds_read_b64_tr_b16 a[190:191], v7 offset:12800\n\t"
  "v_cvt_pk_bf16_f32 v133, v66, v67\n\t"
  "v_mfma_f32_16x16x32_bf16 a[8:11], a[176:179], v[96:99], a[8:11]\n\t"
  "v_exp_f32_e64 v68, v68\n\t"
  "v_mfma_f32_16x16x32_bf16 a[8:11], a[180:183], v[100:103], a[8:11]\n\t"
  "ds_read_b64_tr_b16 a[160:161], v6 offset:1024\n\t"
  "v_mfma_f32_16x16x32_bf16 a[40:43], a[176:179], v[104:107], a[40:43]\n\t"
  "v_exp_f32_e64 v69, v69\n\t"
  "v_mfma_f32_16x16x32_bf16 a[40:43], a[180:183], v[108:111], a[40:43]\n\t"
  "ds_read_b64_tr_b16 a[162:163], v6 offset:5120\n\t"
  "v_cvt_pk_bf16_f32 v140, v68, v69\n\t"
  "v_mfma_f32_16x16x32_bf16 a[72:75], a[176:179], v[112:115], a[72:75]\n\t"
  "v_exp_f32_e64 v70, v70\n\t"
  "v_mfma_f32_16x16x32_bf16 a[72:75], a[180:183], v[116:119], a[72:75]\n\t"
  "ds_read_b64_tr_b16 a[164:165], v6 offset:9216\n\t"
  "v_mfma_f32_16x16x32_bf16 a[104:107], a[176:179], v[120:123], a[104:107]\n\t"
  "s_waitcnt lgkmcnt(2)\n\t"
  "v_exp_f32_e32 v71, v71\n\t"
  "v_mfma_f32_16x16x32_bf16 a[104:107], a[180:183], v[124:127], a[104:107]\n\t"
  "ds_read_b64_tr_b16 a[166:167], v6 offset:13312\n\t"
  "v_cvt_pk_bf16_f32 v141, v70, v71\n\t"
  "v_mfma_f32_16x16x32_bf16 a[12:15], a[184:187], v[96:99], a[12:15]\n\t"
  "v_exp_f32_e64 v72, v72\n\t"
  "v_mfma_f32_16x16x32_bf16 a[12:15], a[188:191], v[100:103], a[12:15]\n\t"
  "ds_read_b64_tr_b16 a[168:169], v7 offset:1024\n\t"
  "v_mfma_f32_16x16x32_bf16 a[44:47], a[184:187], v[104:107], a[44:47]\n\t"
  "v_exp_f32_e64 v73, v73\n\t"
  "v_mfma_f32_16x16x32_bf16 a[44:47], a[188:191], v[108:111], a[44:47]\n\t"
  "ds_read_b64_tr_b16 a[170:171], v7 offset:5120\n\t"
  "v_cvt_pk_bf16_f32 v148, v72, v73\n\t"
  "v_mfma_f32_16x16x32_bf16 a[76:79], a[184:187], v[112:115], a[76:79]\n\t"
  "v_exp_f32_e64 v74, v74\n\t"
  "v_mfma_f32_16x16x32_bf16 a[76:79], a[188:191], v[116:119], a[76:79]\n\t"
  "ds_read_b64_tr_b16 a[172:173], v7 offset:9216\n\t"
  "v_mfma_f32_16x16x32_bf16 a[108:111], a[184:187], v[120:123], a[108:111]\n\t"
  "s_waitcnt lgkmcnt(2)\n\t"
  "v_exp_f32_e32 v75, v75\n\t"
  "v_mfma_f32_16x16x32_bf16 a[108:111], a[188:191], v[124:127], a[108:111]\n\t"
  "ds_read_b64_tr_b16 a[174:175], v7 offset:13312\n\t"
  "v_cvt_pk_bf16_f32 v149, v74, v75\n\t"
  "v_mfma_f32_16x16x32_bf16 a[16:19], a[160:163], v[96:99], a[16:19]\n\t"
  "v_exp_f32_e64 v76, v76\n\t"
  "v_mfma_f32_16x16x32_bf16 a[16:19], a[164:167], v[100:103], a[16:19]\n\t"
  "ds_read_b64_tr_b16 a[176:177], v6 offset:1536\n\t"
  "v_mfma_f32_16x16x32_bf16 a[48:51], a[160:163], v[104:107], a[48:51]\n\t"
  "v_exp_f32_e64 v77, v77\n\t"
  "v_mfma_f32_16x16x32_bf16 a[48:51], a[164:167], v[108:111], a[48:51]\n\t"
  "ds_read_b64_tr_b16 a[178:179], v6 offset:5632\n\t"
  "v_cvt_pk_bf16_f32 v156, v76, v77\n\t"
  "v_mfma_f32_16x16x32_bf16 a[80:83], a[160:163], v[112:115], a[80:83]\n\t"
  "v_exp_f32_e64 v78, v78\n\t"
  "v_mfma_f32_16x16x32_bf16 a[80:83], a[164:167], v[116:119], a[80:83]\n\t"
  "ds_read_b64_tr_b16 a[180:181], v6 offset:9728\n\t"
  "v_mfma_f32_16x16x32_bf16 a[112:115], a[160:163], v[120:123], a[112:115]\n\t"
  "s_waitcnt lgkmcnt(2)\n\t"
  "v_exp_f32_e32 v79, v79\n\t"
  "v_mfma_f32_16x16x32_bf16 a[112:115], a[164:167], v[124:127], a[112:115]\n\t"
  "ds_read_b64_tr_b16 a[182:183], v6 offset:13824\n\t"
  "v_cvt_pk_bf16_f32 v157, v78, v79\n\t"
  "v_mfma_f32_16x16x32_bf16 a[20:23], a[168:171], v[96:99], a[20:23]\n\t"
  "v_exp_f32_e64 v80, v80\n\t"
  "v_mfma_f32_16x16x32_bf16 a[20:23], a[172:175], v[100:103], a[20:23]\n\t"
  "ds_read_b64_tr_b16 a[184:185], v7 offset:1536\n\t"
  "v_mfma_f32_16x16x32_bf16 a[52:55], a[168:171], v[104:107], a[52:55]\n\t"
  "v_exp_f32_e64 v81, v81\n\t"
  "v_mfma_f32_16x16x32_bf16 a[52:55], a[172:175], v[108:111], a[52:55]\n\t"
  "ds_read_b64_tr_b16 a[186:187], v7 offset:5632\n\t"
  "v_cvt_pk_bf16_f32 v134, v80, v81\n\t"
  "v_mfma_f32_16x16x32_bf16 a[84:87], a[168:171], v[112:115], a[84:87]\n\t"
  "v_exp_f32_e64 v82, v82\n\t"
  "v_mfma_f32_16x16x32_bf16 a[84:87], a[172:175], v[116:119], a[84:87]\n\t"
  "ds_read_b64_tr_b16 a[188:189], v7 offset:9728\n\t"
  "v_mfma_f32_16x16x32_bf16 a[116:119], a[168:171], v[120:123], a[116:119]\n\t"
  "s_waitcnt lgkmcnt(2)\n\t"
  "v_exp_f32_e32 v83, v83\n\t"
  "v_mfma_f32_16x16x32_bf16 a[116:119], a[172:175], v[124:127], a[116:119]\n\t"
  "ds_read_b64_tr_b16 a[190:191], v7 offset:13824\n\t"
  "v_cvt_pk_bf16_f32 v135, v82, v83\n\t"
  "v_mfma_f32_16x16x32_bf16 a[24:27], a[176:179], v[96:99], a[24:27]\n\t"
  "v_exp_f32_e64 v84, v84\n\t"
  "v_mfma_f32_16x16x32_bf16 a[24:27], a[180:183], v[100:103], a[24:27]\n\t"
  "ds_read_b128 a[128:131], v2 offset:32768\n\t"
  "v_mfma_f32_16x16x32_bf16 a[56:59], a[176:179], v[104:107], a[56:59]\n\t"
  "v_exp_f32_e64 v85, v85\n\t"
  "v_mfma_f32_16x16x32_bf16 a[56:59], a[180:183], v[108:111], a[56:59]\n\t"
  "ds_read_b128 a[132:135], v3 offset:32768\n\t"
  "v_cvt_pk_bf16_f32 v142, v84, v85\n\t"
  "v_mfma_f32_16x16x32_bf16 a[88:91], a[176:179], v[112:115], a[88:91]\n\t"
  "v_exp_f32_e64 v86, v86\n\t"
  "v_mfma_f32_16x16x32_bf16 a[88:91], a[180:183], v[116:119], a[88:91]\n\t"
  "ds_read_b128 a[136:139], v4 offset:32768\n\t"
  "v_mfma_f32_16x16x32_bf16 a[120:123], a[176:179], v[120:123], a[120:123]\n\t"
  "s_waitcnt lgkmcnt(2)\n\t"
  "v_exp_f32_e32 v87, v87\n\t"
  "v_mfma_f32_16x16x32_bf16 a[120:123], a[180:183], v[124:127], a[120:123]\n\t"
  "ds_read_b128 a[140:143], v5 offset:32768\n\t"
  "v_cvt_pk_bf16_f32 v143, v86, v87\n\t"
  "v_mfma_f32_16x16x32_bf16 a[28:31], a[184:187], v[96:99], a[28:31]\n\t"
  "v_exp_f32_e64 v88, v88\n\t"
  "v_mfma_f32_16x16x32_bf16 a[28:31], a[188:191], v[100:103], a[28:31]\n\t"
  "buffer_load_dwordx4 v12, s[12:15], s20 offen offset:2048 lds\n\t"
  "v_mfma_f32_16x16x32_bf16 a[60:63], a[184:187], v[104:107], a[60:63]\n\t"
  "v_exp_f32_e64 v89, v89\n\t"
  "v_mfma_f32_16x16x32_bf16 a[60:63], a[188:191], v[108:111], a[60:63]\n\t"
  "v_cvt_pk_bf16_f32 v150, v88, v89\n\t"
  "v_mfma_f32_16x16x32_bf16 a[92:95], a[184:187], v[112:115], a[92:95]\n\t"
  "v_exp_f32_e64 v90, v90\n\t"
  "v_mfma_f32_16x16x32_bf16 a[92:95], a[188:191], v[116:119], a[92:95]\n\t"
  "buffer_load_dwordx4 v13, s[12:15], s20 offen offset:3072 lds\n\t"
  "v_mfma_f32_16x16x32_bf16 a[124:127], a[184:187], v[120:123], a[124:127]\n\t"
  "s_add_u32 s20, s20, 0x4000\n\t"
  "v_exp_f32_e64 v91, v91\n\t"
  "v_mfma_f32_16x16x32_bf16 a[124:127], a[188:191], v[124:127], a[124:127]\n\t"
  "v_cvt_pk_bf16_f32 v151, v90, v91\n\t"
  "v_mfma_f32_16x16x32_bf16 a[192:195], a[208:211], v[96:99], a[192:195]\n\t"
  "v_exp_f32_e64 v92, v92\n\t"
  "v_mfma_f32_16x16x32_bf16 a[192:195], a[208:211], v[100:103], a[192:195]\n\t"
  "v_mfma_f32_16x16x32_bf16 a[196:199], a[208:211], v[104:107], a[196:199]\n\t"
  "v_exp_f32_e64 v93, v93\n\t"
  "v_mfma_f32_16x16x32_bf16 a[196:199], a[208:211], v[108:111], a[196:199]\n\t"
  "v_cvt_pk_bf16_f32 v158, v92, v93\n\t"
  "v_mfma_f32_16x16x32_bf16 a[200:203], a[208:211], v[112:115], a[200:203]\n\t"
  "v_exp_f32_e64 v94, v94\n\t"
  "v_mfma_f32_16x16x32_bf16 a[200:203], a[208:211], v[116:119], a[200:203]\n\t"
  "v_mfma_f32_16x16x32_bf16 a[204:207], a[208:211], v[120:123], a[204:207]\n\t"
  "v_exp_f32_e64 v95, v95\n\t"
  "v_mfma_f32_16x16x32_bf16 a[204:207], a[208:211], v[124:127], a[204:207]\n\t"
  "v_cvt_pk_bf16_f32 v159, v94, v95\n\t"
  "s_waitcnt vmcnt(4)\n\t"
  "s_barrier\n\t"
  "v_mfma_f32_16x16x32_bf16 v[32:35], a[128:131], v[160:163], v[224:227]\n\t"
  "s_waitcnt lgkmcnt(0)\n\t"
  "s_nop 0\n\t"
  "v_mfma_f32_16x16x32_bf16 v[32:35], a[132:135], v[164:167], v[32:35]\n\t"
  "ds_read_b128 a[144:147], v2 offset:36864\n\t"
  "v_mfma_f32_16x16x32_bf16 v[32:35], a[136:139], v[168:171], v[32:35]\n\t"
  "v_mfma_f32_16x16x32_bf16 v[32:35], a[140:143], v[172:175], v[32:35]\n\t"
  "ds_read_b128 a[148:151], v3 offset:36864\n\t"
  "v_mfma_f32_16x16x32_bf16 v[36:39], a[128:131], v[176:179], v[228:231]\n\t"
  "v_mfma_f32_16x16x32_bf16 v[36:39], a[132:135], v[180:183], v[36:39]\n\t"
  "ds_read_b128 a[152:155], v4 offset:36864\n\t"
  "v_mfma_f32_16x16x32_bf16 v[36:39], a[136:139], v[184:187], v[36:39]\n\t"
  "v_mfma_f32_16x16x32_bf16 v[36:39], a[140:143], v[188:191], v[36:39]\n\t"
  "ds_read_b128 a[156:159], v5 offset:36864\n\t"
  "v_mfma_f32_16x16x32_bf16 v[40:43], a[128:131], v[192:195], v[232:235]\n\t"
  "v_exp_f32_e64 v32, v32\n\t"
  "v_mfma_f32_16x16x32_bf16 v[40:43], a[132:135], v[196:199], v[40:43]\n\t"
  "v_mfma_f32_16x16x32_bf16 v[40:43], a[136:139], v[200:203], v[40:43]\n\t"
  "v_exp_f32_e64 v33, v33\n\t"
  "v_mfma_f32_16x16x32_bf16 v[40:43], a[140:143], v[204:207], v[40:43]\n\t"
  "v_cvt_pk_bf16_f32 v96, v32, v33\n\t"
  "v_mfma_f32_16x16x32_bf16 v[44:47], a[128:131], v[208:211], v[236:239]\n\t"
  "v_exp_f32_e64 v34, v34\n\t"
  "v_mfma_f32_16x16x32_bf16 v[44:47], a[132:135], v[212:215], v[44:47]\n\t"
  "v_mfma_f32_16x16x32_bf16 v[44:47], a[136:139], v[216:219], v[44:47]\n\t"
  "s_waitcnt lgkmcnt(0)\n\t"
  "v_exp_f32_e32 v35, v35\n\t"
  "v_mfma_f32_16x16x32_bf16 v[44:47], a[140:143], v[220:223], v[44:47]\n\t"
  "v_cvt_pk_bf16_f32 v97, v34, v35\n\t"
  "v_mfma_f32_16x16x32_bf16 v[48:51], a[144:147], v[160:163], v[224:227]\n\t"
  "v_exp_f32_e64 v36, v36\n\t"
  "v_mfma_f32_16x16x32_bf16 v[48:51], a[148:151], v[164:167], v[48:51]\n\t"
  "ds_read_b128 a[128:131], v2 offset:40960\n\t"
  "global_load_dwordx4 v[240:243], v1, s[26:27] offset:0\n\t"
  "v_mfma_f32_16x16x32_bf16 v[48:51], a[152:155], v[168:171], v[48:51]\n\t"
  "v_exp_f32_e64 v37, v37\n\t"
  "v_mfma_f32_16x16x32_bf16 v[48:51], a[156:159], v[172:175], v[48:51]\n\t"
  "ds_read_b128 a[132:135], v3 offset:40960\n\t"
  "v_cvt_pk_bf16_f32 v104, v36, v37\n\t"
  "v_mfma_f32_16x16x32_bf16 v[52:55], a[144:147], v[176:179], v[228:231]\n\t"
  "v_exp_f32_e64 v38, v38\n\t"
  "v_mfma_f32_16x16x32_bf16 v[52:55], a[148:151], v[180:183], v[52:55]\n\t"
  "ds_read_b128 a[136:139], v4 offset:40960\n\t"
  "global_load_dwordx4 v[244:247], v1, s[26:27] offset:1024\n\t"
  "v_mfma_f32_16x16x32_bf16 v[52:55], a[152:155], v[184:187], v[52:55]\n\t"
  "v_exp_f32_e64 v39, v39\n\t"
  "v_mfma_f32_16x16x32_bf16 v[52:55], a[156:159], v[188:191], v[52:55]\n\t"
  "ds_read_b128 a[140:143], v5 offset:40960\n\t"
  "v_cvt_pk_bf16_f32 v105, v38, v39\n\t"
  "v_mfma_f32_16x16x32_bf16 v[56:59], a[144:147], v[192:195], v[232:235]\n\t"
  "v_exp_f32_e64 v40, v40\n\t"
  "v_mfma_f32_16x16x32_bf16 v[56:59], a[148:151], v[196:199], v[56:59]\n\t"
  "global_load_dwordx4 v[248:251], v1, s[26:27] offset:2048\n\t"
  "v_mfma_f32_16x16x32_bf16 v[56:59], a[152:155], v[200:203], v[56:59]\n\t"
  "v_exp_f32_e64 v41, v41\n\t"
  "v_mfma_f32_16x16x32_bf16 v[56:59], a[156:159], v[204:207], v[56:59]\n\t"
  "v_cvt_pk_bf16_f32 v112, v40, v41\n\t"
  "v_mfma_f32_16x16x32_bf16 v[60:63], a[144:147], v[208:211], v[236:239]\n\t"
  "v_exp_f32_e64 v42, v42\n\t"
  "v_mfma_f32_16x16x32_bf16 v[60:63], a[148:151], v[212:215], v[60:63]\n\t"
  "global_load_dwordx4 v[252:255], v1, s[26:27] offset:3072\n\t"
  "v_mfma_f32_16x16x32_bf16 v[60:63], a[152:155], v[216:219], v[60:63]\n\t"
  "s_waitcnt lgkmcnt(0)\n\t"
  "v_exp_f32_e32 v43, v43\n\t"
  "v_mfma_f32_16x16x32_bf16 v[60:63], a[156:159], v[220:223], v[60:63]\n\t"
  "v_cvt_pk_bf16_f32 v113, v42, v43\n\t"
  "v_mfma_f32_16x16x32_bf16 v[64:67], a[128:131], v[160:163], v[224:227]\n\t"
  "v_exp_f32_e64 v44, v44\n\t"
  "v_mfma_f32_16x16x32_bf16 v[64:67], a[132:135], v[164:167], v[64:67]\n\t"
  "ds_read_b128 a[144:147], v2 offset:45056\n\t"
  "v_mfma_f32_16x16x32_bf16 v[64:67], a[136:139], v[168:171], v[64:67]\n\t"
  "v_exp_f32_e64 v45, v45\n\t"
  "v_mfma_f32_16x16x32_bf16 v[64:67], a[140:143], v[172:175], v[64:67]\n\t"
  "ds_read_b128 a[148:151], v3 offset:45056\n\t"
  "v_cvt_pk_bf16_f32 v120, v44, v45\n\t"
  "v_mfma_f32_16x16x32_bf16 v[68:71], a[128:131], v[176:179], v[228:231]\n\t"
  "v_exp_f32_e64 v46, v46\n\t"
  "v_mfma_f32_16x16x32_bf16 v[68:71], a[132:135], v[180:183], v[68:71]\n\t"
  "ds_read_b128 a[152:155], v4 offset:45056\n\t"
  "v_mfma_f32_16x16x32_bf16 v[68:71], a[136:139], v[184:187], v[68:71]\n\t"
  "v_exp_f32_e64 v47, v47\n\t"
  "v_mfma_f32_16x16x32_bf16 v[68:71], a[140:143], v[188:191], v[68:71]\n\t"
  "ds_read_b128 a[156:159], v5 offset:45056\n\t"
  "v_cvt_pk_bf16_f32 v121, v46, v47\n\t"
  "v_mfma_f32_16x16x32_bf16 v[72:75], a[128:131], v[192:195], v[232:235]\n\t"
  "s_add_u32 m0, s19, 49152\n\t"
  "v_exp_f32_e64 v48, v48\n\t"
  "v_mfma_f32_16x16x32_bf16 v[72:75], a[132:135], v[196:199], v[72:75]\n\t"
  "buffer_load_dwordx4 v12, s[12:15], s20 offen offset:0 lds\n\t"
  "v_mfma_f32_16x16x32_bf16 v[72:75], a[136:139], v[200:203], v[72:75]\n\t"
  "v_exp_f32_e64 v49, v49\n\t"
  "v_mfma_f32_16x16x32_bf16 v[72:75], a[140:143], v[204:207], v[72:75]\n\t"
  "v_cvt_pk_bf16_f32 v98, v48, v49\n\t"
  "v_mfma_f32_16x16x32_bf16 v[76:79], a[128:131], v[208:211], v[236:239]\n\t"
  "v_exp_f32_e64 v50, v50\n\t"
  "v_mfma_f32_16x16x32_bf16 v[76:79], a[132:135], v[212:215], v[76:79]\n\t"
  "buffer_load_dwordx4 v13, s[12:15], s20 offen offset:1024 lds\n\t"
  "v_mfma_f32_16x16x32_bf16 v[76:79], a[136:139], v[216:219], v[76:79]\n\t"
  "s_waitcnt lgkmcnt(0)\n\t"
  "v_exp_f32_e32 v51, v51\n\t"
  "v_mfma_f32_16x16x32_bf16 v[76:79], a[140:143], v[220:223], v[76:79]\n\t"
  "v_cvt_pk_bf16_f32 v99, v50, v51\n\t"
  "v_mfma_f32_16x16x32_bf16 v[80:83], a[144:147], v[160:163], v[224:227]\n\t"
  "v_exp_f32_e64 v52, v52\n\t"
  "v_mfma_f32_16x16x32_bf16 v[80:83], a[148:151], v[164:167], v[80:83]\n\t"
  "ds_read_b64_tr_b16 a[160:161], v6 offset:16384\n\t"
  "v_mfma_f32_16x16x32_bf16 v[80:83], a[152:155], v[168:171], v[80:83]\n\t"
  "v_exp_f32_e64 v53, v53\n\t"
  "v_mfma_f32_16x16x32_bf16 v[80:83], a[156:159], v[172:175], v[80:83]\n\t"
  "ds_read_b64_tr_b16 a[162:163], v6 offset:20480\n\t"
  "v_cvt_pk_bf16_f32 v106, v52, v53\n\t"
  "v_mfma_f32_16x16x32_bf16 v[84:87], a[144:147], v[176:179], v[228:231]\n\t"
  "v_exp_f32_e64 v54, v54\n\t"
  "v_mfma_f32_16x16x32_bf16 v[84:87], a[148:151], v[180:183], v[84:87]\n\t"
  "ds_read_b64_tr_b16 a[164:165], v6 offset:24576\n\t"
  "v_mfma_f32_16x16x32_bf16 v[84:87], a[152:155], v[184:187], v[84:87]\n\t"
  "v_exp_f32_e64 v55, v55\n\t"
  "v_mfma_f32_16x16x32_bf16 v[84:87], a[156:159], v[188:191], v[84:87]\n\t"
  "ds_read_b64_tr_b16 a[166:167], v6 offset:28672\n\t"
  "v_cvt_pk_bf16_f32 v107, v54, v55\n\t"
  "v_mfma_f32_16x16x32_bf16 v[88:91], a[144:147], v[192:195], v[232:235]\n\t"
  "v_exp_f32_e64 v56, v56\n\t"
  "v_mfma_f32_16x16x32_bf16 v[88:91], a[148:151], v[196:199], v[88:91]\n\t"
  "ds_read_b64_tr_b16 a[168:169], v7 offset:16384\n\t"
  "v_mfma_f32_16x16x32_bf16 v[88:91], a[152:155], v[200:203], v[88:91]\n\t"
  "v_exp_f32_e64 v57, v57\n\t"
  "v_mfma_f32_16x16x32_bf16 v[88:91], a[156:159], v[204:207], v[88:91]\n\t"
  "ds_read_b64_tr_b16 a[170:171], v7 offset:20480\n\t"
  "v_cvt_pk_bf16_f32 v114, v56, v57\n\t"
  "v_mfma_f32_16x16x32_bf16 v[92:95], a[144:147], v[208:211], v[236:239]\n\t"
  "v_exp_f32_e64 v58, v58\n\t"
  "v_mfma_f32_16x16x32_bf16 v[92:95], a[148:151], v[212:215], v[92:95]\n\t"
  "ds_read_b64_tr_b16 a[172:173], v7 offset:24576\n\t"
  "v_mfma_f32_16x16x32_bf16 v[92:95], a[152:155], v[216:219], v[92:95]\n\t"
  "s_waitcnt lgkmcnt(2)\n\t"
  "v_exp_f32_e32 v59, v59\n\t"
  "v_mfma_f32_16x16x32_bf16 v[92:95], a[156:159], v[220:223], v[92:95]\n\t"
  "ds_read_b64_tr_b16 a[174:175], v7 offset:28672\n\t"
  "v_cvt_pk_bf16_f32 v115, v58, v59\n\t"
  "v_mfma_f32_16x16x32_bf16 a[0:3], a[160:163], v[128:131], a[0:3]\n\t"
  "v_exp_f32_e64 v60, v60\n\t"
  "v_mfma_f32_16x16x32_bf16 a[0:3], a[164:167], v[132:135], a[0:3]\n\t"
  "ds_read_b64_tr_b16 a[176:177], v6 offset:16896\n\t"
  "v_mfma_f32_16x16x32_bf16 a[32:35], a[160:163], v[136:139], a[32:35]\n\t"
  "v_exp_f32_e64 v61, v61\n\t"
  "v_mfma_f32_16x16x32_bf16 a[32:35], a[164:167], v[140:143], a[32:35]\n\t"
  "ds_read_b64_tr_b16 a[178:179], v6 offset:20992\n\t"
  "v_cvt_pk_bf16_f32 v122, v60, v61\n\t"
  "v_mfma_f32_16x16x32_bf16 a[64:67], a[160:163], v[144:147], a[64:67]\n\t"
  "v_exp_f32_e64 v62, v62\n\t"
  "v_mfma_f32_16x16x32_bf16 a[64:67], a[164:167], v[148:151], a[64:67]\n\t"
  "ds_read_b64_tr_b16 a[180:181], v6 offset:25088\n\t"
  "v_mfma_f32_16x16x32_bf16 a[96:99], a[160:163], v[152:155], a[96:99]\n\t"
  "s_waitcnt lgkmcnt(2)\n\t"
  "v_exp_f32_e32 v63, v63\n\t"
  "v_mfma_f32_16x16x32_bf16 a[96:99], a[164:167], v[156:159], a[96:99]\n\t"
  "ds_read_b64_tr_b16 a[182:183], v6 offset:29184\n\t"
  "v_cvt_pk_bf16_f32 v123, v62, v63\n\t"
  "v_mfma_f32_16x16x32_bf16 a[4:7], a[168:171], v[128:131], a[4:7]\n\t"
  "v_exp_f32_e64 v64, v64\n\t"
  "v_mfma_f32_16x16x32_bf16 a[4:7], a[172:175], v[132:135], a[4:7]\n\t"
  "ds_read_b64_tr_b16 a[184:185], v7 offset:16896\n\t"
  "v_mfma_f32_16x16x32_bf16 a[36:39], a[168:171], v[136:139], a[36:39]\n\t"
  "v_exp_f32_e64 v65, v65\n\t"
  "v_mfma_f32_16x16x32_bf16 a[36:39], a[172:175], v[140:143], a[36:39]\n\t"
  "ds_read_b64_tr_b16 a[186:187], v7 offset:20992\n\t"
  "v_cvt_pk_bf16_f32 v100, v64, v65\n\t"
  "v_mfma_f32_16x16x32_bf16 a[68:71], a[168:171], v[144:147], a[68:71]\n\t"
  "v_exp_f32_e64 v66, v66\n\t"
  "v_mfma_f32_16x16x32_bf16 a[68:71], a[172:175], v[148:151], a[68:71]\n\t"
  "ds_read_b64_tr_b16 a[188:189], v7 offset:25088\n\t"
  "v_mfma_f32_16x16x32_bf16 a[100:103], a[168:171], v[152:155], a[100:103]\n\t"
  "s_waitcnt lgkmcnt(2)\n\t"
  "v_exp_f32_e32 v67, v67\n\t"
  "v_mfma_f32_16x16x32_bf16 a[100:103], a[172:175], v[156:159], a[100:103]\n\t"
  "ds_read_b64_tr_b16 a[190:191], v7 offset:29184\n\t"
  "v_cvt_pk_bf16_f32 v101, v66, v67\n\t"
  "v_mfma_f32_16x16x32_bf16 a[8:11], a[176:179], v[128:131], a[8:11]\n\t"
  "v_exp_f32_e64 v68, v68\n\t"
  "v_mfma_f32_16x16x32_bf16 a[8:11], a[180:183], v[132:135], a[8:11]\n\t"
  "ds_read_b64_tr_b16 a[160:161], v6 offset:17408\n\t"
  "v_mfma_f32_16x16x32_bf16 a[40:43], a[176:179], v[136:139], a[40:43]\n\t"
  "v_exp_f32_e64 v69, v69\n\t"
  "v_mfma_f32_16x16x32_bf16 a[40:43], a[180:183], v[140:143], a[40:43]\n\t"
  "ds_read_b64_tr_b16 a[162:163], v6 offset:21504\n\t"
  "v_cvt_pk_bf16_f32 v108, v68, v69\n\t"
  "v_mfma_f32_16x16x32_bf16 a[72:75], a[176:179], v[144:147], a[72:75]\n\t"
  "v_exp_f32_e64 v70, v70\n\t"
  "v_mfma_f32_16x16x32_bf16 a[72:75], a[180:183], v[148:151], a[72:75]\n\t"
  "ds_read_b64_tr_b16 a[164:165], v6 offset:25600\n\t"
  "v_mfma_f32_16x16x32_bf16 a[104:107], a[176:179], v[152:155], a[104:107]\n\t"
  "s_waitcnt lgkmcnt(2)\n\t"
  "v_exp_f32_e32 v71, v71\n\t"
  "v_mfma_f32_16x16x32_bf16 a[104:107], a[180:183], v[156:159], a[104:107]\n\t"
  "ds_read_b64_tr_b16 a[166:167], v6 offset:29696\n\t"
  "v_cvt_pk_bf16_f32 v109, v70, v71\n\t"
  "v_mfma_f32_16x16x32_bf16 a[12:15], a[184:187], v[128:131], a[12:15]\n\t"
  "v_exp_f32_e64 v72, v72\n\t"
  "v_mfma_f32_16x16x32_bf16 a[12:15], a[188:191], v[132:135], a[12:15]\n\t"
  "ds_read_b64_tr_b16 a[168:169], v7 offset:17408\n\t"
  "v_mfma_f32_16x16x32_bf16 a[44:47], a[184:187], v[136:139], a[44:47]\n\t"
  "v_exp_f32_e64 v73, v73\n\t"
  "v_mfma_f32_16x16x32_bf16 a[44:47], a[188:191], v[140:143], a[44:47]\n\t"
  "ds_read_b64_tr_b16 a[170:171], v7 offset:21504\n\t"
  "v_cvt_pk_bf16_f32 v116, v72, v73\n\t"
  "v_mfma_f32_16x16x32_bf16 a[76:79], a[184:187], v[144:147], a[76:79]\n\t"
  "v_exp_f32_e64 v74, v74\n\t"
  "v_mfma_f32_16x16x32_bf16 a[76:79], a[188:191], v[148:151], a[76:79]\n\t"
  "ds_read_b64_tr_b16 a[172:173], v7 offset:25600\n\t"
  "v_mfma_f32_16x16x32_bf16 a[108:111], a[184:187], v[152:155], a[108:111]\n\t"
  "s_waitcnt lgkmcnt(2)\n\t"
  "v_exp_f32_e32 v75, v75\n\t"
  "v_mfma_f32_16x16x32_bf16 a[108:111], a[188:191], v[156:159], a[108:111]\n\t"
  "ds_read_b64_tr_b16 a[174:175], v7 offset:29696\n\t"
  "v_cvt_pk_bf16_f32 v117, v74, v75\n\t"
  "v_mfma_f32_16x16x32_bf16 a[16:19], a[160:163], v[128:131], a[16:19]\n\t"
  "v_exp_f32_e64 v76, v76\n\t"
  "v_mfma_f32_16x16x32_bf16 a[16:19], a[164:167], v[132:135], a[16:19]\n\t"
  "ds_read_b64_tr_b16 a[176:177], v6 offset:17920\n\t"
  "v_mfma_f32_16x16x32_bf16 a[48:51], a[160:163], v[136:139], a[48:51]\n\t"
  "v_exp_f32_e64 v77, v77\n\t"
  "v_mfma_f32_16x16x32_bf16 a[48:51], a[164:167], v[140:143], a[48:51]\n\t"
  "ds_read_b64_tr_b16 a[178:179], v6 offset:22016\n\t"
  "v_cvt_pk_bf16_f32 v124, v76, v77\n\t"
  "v_mfma_f32_16x16x32_bf16 a[80:83], a[160:163], v[144:147], a[80:83]\n\t"
  "v_exp_f32_e64 v78, v78\n\t"
  "v_mfma_f32_16x16x32_bf16 a[80:83], a[164:167], v[148:151], a[80:83]\n\t"
  "ds_read_b64_tr_b16 a[180:181], v6 offset:26112\n\t"
  "v_mfma_f32_16x16x32_bf16 a[112:115], a[160:163], v[152:155], a[112:115]\n\t"
  "s_waitcnt lgkmcnt(2)\n\t"
  "v_exp_f32_e32 v79, v79\n\t"
  "v_mfma_f32_16x16x32_bf16 a[112:115], a[164:167], v[156:159], a[112:115]\n\t"
  "ds_read_b64_tr_b16 a[182:183], v6 offset:30208\n\t"
  "v_cvt_pk_bf16_f32 v125, v78, v79\n\t"
  "v_mfma_f32_16x16x32_bf16 a[20:23], a[168:171], v[128:131], a[20:23]\n\t"
  "v_exp_f32_e64 v80, v80\n\t"
  "v_mfma_f32_16x16x32_bf16 a[20:23], a[172:175], v[132:135], a[20:23]\n\t"
  "ds_read_b64_tr_b16 a[184:185], v7 offset:17920\n\t"
  "v_mfma_f32_16x16x32_bf16 a[52:55], a[168:171], v[136:139], a[52:55]\n\t"
  "v_exp_f32_e64 v81, v81\n\t"
  "v_mfma_f32_16x16x32_bf16 a[52:55], a[172:175], v[140:143], a[52:55]\n\t"
  "ds_read_b64_tr_b16 a[186:187], v7 offset:22016\n\t"
  "v_cvt_pk_bf16_f32 v102, v80, v81\n\t"
  "v_mfma_f32_16x16x32_bf16 a[84:87], a[168:171], v[144:147], a[84:87]\n\t"
  "v_exp_f32_e64 v82, v82\n\t"
  "v_mfma_f32_16x16x32_bf16 a[84:87], a[172:175], v[148:151], a[84:87]\n\t"
  "ds_read_b64_tr_b16 a[188:189], v7 offset:26112\n\t"
  "v_mfma_f32_16x16x32_bf16 a[116:119], a[168:171], v[152:155], a[116:119]\n\t"
  "s_waitcnt lgkmcnt(2)\n\t"
  "v_exp_f32_e32 v83, v83\n\t"
  "v_mfma_f32_16x16x32_bf16 a[116:119], a[172:175], v[156:159], a[116:119]\n\t"
  "ds_read_b64_tr_b16 a[190:191], v7 offset:30208\n\t"
  "v_cvt_pk_bf16_f32 v103, v82, v83\n\t"
  "v_mfma_f32_16x16x32_bf16 a[24:27], a[176:179], v[128:131], a[24:27]\n\t"
  "v_exp_f32_e64 v84, v84\n\t"
  "v_mfma_f32_16x16x32_bf16 a[24:27], a[180:183], v[132:135], a[24:27]\n\t"
  "ds_read_b128 a[128:131], v2 offset:49152\n\t"
  "v_mfma_f32_16x16x32_bf16 a[56:59], a[176:179], v[136:139], a[56:59]\n\t"
  "v_exp_f32_e64 v85, v85\n\t"
  "v_mfma_f32_16x16x32_bf16 a[56:59], a[180:183], v[140:143], a[56:59]\n\t"
  "ds_read_b128 a[132:135], v3 offset:49152\n\t"
  "v_cvt_pk_bf16_f32 v110, v84, v85\n\t"
  "v_mfma_f32_16x16x32_bf16 a[88:91], a[176:179], v[144:147], a[88:91]\n\t"
  "v_exp_f32_e64 v86, v86\n\t"
  "v_mfma_f32_16x16x32_bf16 a[88:91], a[180:183], v[148:151], a[88:91]\n\t"
  "ds_read_b128 a[136:139], v4 offset:49152\n\t"
  "v_mfma_f32_16x16x32_bf16 a[120:123], a[176:179], v[152:155], a[120:123]\n\t"
  "s_waitcnt lgkmcnt(2)\n\t"
  "v_exp_f32_e32 v87, v87\n\t"
  "v_mfma_f32_16x16x32_bf16 a[120:123], a[180:183], v[156:159], a[120:123]\n\t"
  "ds_read_b128 a[140:143], v5 offset:49152\n\t"
  "v_cvt_pk_bf16_f32 v111, v86, v87\n\t"
  "v_mfma_f32_16x16x32_bf16 a[28:31], a[184:187], v[128:131], a[28:31]\n\t"
  "v_exp_f32_e64 v88, v88\n\t"
  "v_mfma_f32_16x16x32_bf16 a[28:31], a[188:191], v[132:135], a[28:31]\n\t"
  "buffer_load_dwordx4 v12, s[12:15], s20 offen offset:2048 lds\n\t"
  "v_mfma_f32_16x16x32_bf16 a[60:63], a[184:187], v[136:139], a[60:63]\n\t"
  "v_exp_f32_e64 v89, v89\n\t"
  "v_mfma_f32_16x16x32_bf16 a[60:63], a[188:191], v[140:143], a[60:63]\n\t"
  "v_cvt_pk_bf16_f32 v118, v88, v89\n\t"
  "v_mfma_f32_16x16x32_bf16 a[92:95], a[184:187], v[144:147], a[92:95]\n\t"
  "v_exp_f32_e64 v90, v90\n\t"
  "v_mfma_f32_16x16x32_bf16 a[92:95], a[188:191], v[148:151], a[92:95]\n\t"
  "buffer_load_dwordx4 v13, s[12:15], s20 offen offset:3072 lds\n\t"
  "v_mfma_f32_16x16x32_bf16 a[124:127], a[184:187], v[152:155], a[124:127]\n\t"
  "s_add_u32 s20, s20, 0x4000\n\t"
  "v_exp_f32_e64 v91, v91\n\t"
  "v_mfma_f32_16x16x32_bf16 a[124:127], a[188:191], v[156:159], a[124:127]\n\t"
  "v_cvt_pk_bf16_f32 v119, v90, v91\n\t"
  "v_mfma_f32_16x16x32_bf16 a[192:195], a[208:211], v[128:131], a[192:195]\n\t"
  "v_exp_f32_e64 v92, v92\n\t"
  "v_mfma_f32_16x16x32_bf16 a[192:195], a[208:211], v[132:135], a[192:195]\n\t"
  "v_mfma_f32_16x16x32_bf16 a[196:199], a[208:211], v[136:139], a[196:199]\n\t"
  "v_exp_f32_e64 v93, v93\n\t"
  "v_mfma_f32_16x16x32_bf16 a[196:199], a[208:211], v[140:143], a[196:199]\n\t"
  "v_cvt_pk_bf16_f32 v126, v92, v93\n\t"
  "v_mfma_f32_16x16x32_bf16 a[200:203], a[208:211], v[144:147], a[200:203]\n\t"
  "v_exp_f32_e64 v94, v94\n\t"
  "v_mfma_f32_16x16x32_bf16 a[200:203], a[208:211], v[148:151], a[200:203]\n\t"
  "v_mfma_f32_16x16x32_bf16 a[204:207], a[208:211], v[152:155], a[204:207]\n\t"
  "v_exp_f32_e64 v95, v95\n\t"
  "v_mfma_f32_16x16x32_bf16 a[204:207], a[208:211], v[156:159], a[204:207]\n\t"
  "v_cvt_pk_bf16_f32 v127, v94, v95\n\t"
  "s_waitcnt vmcnt(4)\n\t"
  "s_barrier\n\t"
  "v_mfma_f32_16x16x32_bf16 v[32:35], a[128:131], v[160:163], v[224:227]\n\t"
  "s_waitcnt lgkmcnt(0)\n\t"
  "s_nop 0\n\t"
  "v_mfma_f32_16x16x32_bf16 v[32:35], a[132:135], v[164:167], v[32:35]\n\t"
  "ds_read_b128 a[144:147], v2 offset:53248\n\t"
  "v_mfma_f32_16x16x32_bf16 v[32:35], a[136:139], v[168:171], v[32:35]\n\t"
  "v_mfma_f32_16x16x32_bf16 v[32:35], a[140:143], v[172:175], v[32:35]\n\t"
  "ds_read_b128 a[148:151], v3 offset:53248\n\t"
  "v_mfma_f32_16x16x32_bf16 v[36:39], a[128:131], v[176:179], v[228:231]\n\t"
  "v_mfma_f32_16x16x32_bf16 v[36:39], a[132:135], v[180:183], v[36:39]\n\t"
  "ds_read_b128 a[152:155], v4 offset:53248\n\t"
  "v_mfma_f32_16x16x32_bf16 v[36:39], a[136:139], v[184:187], v[36:39]\n\t"
  "v_mfma_f32_16x16x32_bf16 v[36:39], a[140:143], v[188:191], v[36:39]\n\t"
  "ds_read_b128 a[156:159], v5 offset:53248\n\t"
  "v_mfma_f32_16x16x32_bf16 v[40:43], a[128:131], v[192:195], v[232:235]\n\t"
  "v_exp_f32_e64 v32, v32\n\t"
  "v_mfma_f32_16x16x32_bf16 v[40:43], a[132:135], v[196:199], v[40:43]\n\t"
  "v_mfma_f32_16x16x32_bf16 v[40:43], a[136:139], v[200:203], v[40:43]\n\t"
  "v_exp_f32_e64 v33, v33\n\t"
  "v_mfma_f32_16x16x32_bf16 v[40:43], a[140:143], v[204:207], v[40:43]\n\t"
  "v_cvt_pk_bf16_f32 v128, v32, v33\n\t"
  "v_mfma_f32_16x16x32_bf16 v[44:47], a[128:131], v[208:211], v[236:239]\n\t"
  "v_exp_f32_e64 v34, v34\n\t"
  "v_mfma_f32_16x16x32_bf16 v[44:47], a[132:135], v[212:215], v[44:47]\n\t"
  "v_mfma_f32_16x16x32_bf16 v[44:47], a[136:139], v[216:219], v[44:47]\n\t"
  "s_waitcnt lgkmcnt(0)\n\t"
  "v_exp_f32_e32 v35, v35\n\t"
  "v_mfma_f32_16x16x32_bf16 v[44:47], a[140:143], v[220:223], v[44:47]\n\t"
  "v_cvt_pk_bf16_f32 v129, v34, v35\n\t"
  "v_mfma_f32_16x16x32_bf16 v[48:51], a[144:147], v[160:163], v[224:227]\n\t"
  "v_exp_f32_e64 v36, v36\n\t"
  "v_mfma_f32_16x16x32_bf16 v[48:51], a[148:151], v[164:167], v[48:51]\n\t"
  "ds_read_b128 a[128:131], v2 offset:57344\n\t"
  "global_load_dwordx4 v[240:243], v1, s[28:29] offset:0\n\t"
  "v_mfma_f32_16x16x32_bf16 v[48:51], a[152:155], v[168:171], v[48:51]\n\t"
  "v_exp_f32_e64 v37, v37\n\t"
  "v_mfma_f32_16x16x32_bf16 v[48:51], a[156:159], v[172:175], v[48:51]\n\t"
  "ds_read_b128 a[132:135], v3 offset:57344\n\t"
  "v_cvt_pk_bf16_f32 v136, v36, v37\n\t"
  "v_mfma_f32_16x16x32_bf16 v[52:55], a[144:147], v[176:179], v[228:231]\n\t"
  "v_exp_f32_e64 v38, v38\n\t"
  "v_mfma_f32_16x16x32_bf16 v[52:55], a[148:151], v[180:183], v[52:55]\n\t"
  "ds_read_b128 a[136:139], v4 offset:57344\n\t"
  "global_load_dwordx4 v[244:247], v1, s[28:29] offset:1024\n\t"
  "v_mfma_f32_16x16x32_bf16 v[52:55], a[152:155], v[184:187], v[52:55]\n\t"
  "v_exp_f32_e64 v39, v39\n\t"
  "v_mfma_f32_16x16x32_bf16 v[52:55], a[156:159], v[188:191], v[52:55]\n\t"
  "ds_read_b128 a[140:143], v5 offset:57344\n\t"
  "v_cvt_pk_bf16_f32 v137, v38, v39\n\t"
  "v_mfma_f32_16x16x32_bf16 v[56:59], a[144:147], v[192:195], v[232:235]\n\t"
  "v_exp_f32_e64 v40, v40\n\t"
  "v_mfma_f32_16x16x32_bf16 v[56:59], a[148:151], v[196:199], v[56:59]\n\t"
  "global_load_dwordx4 v[248:251], v1, s[28:29] offset:2048\n\t"
  "v_mfma_f32_16x16x32_bf16 v[56:59], a[152:155], v[200:203], v[56:59]\n\t"
  "v_exp_f32_e64 v41, v41\n\t"
  "v_mfma_f32_16x16x32_bf16 v[56:59], a[156:159], v[204:207], v[56:59]\n\t"
  "v_cvt_pk_bf16_f32 v144, v40, v41\n\t"
  "v_mfma_f32_16x16x32_bf16 v[60:63], a[144:147], v[208:211], v[236:239]\n\t"
  "v_exp_f32_e64 v42, v42\n\t"
  "v_mfma_f32_16x16x32_bf16 v[60:63], a[148:151], v[212:215], v[60:63]\n\t"
  "global_load_dwordx4 v[252:255], v1, s[28:29] offset:3072\n\t"
  "v_mfma_f32_16x16x32_bf16 v[60:63], a[152:155], v[216:219], v[60:63]\n\t"
  "s_waitcnt lgkmcnt(0)\n\t"
  "v_exp_f32_e32 v43, v43\n\t"
  "v_mfma_f32_16x16x32_bf16 v[60:63], a[156:159], v[220:223], v[60:63]\n\t"
  "v_cvt_pk_bf16_f32 v145, v42, v43\n\t"
  "v_mfma_f32_16x16x32_bf16 v[64:67], a[128:131], v[160:163], v[224:227]\n\t"
  "v_exp_f32_e64 v44, v44\n\t"
  "v_mfma_f32_16x16x32_bf16 v[64:67], a[132:135], v[164:167], v[64:67]\n\t"
  "ds_read_b128 a[144:147], v2 offset:61440\n\t"
  "global_load_dword v20, v21, s[50:51] sc1\n\t"
  "v_mfma_f32_16x16x32_bf16 v[64:67], a[136:139], v[168:171], v[64:67]\n\t"
  "v_exp_f32_e64 v45, v45\n\t"
  "v_mfma_f32_16x16x32_bf16 v[64:67], a[140:143], v[172:175], v[64:67]\n\t"
  "ds_read_b128 a[148:151], v3 offset:61440\n\t"
  "v_cvt_pk_bf16_f32 v152, v44, v45\n\t"
  "v_mfma_f32_16x16x32_bf16 v[68:71], a[128:131], v[176:179], v[228:231]\n\t"
  "v_exp_f32_e64 v46, v46\n\t"
  "v_mfma_f32_16x16x32_bf16 v[68:71], a[132:135], v[180:183], v[68:71]\n\t"
  "ds_read_b128 a[152:155], v4 offset:61440\n\t"
  "v_mfma_f32_16x16x32_bf16 v[68:71], a[136:139], v[184:187], v[68:71]\n\t"
  "v_exp_f32_e64 v47, v47\n\t"
  "v_mfma_f32_16x16x32_bf16 v[68:71], a[140:143], v[188:191], v[68:71]\n\t"
  "ds_read_b128 a[156:159], v5 offset:61440\n\t"
  "v_cvt_pk_bf16_f32 v153, v46, v47\n\t"
  "v_mfma_f32_16x16x32_bf16 v[72:75], a[128:131], v[192:195], v[232:235]\n\t"
  "v_exp_f32_e64 v48, v48\n\t"
  "v_mfma_f32_16x16x32_bf16 v[72:75], a[132:135], v[196:199], v[72:75]\n\t"
  "v_mfma_f32_16x16x32_bf16 v[72:75], a[136:139], v[200:203], v[72:75]\n\t"
  "v_exp_f32_e64 v49, v49\n\t"
  "v_mfma_f32_16x16x32_bf16 v[72:75], a[140:143], v[204:207], v[72:75]\n\t"
  "v_cvt_pk_bf16_f32 v130, v48, v49\n\t"
  "v_mfma_f32_16x16x32_bf16 v[76:79], a[128:131], v[208:211], v[236:239]\n\t"
  "v_exp_f32_e64 v50, v50\n\t"
  "v_mfma_f32_16x16x32_bf16 v[76:79], a[132:135], v[212:215], v[76:79]\n\t"
  "v_mfma_f32_16x16x32_bf16 v[76:79], a[136:139], v[216:219], v[76:79]\n\t"
  "s_waitcnt lgkmcnt(0)\n\t"
  "v_exp_f32_e32 v51, v51\n\t"
  "v_mfma_f32_16x16x32_bf16 v[76:79], a[140:143], v[220:223], v[76:79]\n\t"
  "v_cvt_pk_bf16_f32 v131, v50, v51\n\t"
  "v_mfma_f32_16x16x32_bf16 v[80:83], a[144:147], v[160:163], v[224:227]\n\t"
  "v_exp_f32_e64 v52, v52\n\t"
  "v_mfma_f32_16x16x32_bf16 v[80:83], a[148:151], v[164:167], v[80:83]\n\t"
  "ds_read_b64_tr_b16 a[160:161], v6 offset:32768\n\t"
  "v_mfma_f32_16x16x32_bf16 v[80:83], a[152:155], v[168:171], v[80:83]\n\t"
  "v_exp_f32_e64 v53, v53\n\t"
  "v_mfma_f32_16x16x32_bf16 v[80:83], a[156:159], v[172:175], v[80:83]\n\t"
  "ds_read_b64_tr_b16 a[162:163], v6 offset:36864\n\t"
  "v_cvt_pk_bf16_f32 v138, v52, v53\n\t"
  "v_mfma_f32_16x16x32_bf16 v[84:87], a[144:147], v[176:179], v[228:231]\n\t"
  "v_exp_f32_e64 v54, v54\n\t"
  "v_mfma_f32_16x16x32_bf16 v[84:87], a[148:151], v[180:183], v[84:87]\n\t"
  "ds_read_b64_tr_b16 a[164:165], v6 offset:40960\n\t"
  "v_mfma_f32_16x16x32_bf16 v[84:87], a[152:155], v[184:187], v[84:87]\n\t"
  "v_exp_f32_e64 v55, v55\n\t"
  "v_mfma_f32_16x16x32_bf16 v[84:87], a[156:159], v[188:191], v[84:87]\n\t"
  "ds_read_b64_tr_b16 a[166:167], v6 offset:45056\n\t"
  "v_cvt_pk_bf16_f32 v139, v54, v55\n\t"
  "v_mfma_f32_16x16x32_bf16 v[88:91], a[144:147], v[192:195], v[232:235]\n\t"
  "v_exp_f32_e64 v56, v56\n\t"
  "v_mfma_f32_16x16x32_bf16 v[88:91], a[148:151], v[196:199], v[88:91]\n\t"
  "ds_read_b64_tr_b16 a[168:169], v7 offset:32768\n\t"
  "v_mfma_f32_16x16x32_bf16 v[88:91], a[152:155], v[200:203], v[88:91]\n\t"
  "v_exp_f32_e64 v57, v57\n\t"
  "v_mfma_f32_16x16x32_bf16 v[88:91], a[156:159], v[204:207], v[88:91]\n\t"
  "ds_read_b64_tr_b16 a[170:171], v7 offset:36864\n\t"
  "v_cvt_pk_bf16_f32 v146, v56, v57\n\t"
  "v_mfma_f32_16x16x32_bf16 v[92:95], a[144:147], v[208:211], v[236:239]\n\t"
  "v_exp_f32_e64 v58, v58\n\t"
  "v_mfma_f32_16x16x32_bf16 v[92:95], a[148:151], v[212:215], v[92:95]\n\t"
  "ds_read_b64_tr_b16 a[172:173], v7 offset:40960\n\t"
  "v_mfma_f32_16x16x32_bf16 v[92:95], a[152:155], v[216:219], v[92:95]\n\t"
  "s_waitcnt lgkmcnt(2)\n\t"
  "v_exp_f32_e32 v59, v59\n\t"
  "v_mfma_f32_16x16x32_bf16 v[92:95], a[156:159], v[220:223], v[92:95]\n\t"
  "ds_read_b64_tr_b16 a[174:175], v7 offset:45056\n\t"
  "v_cvt_pk_bf16_f32 v147, v58, v59\n\t"
  "v_mfma_f32_16x16x32_bf16 a[0:3], a[160:163], v[96:99], a[0:3]\n\t"
  "v_exp_f32_e64 v60, v60\n\t"
  "v_mfma_f32_16x16x32_bf16 a[0:3], a[164:167], v[100:103], a[0:3]\n\t"
  "ds_read_b64_tr_b16 a[176:177], v6 offset:33280\n\t"
  "v_mfma_f32_16x16x32_bf16 a[32:35], a[160:163], v[104:107], a[32:35]\n\t"
  "v_exp_f32_e64 v61, v61\n\t"
  "v_mfma_f32_16x16x32_bf16 a[32:35], a[164:167], v[108:111], a[32:35]\n\t"
  "ds_read_b64_tr_b16 a[178:179], v6 offset:37376\n\t"
  "v_cvt_pk_bf16_f32 v154, v60, v61\n\t"
  "v_mfma_f32_16x16x32_bf16 a[64:67], a[160:163], v[112:115], a[64:67]\n\t"
  "v_exp_f32_e64 v62, v62\n\t"
  "v_mfma_f32_16x16x32_bf16 a[64:67], a[164:167], v[116:119], a[64:67]\n\t"
  "ds_read_b64_tr_b16 a[180:181], v6 offset:41472\n\t"
  "v_mfma_f32_16x16x32_bf16 a[96:99], a[160:163], v[120:123], a[96:99]\n\t"
  "s_waitcnt lgkmcnt(2)\n\t"
  "v_exp_f32_e32 v63, v63\n\t"
  "v_mfma_f32_16x16x32_bf16 a[96:99], a[164:167], v[124:127], a[96:99]\n\t"
  "ds_read_b64_tr_b16 a[182:183], v6 offset:45568\n\t"
  "v_cvt_pk_bf16_f32 v155, v62, v63\n\t"
  "v_mfma_f32_16x16x32_bf16 a[4:7], a[168:171], v[96:99], a[4:7]\n\t"
  "v_exp_f32_e64 v64, v64\n\t"
  "v_mfma_f32_16x16x32_bf16 a[4:7], a[172:175], v[100:103], a[4:7]\n\t"
  "ds_read_b64_tr_b16 a[184:185], v7 offset:33280\n\t"
  "v_mfma_f32_16x16x32_bf16 a[36:39], a[168:171], v[104:107], a[36:39]\n\t"
  "v_exp_f32_e64 v65, v65\n\t"
  "v_mfma_f32_16x16x32_bf16 a[36:39], a[172:175], v[108:111], a[36:39]\n\t"
  "ds_read_b64_tr_b16 a[186:187], v7 offset:37376\n\t"
  "v_cvt_pk_bf16_f32 v132, v64, v65\n\t"
  "v_mfma_f32_16x16x32_bf16 a[68:71], a[168:171], v[112:115], a[68:71]\n\t"
  "v_exp_f32_e64 v66, v66\n\t"
  "v_mfma_f32_16x16x32_bf16 a[68:71], a[172:175], v[116:119], a[68:71]\n\t"
  "ds_read_b64_tr_b16 a[188:189], v7 offset:41472\n\t"
  "v_mfma_f32_16x16x32_bf16 a[100:103], a[168:171], v[120:123], a[100:103]\n\t"
  "s_waitcnt lgkmcnt(2)\n\t"
  "v_exp_f32_e32 v67, v67\n\t"
  "v_mfma_f32_16x16x32_bf16 a[100:103], a[172:175], v[124:127], a[100:103]\n\t"
  "ds_read_b64_tr_b16 a[190:191], v7 offset:45568\n\t"
  "v_cvt_pk_bf16_f32 v133, v66, v67\n\t"
  "v_mfma_f32_16x16x32_bf16 a[8:11], a[176:179], v[96:99], a[8:11]\n\t"
  "v_exp_f32_e64 v68, v68\n\t"
  "v_mfma_f32_16x16x32_bf16 a[8:11], a[180:183], v[100:103], a[8:11]\n\t"
  "ds_read_b64_tr_b16 a[160:161], v6 offset:33792\n\t"
  "v_mfma_f32_16x16x32_bf16 a[40:43], a[176:179], v[104:107], a[40:43]\n\t"
  "v_exp_f32_e64 v69, v69\n\t"
  "v_mfma_f32_16x16x32_bf16 a[40:43], a[180:183], v[108:111], a[40:43]\n\t"
  "ds_read_b64_tr_b16 a[162:163], v6 offset:37888\n\t"
  "v_cvt_pk_bf16_f32 v140, v68, v69\n\t"
  "v_mfma_f32_16x16x32_bf16 a[72:75], a[176:179], v[112:115], a[72:75]\n\t"
  "v_exp_f32_e64 v70, v70\n\t"
  "v_mfma_f32_16x16x32_bf16 a[72:75], a[180:183], v[116:119], a[72:75]\n\t"
  "ds_read_b64_tr_b16 a[164:165], v6 offset:41984\n\t"
  "v_mfma_f32_16x16x32_bf16 a[104:107], a[176:179], v[120:123], a[104:107]\n\t"
  "s_waitcnt lgkmcnt(2)\n\t"
  "v_exp_f32_e32 v71, v71\n\t"
  "v_mfma_f32_16x16x32_bf16 a[104:107], a[180:183], v[124:127], a[104:107]\n\t"
  "ds_read_b64_tr_b16 a[166:167], v6 offset:46080\n\t"
  "v_cvt_pk_bf16_f32 v141, v70, v71\n\t"
  "v_mfma_f32_16x16x32_bf16 a[12:15], a[184:187], v[96:99], a[12:15]\n\t"
  "v_exp_f32_e64 v72, v72\n\t"
  "v_mfma_f32_16x16x32_bf16 a[12:15], a[188:191], v[100:103], a[12:15]\n\t"
  "ds_read_b64_tr_b16 a[168:169], v7 offset:33792\n\t"
  "v_mfma_f32_16x16x32_bf16 a[44:47], a[184:187], v[104:107], a[44:47]\n\t"
  "v_exp_f32_e64 v73, v73\n\t"
  "v_mfma_f32_16x16x32_bf16 a[44:47], a[188:191], v[108:111], a[44:47]\n\t"
  "ds_read_b64_tr_b16 a[170:171], v7 offset:37888\n\t"
  "v_cvt_pk_bf16_f32 v148, v72, v73\n\t"
  "v_mfma_f32_16x16x32_bf16 a[76:79], a[184:187], v[112:115], a[76:79]\n\t"
  "v_exp_f32_e64 v74, v74\n\t"
  "v_mfma_f32_16x16x32_bf16 a[76:79], a[188:191], v[116:119], a[76:79]\n\t"
  "ds_read_b64_tr_b16 a[172:173], v7 offset:41984\n\t"
  "v_mfma_f32_16x16x32_bf16 a[108:111], a[184:187], v[120:123], a[108:111]\n\t"
  "s_waitcnt lgkmcnt(2)\n\t"
  "v_exp_f32_e32 v75, v75\n\t"
  "v_mfma_f32_16x16x32_bf16 a[108:111], a[188:191], v[124:127], a[108:111]\n\t"
  "ds_read_b64_tr_b16 a[174:175], v7 offset:46080\n\t"
  "v_cvt_pk_bf16_f32 v149, v74, v75\n\t"
  "v_mfma_f32_16x16x32_bf16 a[16:19], a[160:163], v[96:99], a[16:19]\n\t"
  "v_exp_f32_e64 v76, v76\n\t"
  "v_mfma_f32_16x16x32_bf16 a[16:19], a[164:167], v[100:103], a[16:19]\n\t"
  "ds_read_b64_tr_b16 a[176:177], v6 offset:34304\n\t"
  "v_mfma_f32_16x16x32_bf16 a[48:51], a[160:163], v[104:107], a[48:51]\n\t"
  "v_exp_f32_e64 v77, v77\n\t"
  "v_mfma_f32_16x16x32_bf16 a[48:51], a[164:167], v[108:111], a[48:51]\n\t"
  "ds_read_b64_tr_b16 a[178:179], v6 offset:38400\n\t"
  "v_cvt_pk_bf16_f32 v156, v76, v77\n\t"
  "v_mfma_f32_16x16x32_bf16 a[80:83], a[160:163], v[112:115], a[80:83]\n\t"
  "v_exp_f32_e64 v78, v78\n\t"
  "v_mfma_f32_16x16x32_bf16 a[80:83], a[164:167], v[116:119], a[80:83]\n\t"
  "ds_read_b64_tr_b16 a[180:181], v6 offset:42496\n\t"
  "v_mfma_f32_16x16x32_bf16 a[112:115], a[160:163], v[120:123], a[112:115]\n\t"
  "s_waitcnt lgkmcnt(2)\n\t"
  "v_exp_f32_e32 v79, v79\n\t"
  "v_mfma_f32_16x16x32_bf16 a[112:115], a[164:167], v[124:127], a[112:115]\n\t"
  "ds_read_b64_tr_b16 a[182:183], v6 offset:46592\n\t"
  "v_cvt_pk_bf16_f32 v157, v78, v79\n\t"
  "v_mfma_f32_16x16x32_bf16 a[20:23], a[168:171], v[96:99], a[20:23]\n\t"
  "v_exp_f32_e64 v80, v80\n\t"
  "v_mfma_f32_16x16x32_bf16 a[20:23], a[172:175], v[100:103], a[20:23]\n\t"
  "ds_read_b64_tr_b16 a[184:185], v7 offset:34304\n\t"
  "v_mfma_f32_16x16x32_bf16 a[52:55], a[168:171], v[104:107], a[52:55]\n\t"
  "v_exp_f32_e64 v81, v81\n\t"
  "v_mfma_f32_16x16x32_bf16 a[52:55], a[172:175], v[108:111], a[52:55]\n\t"
  "ds_read_b64_tr_b16 a[186:187], v7 offset:38400\n\t"
  "v_cvt_pk_bf16_f32 v134, v80, v81\n\t"
  "v_mfma_f32_16x16x32_bf16 a[84:87], a[168:171], v[112:115], a[84:87]\n\t"
  "v_exp_f32_e64 v82, v82\n\t"
  "v_mfma_f32_16x16x32_bf16 a[84:87], a[172:175], v[116:119], a[84:87]\n\t"
  "ds_read_b64_tr_b16 a[188:189], v7 offset:42496\n\t"
  "v_mfma_f32_16x16x32_bf16 a[116:119], a[168:171], v[120:123], a[116:119]\n\t"
  "s_waitcnt lgkmcnt(2)\n\t"
  "v_exp_f32_e32 v83, v83\n\t"
  "v_mfma_f32_16x16x32_bf16 a[116:119], a[172:175], v[124:127], a[116:119]\n\t"
  "ds_read_b64_tr_b16 a[190:191], v7 offset:46592\n\t"
  "v_cvt_pk_bf16_f32 v135, v82, v83\n\t"
  "v_mfma_f32_16x16x32_bf16 a[24:27], a[176:179], v[96:99], a[24:27]\n\t"
  "v_exp_f32_e64 v84, v84\n\t"
  "v_mfma_f32_16x16x32_bf16 a[24:27], a[180:183], v[100:103], a[24:27]\n\t"
  "v_mfma_f32_16x16x32_bf16 a[56:59], a[176:179], v[104:107], a[56:59]\n\t"
  "v_exp_f32_e64 v85, v85\n\t"
  "v_mfma_f32_16x16x32_bf16 a[56:59], a[180:183], v[108:111], a[56:59]\n\t"
  "v_cvt_pk_bf16_f32 v142, v84, v85\n\t"
  "v_mfma_f32_16x16x32_bf16 a[88:91], a[176:179], v[112:115], a[88:91]\n\t"
  "v_exp_f32_e64 v86, v86\n\t"
  "v_mfma_f32_16x16x32_bf16 a[88:91], a[180:183], v[116:119], a[88:91]\n\t"
  "v_mfma_f32_16x16x32_bf16 a[120:123], a[176:179], v[120:123], a[120:123]\n\t"
  "s_waitcnt lgkmcnt(0)\n\t"
  "v_exp_f32_e32 v87, v87\n\t"
  "v_mfma_f32_16x16x32_bf16 a[120:123], a[180:183], v[124:127], a[120:123]\n\t"
  "v_cvt_pk_bf16_f32 v143, v86, v87\n\t"
  "v_mfma_f32_16x16x32_bf16 a[28:31], a[184:187], v[96:99], a[28:31]\n\t"
  "v_exp_f32_e64 v88, v88\n\t"
  "v_mfma_f32_16x16x32_bf16 a[28:31], a[188:191], v[100:103], a[28:31]\n\t"
  "v_mfma_f32_16x16x32_bf16 a[60:63], a[184:187], v[104:107], a[60:63]\n\t"
  "v_exp_f32_e64 v89, v89\n\t"
  "v_mfma_f32_16x16x32_bf16 a[60:63], a[188:191], v[108:111], a[60:63]\n\t"
  "v_cvt_pk_bf16_f32 v150, v88, v89\n\t"
  "v_mfma_f32_16x16x32_bf16 a[92:95], a[184:187], v[112:115], a[92:95]\n\t"
  "v_exp_f32_e64 v90, v90\n\t"
  "v_mfma_f32_16x16x32_bf16 a[92:95], a[188:191], v[116:119], a[92:95]\n\t"
  "v_mfma_f32_16x16x32_bf16 a[124:127], a[184:187], v[120:123], a[124:127]\n\t"
  "v_exp_f32_e64 v91, v91\n\t"
  "v_mfma_f32_16x16x32_bf16 a[124:127], a[188:191], v[124:127], a[124:127]\n\t"
  "v_cvt_pk_bf16_f32 v151, v90, v91\n\t"
  "v_mfma_f32_16x16x32_bf16 a[192:195], a[208:211], v[96:99], a[192:195]\n\t"
  "v_exp_f32_e64 v92, v92\n\t"
  "v_mfma_f32_16x16x32_bf16 a[192:195], a[208:211], v[100:103], a[192:195]\n\t"
  "v_mfma_f32_16x16x32_bf16 a[196:199], a[208:211], v[104:107], a[196:199]\n\t"
  "v_exp_f32_e64 v93, v93\n\t"
  "v_mfma_f32_16x16x32_bf16 a[196:199], a[208:211], v[108:111], a[196:199]\n\t"
  "v_cvt_pk_bf16_f32 v158, v92, v93\n\t"
  "v_mfma_f32_16x16x32_bf16 a[200:203], a[208:211], v[112:115], a[200:203]\n\t"
  "v_exp_f32_e64 v94, v94\n\t"
  "v_mfma_f32_16x16x32_bf16 a[200:203], a[208:211], v[116:119], a[200:203]\n\t"
  "v_mfma_f32_16x16x32_bf16 a[204:207], a[208:211], v[120:123], a[204:207]\n\t"
  "v_exp_f32_e64 v95, v95\n\t"
  "v_mfma_f32_16x16x32_bf16 a[204:207], a[208:211], v[124:127], a[204:207]\n\t"
  "v_cvt_pk_bf16_f32 v159, v94, v95\n\t"
  "s_waitcnt vmcnt(0)\n\t"
  "s_barrier\n\t"
  "v_readfirstlane_b32 s40, v20\n\t"
  "s_nop 0\n\t"
  "s_cmp_ge_u32 s40, 7\n\t"
  "s_cbranch_scc1 L_okh_%=\n\t"
  "s_mov_b32 s41, 0x40000\n\t"
  "L_spinh_%=:\n\t"
  "s_sleep 1\n\t"
  "s_nop 0\n\t"
  "global_load_dword v20, v21, s[50:51] sc1\n\t"
  "s_waitcnt vmcnt(0)\n\t"
  "v_readfirstlane_b32 s40, v20\n\t"
  "s_nop 0\n\t"
  "s_cmp_ge_u32 s40, 7\n\t"
  "s_cbranch_scc1 L_okh_%=\n\t"
  "s_sub_u32 s41, s41, 1\n\t"
  "s_cmp_lg_u32 s41, 0\n\t"
  "s_cbranch_scc1 L_spinh_%=\n\t"
  "L_okh_%=:\n\t"
  "ds_read_b64_tr_b16 a[160:161], v6 offset:49152\n\t"
  "ds_read_b64_tr_b16 a[162:163], v6 offset:53248\n\t"
  "ds_read_b64_tr_b16 a[164:165], v6 offset:57344\n\t"
  "ds_read_b64_tr_b16 a[166:167], v6 offset:61440\n\t"
  "ds_read_b64_tr_b16 a[168:169], v7 offset:49152\n\t"
  "ds_read_b64_tr_b16 a[170:171], v7 offset:53248\n\t"
  "ds_read_b64_tr_b16 a[172:173], v7 offset:57344\n\t"
  "ds_read_b64_tr_b16 a[174:175], v7 offset:61440\n\t"
  "s_mul_i32 s40, %12, 1507328\n\t"
  "s_add_u32 s40, s40, 4194304\n\t"
  "s_add_u32 s40, s64, s40\n\t"
  "s_addc_u32 s41, s65, 0\n\t"
  "s_lshl_b32 s42, %12, 2\n\t"
  "s_add_u32 s42, s42, %13\n\t"
  "s_lshl_b32 s42, s42, 18\n\t"
  "s_add_u32 s42, s42, 33554432\n\t"
  "s_add_u32 s42, s64, s42\n\t"
  "s_addc_u32 s43, s65, 0\n\t"
  "s_lshl_b32 s44, %13, 8\n\t"
  "s_add_u32 s46, s74, s44\n\t"
  "s_addc_u32 s47, s75, 0\n\t"
  "s_add_u32 s48, s76, s44\n\t"
  "s_addc_u32 s49, s77, 0\n\t"
  "s_mov_b64 s[8:9], s[46:47]\n\t"
  "s_sub_u32 s8, s8, 0x400\n\t"
  "s_subb_u32 s9, s9, 0\n\t"
  "s_and_b32 s9, s9, 0xffff\n\t"
  "s_mov_b32 s10, 1836032\n\t"
  "s_mov_b32 s11, 0x20000\n\t"
  "s_mov_b64 s[12:13], s[48:49]\n\t"
  "s_sub_u32 s12, s12, 0x400\n\t"
  "s_subb_u32 s13, s13, 0\n\t"
  "s_waitcnt lgkmcnt(6)\n\t"
  "s_nop 0\n\t"
  "v_mfma_f32_16x16x32_bf16 a[0:3], a[160:163], v[128:131], a[0:3]\n\t"
  "s_waitcnt lgkmcnt(4)\n\t"
  "s_and_b32 s13, s13, 0xffff\n\t"
  "s_mov_b32 s14, 4195328\n\t"
  "s_mov_b32 s15, 0x20000\n\t"
  "s_nop 0\n\t"
  "v_mfma_f32_16x16x32_bf16 a[0:3], a[164:167], v[132:135], a[0:3]\n\t"
  "ds_read_b64_tr_b16 a[176:177], v6 offset:49664\n\t"
  "s_mov_b64 s[16:17], s[40:41]\n\t"
  "s_and_b32 s17, s17, 0xffff\n\t"
  "s_mov_b32 s18, 1507328\n\t"
  "s_nop 0\n\t"
  "v_mfma_f32_16x16x32_bf16 a[32:35], a[160:163], v[136:139], a[32:35]\n\t"
  "s_mov_b32 s19, 0x20000\n\t"
  "s_mov_b64 s[20:21], s[42:43]\n\t"
  "s_and_b32 s21, s21, 0xffff\n\t"
  "s_nop 0\n\t"
  "v_mfma_f32_16x16x32_bf16 a[32:35], a[164:167], v[140:143], a[32:35]\n\t"
  "ds_read_b64_tr_b16 a[178:179], v6 offset:53760\n\t"
  "s_mov_b32 s22, 0x40000\n\t"
  "s_mov_b32 s23, 0x20000\n\t"
  "v_mbcnt_lo_u32_b32 v0, -1, 0\n\t"
  "v_mfma_f32_16x16x32_bf16 a[64:67], a[160:163], v[144:147], a[64:67]\n\t"
  "v_mbcnt_hi_u32_b32 v0, -1, v0\n\t"
  "v_lshlrev_b32_e64 v1, 4, v0\n\t"
  "v_mfma_f32_16x16x32_bf16 a[64:67], a[164:167], v[148:151], a[64:67]\n\t"
  "ds_read_b64_tr_b16 a[180:181], v6 offset:57856\n\t"
  "v_bfe_u32 v10, v0, 2, 3\n\t"
  "v_mfma_f32_16x16x32_bf16 a[96:99], a[160:163], v[152:155], a[96:99]\n\t"
  "v_lshrrev_b32_e32 v11, 2, v10\n\t"
  "v_lshlrev_b32_e32 v11, 1, v11\n\t"
  "v_mfma_f32_16x16x32_bf16 a[96:99], a[164:167], v[156:159], a[96:99]\n\t"
  "s_waitcnt lgkmcnt(2)\n\t"
  "s_nop 0\n\t"
  "ds_read_b64_tr_b16 a[182:183], v6 offset:61952\n\t"
  "v_and_b32_e64 v12, 3, v0\n\t"
  "v_mfma_f32_16x16x32_bf16 a[4:7], a[168:171], v[128:131], a[4:7]\n\t"
  "v_xor_b32_e32 v11, v11, v12\n\t"
  "v_lshlrev_b32_e32 v11, 4, v11\n\t"
  "v_mfma_f32_16x16x32_bf16 a[4:7], a[172:175], v[132:135], a[4:7]\n\t"
  "ds_read_b64_tr_b16 a[184:185], v7 offset:49664\n\t"
  "v_lshrrev_b32_e64 v12, 5, v0\n\t"
  "v_mfma_f32_16x16x32_bf16 a[36:39], a[168:171], v[136:139], a[36:39]\n\t"
  "v_lshl_add_u32 v11, v12, 6, v11\n\t"
  "v_lshl_add_u32 v11, v10, 10, v11\n\t"
  "v_mfma_f32_16x16x32_bf16 a[36:39], a[172:175], v[140:143], a[36:39]\n\t"
  "ds_read_b64_tr_b16 a[186:187], v7 offset:53760\n\t"
  "v_add_u32_e32 v8, 0x400, v11\n\t"
  "v_mfma_f32_16x16x32_bf16 a[68:71], a[168:171], v[144:147], a[68:71]\n\t"
  "v_add_u32_e32 v9, 0x80, v11\n\t"
  "s_lshl_b32 s24, %7, 12\n\t"
  "s_add_u32 s25, s24, 0x4000\n\t"
  "s_nop 0\n\t"
  "v_mfma_f32_16x16x32_bf16 a[68:71], a[172:175], v[148:151], a[68:71]\n\t"
  "ds_read_b64_tr_b16 a[188:189], v7 offset:57856\n\t"
  "s_lshl_b32 s30, %7, 14\n\t"
  "s_mul_i32 s26, %11, 458752\n\t"
  "s_add_u32 s26, s26, s30\n\t"
  "v_mfma_f32_16x16x32_bf16 a[100:103], a[168:171], v[152:155], a[100:103]\n\t"
  "s_lshl_b32 s27, %11, 20\n\t"
  "s_add_u32 s27, s27, s30\n\t"
  "s_mul_i32 s30, %7, 376832\n\t"
  "v_mfma_f32_16x16x32_bf16 a[100:103], a[172:175], v[156:159], a[100:103]\n\t"
  "s_waitcnt lgkmcnt(2)\n\t"
  "s_nop 0\n\t"
  "ds_read_b64_tr_b16 a[190:191], v7 offset:61952\n\t"
  "s_mul_i32 s28, %11, 14336\n\t"
  "s_add_u32 s28, s28, s30\n\t"
  "s_lshl_b32 s29, %11, 15\n\t"
  "v_mfma_f32_16x16x32_bf16 a[8:11], a[176:179], v[128:131], a[8:11]\n\t"
  "s_add_u32 s29, s29, s30\n\t"
  "s_add_u32 s29, s29, 57344\n\t"
  "s_add_u32 m0, s24, 0\n\t"
  "v_mfma_f32_16x16x32_bf16 a[8:11], a[180:183], v[132:135], a[8:11]\n\t"
  "ds_read_b64_tr_b16 a[160:161], v6 offset:50176\n\t"
  "s_add_u32 s30, s26, 0\n\t"
  "s_nop 0\n\t"
  "buffer_load_dwordx4 v8, s[8:11], s30 offen offset:0 lds\n\t"
  "v_mfma_f32_16x16x32_bf16 a[40:43], a[176:179], v[136:139], a[40:43]\n\t"
  "buffer_load_dwordx4 v9, s[8:11], s30 offen offset:1024 lds\n\t"
  "v_mfma_f32_16x16x32_bf16 a[40:43], a[180:183], v[140:143], a[40:43]\n\t"
  "ds_read_b64_tr_b16 a[162:163], v6 offset:54272\n\t"
  "s_add_u32 m0, s24, 2048\n\t"
  "s_add_u32 s30, s26, 8192\n\t"
  "buffer_load_dwordx4 v8, s[8:11], s30 offen offset:0 lds\n\t"
  "v_mfma_f32_16x16x32_bf16 a[72:75], a[176:179], v[144:147], a[72:75]\n\t"
  "buffer_load_dwordx4 v9, s[8:11], s30 offen offset:1024 lds\n\t"
  "v_mfma_f32_16x16x32_bf16 a[72:75], a[180:183], v[148:151], a[72:75]\n\t"
  "ds_read_b64_tr_b16 a[164:165], v6 offset:58368\n\t"
  "s_add_u32 m0, s25, 0\n\t"
  "s_add_u32 s31, s28, 0\n\t"
  "buffer_load_dwordx4 v1, s[16:19], s31 offen offset:0 sc1 lds\n\t"
  "v_mfma_f32_16x16x32_bf16 a[104:107], a[176:179], v[152:155], a[104:107]\n\t"
  "buffer_load_dwordx4 v1, s[16:19], s31 offen offset:1024 sc1 lds\n\t"
  "v_mfma_f32_16x16x32_bf16 a[104:107], a[180:183], v[156:159], a[104:107]\n\t"
  "s_waitcnt lgkmcnt(2)\n\t"
  "s_nop 0\n\t"
  "ds_read_b64_tr_b16 a[166:167], v6 offset:62464\n\t"
  "s_add_u32 m0, s25, 2048\n\t"
  "s_add_u32 s31, s28, 188416\n\t"
  "buffer_load_dwordx4 v1, s[16:19], s31 offen offset:0 sc1 lds\n\t"
  "v_mfma_f32_16x16x32_bf16 a[12:15], a[184:187], v[128:131], a[12:15]\n\t"
  "buffer_load_dwordx4 v1, s[16:19], s31 offen offset:1024 sc1 lds\n\t"
  "v_mfma_f32_16x16x32_bf16 a[12:15], a[188:191], v[132:135], a[12:15]\n\t"
  "ds_read_b64_tr_b16 a[168:169], v7 offset:50176\n\t"
  "s_add_u32 m0, s24, 32768\n\t"
  "s_add_u32 s30, s26, 65536\n\t"
  "buffer_load_dwordx4 v8, s[8:11], s30 offen offset:0 lds\n\t"
  "v_mfma_f32_16x16x32_bf16 a[44:47], a[184:187], v[136:139], a[44:47]\n\t"
  "buffer_load_dwordx4 v9, s[8:11], s30 offen offset:1024 lds\n\t"
  "v_mfma_f32_16x16x32_bf16 a[44:47], a[188:191], v[140:143], a[44:47]\n\t"
  "ds_read_b64_tr_b16 a[170:171], v7 offset:54272\n\t"
  "s_add_u32 m0, s24, 34816\n\t"
  "s_add_u32 s30, s26, 73728\n\t"
  "buffer_load_dwordx4 v8, s[8:11], s30 offen offset:0 lds\n\t"
  "v_mfma_f32_16x16x32_bf16 a[76:79], a[184:187], v[144:147], a[76:79]\n\t"
  "buffer_load_dwordx4 v9, s[8:11], s30 offen offset:1024 lds\n\t"
  "v_mfma_f32_16x16x32_bf16 a[76:79], a[188:191], v[148:151], a[76:79]\n\t"
  "ds_read_b64_tr_b16 a[172:173], v7 offset:58368\n\t"
  "s_add_u32 m0, s25, 32768\n\t"
  "s_add_u32 s31, s28, 2048\n\t"
  "buffer_load_dwordx4 v1, s[16:19], s31 offen offset:0 sc1 lds\n\t"
  "v_mfma_f32_16x16x32_bf16 a[108:111], a[184:187], v[152:155], a[108:111]\n\t"
  "buffer_load_dwordx4 v1, s[16:19], s31 offen offset:1024 sc1 lds\n\t"
  "v_mfma_f32_16x16x32_bf16 a[108:111], a[188:191], v[156:159], a[108:111]\n\t"
  "s_waitcnt lgkmcnt(2)\n\t"
  "s_nop 0\n\t"
  "ds_read_b64_tr_b16 a[174:175], v7 offset:62464\n\t"
  "s_add_u32 m0, s25, 34816\n\t"
  "s_add_u32 s31, s28, 190464\n\t"
  "buffer_load_dwordx4 v1, s[16:19], s31 offen offset:0 sc1 lds\n\t"
  "v_mfma_f32_16x16x32_bf16 a[16:19], a[160:163], v[128:131], a[16:19]\n\t"
  "buffer_load_dwordx4 v1, s[16:19], s31 offen offset:1024 sc1 lds\n\t"
  "v_mfma_f32_16x16x32_bf16 a[16:19], a[164:167], v[132:135], a[16:19]\n\t"
  "ds_read_b64_tr_b16 a[176:177], v6 offset:50688\n\t"
  "s_and_b32 s30, %7, 1\n\t"
  "s_lshl_b32 s30, s30, 13\n\t"
  "s_add_u32 s30, s30, 0x4000\n\t"
  "v_mfma_f32_16x16x32_bf16 a[48:51], a[160:163], v[136:139], a[48:51]\n\t"
  "v_add_u32_e64 v16, s30, v1\n\t"
  "v_add_u32_e32 v17, 0x10000, v16\n\t"
  "s_lshr_b32 s30, %7, 1\n\t"
  "s_nop 0\n\t"
  "v_mfma_f32_16x16x32_bf16 a[48:51], a[164:167], v[140:143], a[48:51]\n\t"
  "ds_read_b64_tr_b16 a[178:179], v6 offset:54784\n\t"
  "s_lshl_b32 s30, s30, 10\n\t"
  "v_lshrrev_b32_e32 v10, 5, v0\n\t"
  "v_mfma_f32_16x16x32_bf16 a[80:83], a[160:163], v[144:147], a[80:83]\n\t"
  "v_bfe_u32 v11, v0, 4, 1\n\t"
  "v_bfe_u32 v12, v0, 2, 2\n\t"
  "v_mfma_f32_16x16x32_bf16 a[80:83], a[164:167], v[148:151], a[80:83]\n\t"
  "ds_read_b64_tr_b16 a[180:181], v6 offset:58880\n\t"
  "v_lshlrev_b32_e64 v12, 6, v12\n\t"
  "v_mfma_f32_16x16x32_bf16 a[112:115], a[160:163], v[152:155], a[112:115]\n\t"
  "v_lshl_add_u32 v12, v11, 8, v12\n\t"
  "v_lshl_add_u32 v12, v10, 11, v12\n\t"
  "v_mfma_f32_16x16x32_bf16 a[112:115], a[164:167], v[156:159], a[112:115]\n\t"
  "s_waitcnt lgkmcnt(2)\n\t"
  "s_nop 0\n\t"
  "ds_read_b64_tr_b16 a[182:183], v6 offset:62976\n\t"
  "v_and_b32_e64 v13, 3, v0\n\t"
  "v_mfma_f32_16x16x32_bf16 a[20:23], a[168:171], v[128:131], a[20:23]\n\t"
  "v_lshl_add_u32 v12, v13, 3, v12\n\t"
  "v_add_u32_e64 v12, s30, v12\n\t"
  "v_mfma_f32_16x16x32_bf16 a[20:23], a[172:175], v[132:135], a[20:23]\n\t"
  "ds_read_b64_tr_b16 a[184:185], v7 offset:50688\n\t"
  "v_lshl_add_u32 v2, v11, 5, v12\n\t"
  "v_mfma_f32_16x16x32_bf16 a[52:55], a[168:171], v[136:139], a[52:55]\n\t"
  "v_xor_b32_e64 v11, 1, v11\n\t"
  "v_lshl_add_u32 v3, v11, 5, v12\n\t"
  "v_mfma_f32_16x16x32_bf16 a[52:55], a[172:175], v[140:143], a[52:55]\n\t"
  "ds_read_b64_tr_b16 a[186:187], v7 offset:54784\n\t"
  "v_add_u32_e32 v4, 0x10000, v2\n\t"
  "v_mfma_f32_16x16x32_bf16 a[84:87], a[168:171], v[144:147], a[84:87]\n\t"
  "v_add_u32_e32 v5, 0x10000, v3\n\t"
  "s_and_b32 s30, %7, 1\n\t"
  "s_lshl_b32 s36, s30, 17\n\t"
  "v_mfma_f32_16x16x32_bf16 a[84:87], a[172:175], v[148:151], a[84:87]\n\t"
  "ds_read_b64_tr_b16 a[188:189], v7 offset:58880\n\t"
  "s_lshl_b32 s30, %11, 14\n\t"
  "s_add_u32 s36, s36, s30\n\t"
  "s_lshr_b32 s30, %7, 1\n\t"
  "s_nop 0\n\t"
  "v_mfma_f32_16x16x32_bf16 a[116:119], a[168:171], v[152:155], a[116:119]\n\t"
  "s_lshl_b32 s30, s30, 11\n\t"
  "s_add_u32 s36, s36, s30\n\t"
  "s_lshr_b32 s30, %7, 1\n\t"
  "s_nop 0\n\t"
  "v_mfma_f32_16x16x32_bf16 a[116:119], a[172:175], v[156:159], a[116:119]\n\t"
  "s_waitcnt lgkmcnt(2)\n\t"
  "s_nop 0\n\t"
  "ds_read_b64_tr_b16 a[190:191], v7 offset:62976\n\t"
  "s_lshl_b32 s30, s30, 11\n\t"
  "s_add_u32 s30, s30, 131072\n\t"
  "v_add_u32_e32 v14, s30, v1\n\t"
  "v_mfma_f32_16x16x32_bf16 a[24:27], a[176:179], v[128:131], a[24:27]\n\t"
  "s_and_b32 s30, %7, 1\n\t"
  "s_lshl_b32 s37, s30, 1\n\t"
  "v_mfma_f32_16x16x32_bf16 a[24:27], a[180:183], v[132:135], a[24:27]\n\t"
  "v_mfma_f32_16x16x32_bf16 a[56:59], a[176:179], v[136:139], a[56:59]\n\t"
  "v_mfma_f32_16x16x32_bf16 a[56:59], a[180:183], v[140:143], a[56:59]\n\t"
  "v_mfma_f32_16x16x32_bf16 a[88:91], a[176:179], v[144:147], a[88:91]\n\t"
  "v_mfma_f32_16x16x32_bf16 a[88:91], a[180:183], v[148:151], a[88:91]\n\t"
  "v_mfma_f32_16x16x32_bf16 a[120:123], a[176:179], v[152:155], a[120:123]\n\t"
  "v_mfma_f32_16x16x32_bf16 a[120:123], a[180:183], v[156:159], a[120:123]\n\t"
  "s_waitcnt lgkmcnt(0)\n\t"
  "s_nop 0\n\t"
  "v_mfma_f32_16x16x32_bf16 a[28:31], a[184:187], v[128:131], a[28:31]\n\t"
  "v_mfma_f32_16x16x32_bf16 a[28:31], a[188:191], v[132:135], a[28:31]\n\t"
  "v_mfma_f32_16x16x32_bf16 a[60:63], a[184:187], v[136:139], a[60:63]\n\t"
  "v_mfma_f32_16x16x32_bf16 a[60:63], a[188:191], v[140:143], a[60:63]\n\t"
  "v_mfma_f32_16x16x32_bf16 a[92:95], a[184:187], v[144:147], a[92:95]\n\t"
  "v_mfma_f32_16x16x32_bf16 a[92:95], a[188:191], v[148:151], a[92:95]\n\t"
  "v_mfma_f32_16x16x32_bf16 a[124:127], a[184:187], v[152:155], a[124:127]\n\t"
  "v_mfma_f32_16x16x32_bf16 a[124:127], a[188:191], v[156:159], a[124:127]\n\t"
  "v_mfma_f32_16x16x32_bf16 a[192:195], a[208:211], v[128:131], a[192:195]\n\t"
  "v_mfma_f32_16x16x32_bf16 a[192:195], a[208:211], v[132:135], a[192:195]\n\t"
  "v_mfma_f32_16x16x32_bf16 a[196:199], a[208:211], v[136:139], a[196:199]\n\t"
  "v_mfma_f32_16x16x32_bf16 a[196:199], a[208:211], v[140:143], a[196:199]\n\t"
  "v_mfma_f32_16x16x32_bf16 a[200:203], a[208:211], v[144:147], a[200:203]\n\t"
  "v_mfma_f32_16x16x32_bf16 a[200:203], a[208:211], v[148:151], a[200:203]\n\t"
  "v_mfma_f32_16x16x32_bf16 a[204:207], a[208:211], v[152:155], a[204:207]\n\t"
  "v_mfma_f32_16x16x32_bf16 a[204:207], a[208:211], v[156:159], a[204:207]\n\t"
  "s_nop 1\n\t"
  "s_nop 0\n\t"
  "v_accvgpr_read_b32 v224, a192\n\t"
  "s_nop 0\n\t"
  "s_nop 0\n\t"
  "v_accvgpr_read_b32 v225, a196\n\t"
  "s_nop 0\n\t"
  "s_nop 0\n\t"
  "v_accvgpr_read_b32 v226, a200\n\t"
  "s_nop 0\n\t"
  "s_nop 0\n\t"
  "v_accvgpr_read_b32 v227, a204\n\t"
  "v_max3_f32 v228, v224, v225, v226\n\t"
  "v_max_f32_e32 v228, v228, v227\n\t"
  "v_add_f32_e32 v229, v224, v225\n\t"
  "v_add_f32_e32 v230, v226, v227\n\t"
  "v_add_f32_e32 v229, v229, v230\n\t"
  "s_mov_b32 s40, 0x5d800000\n\t"
  "v_cmp_nlt_f32_e64 s[42:43], v228, s40\n\t"
  "v_cmp_u_f32_e64 s[44:45], v229, v229\n\t"
  "s_nop 0\n\t"
  "s_or_b64 s[42:43], s[42:43], s[44:45]\n\t"
  "s_cmp_lg_u64 s[42:43], 0\n\t"
  "s_cbranch_scc1 L_fb_%=\n\t"
  "L_fbret_%=:\n\t"
  "v_accvgpr_read_b32 v224, a192\n\t"
  "v_accvgpr_read_b32 v226, a196\n\t"
  "v_accvgpr_read_b32 v228, a200\n\t"
  "v_accvgpr_read_b32 v230, a204\n\t"
  "v_rcp_f32_e32 v224, v224\n\t"
  "v_rcp_f32_e32 v226, v226\n\t"
  "v_rcp_f32_e32 v228, v228\n\t"
  "v_rcp_f32_e64 v230, v230\n\t"
  "s_mov_b32 s34, 753664\n\t"
  "s_lshl_b32 s38, %8, 12\n\t"
  "s_add_u32 s38, s38, 57344\n\t"
  "v_accvgpr_read_b32 v32, a0\n\t"
  "v_accvgpr_read_b32 v33, a1\n\t"
  "v_accvgpr_read_b32 v34, a2\n\t"
  "v_accvgpr_read_b32 v35, a3\n\t"
  "v_accvgpr_read_b32 v36, a4\n\t"
  "v_accvgpr_read_b32 v37, a5\n\t"
  "v_accvgpr_read_b32 v38, a6\n\t"
  "v_accvgpr_read_b32 v39, a7\n\t"
  "v_mul_f32_e32 v32, v32, v224\n\t"
  "v_mul_f32_e32 v33, v33, v224\n\t"
  "v_mul_f32_e32 v34, v34, v224\n\t"
  "v_mul_f32_e32 v35, v35, v224\n\t"
  "v_mul_f32_e32 v36, v36, v224\n\t"
  "v_mul_f32_e32 v37, v37, v224\n\t"
  "v_mul_f32_e32 v38, v38, v224\n\t"
  "v_mul_f32_e32 v39, v39, v224\n\t"
  "v_cvt_pk_bf16_f32 v96, v32, v33\n\t"
  "v_cvt_pk_bf16_f32 v97, v34, v35\n\t"
  "v_cvt_pk_bf16_f32 v98, v36, v37\n\t"
  "v_cvt_pk_bf16_f32 v99, v38, v39\n\t"
  "buffer_store_dwordx4 v[96:99], v1, s[32:35], s38 offen offset:0 sc1\n\t"
  "v_accvgpr_read_b32 v40, a8\n\t"
  "v_accvgpr_read_b32 v41, a9\n\t"
  "v_accvgpr_read_b32 v42, a10\n\t"
  "v_accvgpr_read_b32 v43, a11\n\t"
  "v_accvgpr_read_b32 v44, a12\n\t"
  "v_accvgpr_read_b32 v45, a13\n\t"
  "v_accvgpr_read_b32 v46, a14\n\t"
  "v_accvgpr_read_b32 v47, a15\n\t"
  "v_mul_f32_e32 v40, v40, v224\n\t"
  "v_mul_f32_e32 v41, v41, v224\n\t"
  "v_mul_f32_e32 v42, v42, v224\n\t"
  "v_mul_f32_e32 v43, v43, v224\n\t"
  "v_mul_f32_e32 v44, v44, v224\n\t"
  "v_mul_f32_e32 v45, v45, v224\n\t"
  "v_mul_f32_e32 v46, v46, v224\n\t"
  "v_mul_f32_e32 v47, v47, v224\n\t"
  "v_cvt_pk_bf16_f32 v100, v40, v41\n\t"
  "v_cvt_pk_bf16_f32 v101, v42, v43\n\t"
  "v_cvt_pk_bf16_f32 v102, v44, v45\n\t"
  "v_cvt_pk_bf16_f32 v103, v46, v47\n\t"
  "buffer_store_dwordx4 v[100:103], v1, s[32:35], s38 offen offset:1024 sc1\n\t"
  "v_accvgpr_read_b32 v48, a16\n\t"
  "v_accvgpr_read_b32 v49, a17\n\t"
  "v_accvgpr_read_b32 v50, a18\n\t"
  "v_accvgpr_read_b32 v51, a19\n\t"
  "v_accvgpr_read_b32 v52, a20\n\t"
  "v_accvgpr_read_b32 v53, a21\n\t"
  "v_accvgpr_read_b32 v54, a22\n\t"
  "v_accvgpr_read_b32 v55, a23\n\t"
  "v_mul_f32_e32 v48, v48, v224\n\t"
  "v_mul_f32_e32 v49, v49, v224\n\t"
  "v_mul_f32_e32 v50, v50, v224\n\t"
  "v_mul_f32_e32 v51, v51, v224\n\t"
  "v_mul_f32_e32 v52, v52, v224\n\t"
  "v_mul_f32_e32 v53, v53, v224\n\t"
  "v_mul_f32_e32 v54, v54, v224\n\t"
  "v_mul_f32_e32 v55, v55, v224\n\t"
  "v_cvt_pk_bf16_f32 v104, v48, v49\n\t"
  "v_cvt_pk_bf16_f32 v105, v50, v51\n\t"
  "v_cvt_pk_bf16_f32 v106, v52, v53\n\t"
  "v_cvt_pk_bf16_f32 v107, v54, v55\n\t"
  "buffer_store_dwordx4 v[104:107], v1, s[32:35], s38 offen offset:2048 sc1\n\t"
  "v_accvgpr_read_b32 v56, a24\n\t"
  "v_accvgpr_read_b32 v57, a25\n\t"
  "v_accvgpr_read_b32 v58, a26\n\t"
  "v_accvgpr_read_b32 v59, a27\n\t"
  "v_accvgpr_read_b32 v60, a28\n\t"
  "v_accvgpr_read_b32 v61, a29\n\t"
  "v_accvgpr_read_b32 v62, a30\n\t"
  "v_accvgpr_read_b32 v63, a31\n\t"
  "v_mul_f32_e32 v56, v56, v224\n\t"
  "v_mul_f32_e32 v57, v57, v224\n\t"
  "v_mul_f32_e32 v58, v58, v224\n\t"
  "v_mul_f32_e32 v59, v59, v224\n\t"
  "v_mul_f32_e32 v60, v60, v224\n\t"
  "v_mul_f32_e32 v61, v61, v224\n\t"
  "v_mul_f32_e32 v62, v62, v224\n\t"
  "v_mul_f32_e32 v63, v63, v224\n\t"
  "v_cvt_pk_bf16_f32 v108, v56, v57\n\t"
  "v_cvt_pk_bf16_f32 v109, v58, v59\n\t"
  "v_cvt_pk_bf16_f32 v110, v60, v61\n\t"
  "v_cvt_pk_bf16_f32 v111, v62, v63\n\t"
  "buffer_store_dwordx4 v[108:111], v1, s[32:35], s38 offen offset:3072 sc1\n\t"
  "s_lshl_b32 s38, %8, 12\n\t"
  "s_add_u32 s38, s38, 245760\n\t"
  "s_nop 0\n\t"
  "v_accvgpr_read_b32 v32, a32\n\t"
  "v_accvgpr_read_b32 v33, a33\n\t"
  "v_accvgpr_read_b32 v34, a34\n\t"
  "v_accvgpr_read_b32 v35, a35\n\t"
  "v_accvgpr_read_b32 v36, a36\n\t"
  "v_accvgpr_read_b32 v37, a37\n\t"
  "v_accvgpr_read_b32 v38, a38\n\t"
  "v_accvgpr_read_b32 v39, a39\n\t"
  "v_mul_f32_e32 v32, v32, v226\n\t"
  "v_mul_f32_e32 v33, v33, v226\n\t"
  "v_mul_f32_e32 v34, v34, v226\n\t"
  "v_mul_f32_e32 v35, v35, v226\n\t"
  "v_mul_f32_e32 v36, v36, v226\n\t"
  "v_mul_f32_e32 v37, v37, v226\n\t"
  "v_mul_f32_e32 v38, v38, v226\n\t"
  "v_mul_f32_e32 v39, v39, v226\n\t"
  "v_cvt_pk_bf16_f32 v96, v32, v33\n\t"
  "v_cvt_pk_bf16_f32 v97, v34, v35\n\t"
  "v_cvt_pk_bf16_f32 v98, v36, v37\n\t"
  "v_cvt_pk_bf16_f32 v99, v38, v39\n\t"
  "buffer_store_dwordx4 v[96:99], v1, s[32:35], s38 offen offset:0 sc1\n\t"
  "v_accvgpr_read_b32 v40, a40\n\t"
  "v_accvgpr_read_b32 v41, a41\n\t"
  "v_accvgpr_read_b32 v42, a42\n\t"
  "v_accvgpr_read_b32 v43, a43\n\t"
  "v_accvgpr_read_b32 v44, a44\n\t"
  "v_accvgpr_read_b32 v45, a45\n\t"
  "v_accvgpr_read_b32 v46, a46\n\t"
  "v_accvgpr_read_b32 v47, a47\n\t"
  "v_mul_f32_e32 v40, v40, v226\n\t"
  "v_mul_f32_e32 v41, v41, v226\n\t"
  "v_mul_f32_e32 v42, v42, v226\n\t"
  "v_mul_f32_e32 v43, v43, v226\n\t"
  "v_mul_f32_e32 v44, v44, v226\n\t"
  "v_mul_f32_e32 v45, v45, v226\n\t"
  "v_mul_f32_e32 v46, v46, v226\n\t"
  "v_mul_f32_e32 v47, v47, v226\n\t"
  "v_cvt_pk_bf16_f32 v100, v40, v41\n\t"
  "v_cvt_pk_bf16_f32 v101, v42, v43\n\t"
  "v_cvt_pk_bf16_f32 v102, v44, v45\n\t"
  "v_cvt_pk_bf16_f32 v103, v46, v47\n\t"
  "buffer_store_dwordx4 v[100:103], v1, s[32:35], s38 offen offset:1024 sc1\n\t"
  "v_accvgpr_read_b32 v48, a48\n\t"
  "v_accvgpr_read_b32 v49, a49\n\t"
  "v_accvgpr_read_b32 v50, a50\n\t"
  "v_accvgpr_read_b32 v51, a51\n\t"
  "v_accvgpr_read_b32 v52, a52\n\t"
  "v_accvgpr_read_b32 v53, a53\n\t"
  "v_accvgpr_read_b32 v54, a54\n\t"
  "v_accvgpr_read_b32 v55, a55\n\t"
  "v_mul_f32_e32 v48, v48, v226\n\t"
  "v_mul_f32_e32 v49, v49, v226\n\t"
  "v_mul_f32_e32 v50, v50, v226\n\t"
  "v_mul_f32_e32 v51, v51, v226\n\t"
  "v_mul_f32_e32 v52, v52, v226\n\t"
  "v_mul_f32_e32 v53, v53, v226\n\t"
  "v_mul_f32_e32 v54, v54, v226\n\t"
  "v_mul_f32_e32 v55, v55, v226\n\t"
  "v_cvt_pk_bf16_f32 v104, v48, v49\n\t"
  "v_cvt_pk_bf16_f32 v105, v50, v51\n\t"
  "v_cvt_pk_bf16_f32 v106, v52, v53\n\t"
  "v_cvt_pk_bf16_f32 v107, v54, v55\n\t"
  "buffer_store_dwordx4 v[104:107], v1, s[32:35], s38 offen offset:2048 sc1\n\t"
  "v_accvgpr_read_b32 v56, a56\n\t"
  "v_accvgpr_read_b32 v57, a57\n\t"
  "v_accvgpr_read_b32 v58, a58\n\t"
  "v_accvgpr_read_b32 v59, a59\n\t"
  "v_accvgpr_read_b32 v60, a60\n\t"
  "v_accvgpr_read_b32 v61, a61\n\t"
  "v_accvgpr_read_b32 v62, a62\n\t"
  "v_accvgpr_read_b32 v63, a63\n\t"
  "v_mul_f32_e32 v56, v56, v226\n\t"
  "v_mul_f32_e32 v57, v57, v226\n\t"
  "v_mul_f32_e32 v58, v58, v226\n\t"
  "v_mul_f32_e32 v59, v59, v226\n\t"
  "v_mul_f32_e32 v60, v60, v226\n\t"
  "v_mul_f32_e32 v61, v61, v226\n\t"
  "v_mul_f32_e32 v62, v62, v226\n\t"
  "v_mul_f32_e32 v63, v63, v226\n\t"
  "v_cvt_pk_bf16_f32 v108, v56, v57\n\t"
  "v_cvt_pk_bf16_f32 v109, v58, v59\n\t"
  "v_cvt_pk_bf16_f32 v110, v60, v61\n\t"
  "v_cvt_pk_bf16_f32 v111, v62, v63\n\t"
  "buffer_store_dwordx4 v[108:111], v1, s[32:35], s38 offen offset:3072 sc1\n\t"
  "s_lshl_b32 s38, %8, 12\n\t"
  "s_add_u32 s38, s38, 434176\n\t"
  "s_nop 0\n\t"
  "v_accvgpr_read_b32 v32, a64\n\t"
  "v_accvgpr_read_b32 v33, a65\n\t"
  "v_accvgpr_read_b32 v34, a66\n\t"
  "v_accvgpr_read_b32 v35, a67\n\t"
  "v_accvgpr_read_b32 v36, a68\n\t"
  "v_accvgpr_read_b32 v37, a69\n\t"
  "v_accvgpr_read_b32 v38, a70\n\t"
  "v_accvgpr_read_b32 v39, a71\n\t"
  "v_mul_f32_e32 v32, v32, v228\n\t"
  "v_mul_f32_e32 v33, v33, v228\n\t"
  "v_mul_f32_e32 v34, v34, v228\n\t"
  "v_mul_f32_e32 v35, v35, v228\n\t"
  "v_mul_f32_e32 v36, v36, v228\n\t"
  "v_mul_f32_e32 v37, v37, v228\n\t"
  "v_mul_f32_e32 v38, v38, v228\n\t"
  "v_mul_f32_e32 v39, v39, v228\n\t"
  "v_cvt_pk_bf16_f32 v96, v32, v33\n\t"
  "v_cvt_pk_bf16_f32 v97, v34, v35\n\t"
  "v_cvt_pk_bf16_f32 v98, v36, v37\n\t"
  "v_cvt_pk_bf16_f32 v99, v38, v39\n\t"
  "buffer_store_dwordx4 v[96:99], v1, s[32:35], s38 offen offset:0 sc1\n\t"
  "v_accvgpr_read_b32 v40, a72\n\t"
  "v_accvgpr_read_b32 v41, a73\n\t"
  "v_accvgpr_read_b32 v42, a74\n\t"
  "v_accvgpr_read_b32 v43, a75\n\t"
  "v_accvgpr_read_b32 v44, a76\n\t"
  "v_accvgpr_read_b32 v45, a77\n\t"
  "v_accvgpr_read_b32 v46, a78\n\t"
  "v_accvgpr_read_b32 v47, a79\n\t"
  "v_mul_f32_e32 v40, v40, v228\n\t"
  "v_mul_f32_e32 v41, v41, v228\n\t"
  "v_mul_f32_e32 v42, v42, v228\n\t"
  "v_mul_f32_e32 v43, v43, v228\n\t"
  "v_mul_f32_e32 v44, v44, v228\n\t"
  "v_mul_f32_e32 v45, v45, v228\n\t"
  "v_mul_f32_e32 v46, v46, v228\n\t"
  "v_mul_f32_e32 v47, v47, v228\n\t"
  "v_cvt_pk_bf16_f32 v100, v40, v41\n\t"
  "v_cvt_pk_bf16_f32 v101, v42, v43\n\t"
  "v_cvt_pk_bf16_f32 v102, v44, v45\n\t"
  "v_cvt_pk_bf16_f32 v103, v46, v47\n\t"
  "buffer_store_dwordx4 v[100:103], v1, s[32:35], s38 offen offset:1024 sc1\n\t"
  "v_accvgpr_read_b32 v48, a80\n\t"
  "v_accvgpr_read_b32 v49, a81\n\t"
  "v_accvgpr_read_b32 v50, a82\n\t"
  "v_accvgpr_read_b32 v51, a83\n\t"
  "v_accvgpr_read_b32 v52, a84\n\t"
  "v_accvgpr_read_b32 v53, a85\n\t"
  "v_accvgpr_read_b32 v54, a86\n\t"
  "v_accvgpr_read_b32 v55, a87\n\t"
  "v_mul_f32_e32 v48, v48, v228\n\t"
  "v_mul_f32_e32 v49, v49, v228\n\t"
  "v_mul_f32_e32 v50, v50, v228\n\t"
  "v_mul_f32_e32 v51, v51, v228\n\t"
  "v_mul_f32_e32 v52, v52, v228\n\t"
  "v_mul_f32_e32 v53, v53, v228\n\t"
  "v_mul_f32_e32 v54, v54, v228\n\t"
  "v_mul_f32_e32 v55, v55, v228\n\t"
  "v_cvt_pk_bf16_f32 v104, v48, v49\n\t"
  "v_cvt_pk_bf16_f32 v105, v50, v51\n\t"
  "v_cvt_pk_bf16_f32 v106, v52, v53\n\t"
  "v_cvt_pk_bf16_f32 v107, v54, v55\n\t"
  "buffer_store_dwordx4 v[104:107], v1, s[32:35], s38 offen offset:2048 sc1\n\t"
  "v_accvgpr_read_b32 v56, a88\n\t"
  "v_accvgpr_read_b32 v57, a89\n\t"
  "v_accvgpr_read_b32 v58, a90\n\t"
  "v_accvgpr_read_b32 v59, a91\n\t"
  "v_accvgpr_read_b32 v60, a92\n\t"
  "v_accvgpr_read_b32 v61, a93\n\t"
  "v_accvgpr_read_b32 v62, a94\n\t"
  "v_accvgpr_read_b32 v63, a95\n\t"
  "v_mul_f32_e32 v56, v56, v228\n\t"
  "v_mul_f32_e32 v57, v57, v228\n\t"
  "v_mul_f32_e32 v58, v58, v228\n\t"
  "v_mul_f32_e32 v59, v59, v228\n\t"
  "v_mul_f32_e32 v60, v60, v228\n\t"
  "v_mul_f32_e32 v61, v61, v228\n\t"
  "v_mul_f32_e32 v62, v62, v228\n\t"
  "v_mul_f32_e32 v63, v63, v228\n\t"
  "v_cvt_pk_bf16_f32 v108, v56, v57\n\t"
  "v_cvt_pk_bf16_f32 v109, v58, v59\n\t"
  "v_cvt_pk_bf16_f32 v110, v60, v61\n\t"
  "v_cvt_pk_bf16_f32 v111, v62, v63\n\t"
  "buffer_store_dwordx4 v[108:111], v1, s[32:35], s38 offen offset:3072 sc1\n\t"
  "s_lshl_b32 s38, %8, 12\n\t"
  "s_add_u32 s38, s38, 622592\n\t"
  "s_nop 0\n\t"
  "v_accvgpr_read_b32 v32, a96\n\t"
  "v_accvgpr_read_b32 v33, a97\n\t"
  "v_accvgpr_read_b32 v34, a98\n\t"
  "v_accvgpr_read_b32 v35, a99\n\t"
  "v_accvgpr_read_b32 v36, a100\n\t"
  "v_accvgpr_read_b32 v37, a101\n\t"
  "v_accvgpr_read_b32 v38, a102\n\t"
  "v_accvgpr_read_b32 v39, a103\n\t"
  "v_mul_f32_e32 v32, v32, v230\n\t"
  "v_mul_f32_e32 v33, v33, v230\n\t"
  "v_mul_f32_e32 v34, v34, v230\n\t"
  "v_mul_f32_e32 v35, v35, v230\n\t"
  "v_mul_f32_e32 v36, v36, v230\n\t"
  "v_mul_f32_e32 v37, v37, v230\n\t"
  "v_mul_f32_e32 v38, v38, v230\n\t"
  "v_mul_f32_e32 v39, v39, v230\n\t"
  "v_cvt_pk_bf16_f32 v96, v32, v33\n\t"
  "v_cvt_pk_bf16_f32 v97, v34, v35\n\t"
  "v_cvt_pk_bf16_f32 v98, v36, v37\n\t"
  "v_cvt_pk_bf16_f32 v99, v38, v39\n\t"
  "buffer_store_dwordx4 v[96:99], v1, s[32:35], s38 offen offset:0 sc1\n\t"
  "v_accvgpr_read_b32 v40, a104\n\t"
  "v_accvgpr_read_b32 v41, a105\n\t"
  "v_accvgpr_read_b32 v42, a106\n\t"
  "v_accvgpr_read_b32 v43, a107\n\t"
  "v_accvgpr_read_b32 v44, a108\n\t"
  "v_accvgpr_read_b32 v45, a109\n\t"
  "v_accvgpr_read_b32 v46, a110\n\t"
  "v_accvgpr_read_b32 v47, a111\n\t"
  "v_mul_f32_e32 v40, v40, v230\n\t"
  "v_mul_f32_e32 v41, v41, v230\n\t"
  "v_mul_f32_e32 v42, v42, v230\n\t"
  "v_mul_f32_e32 v43, v43, v230\n\t"
  "v_mul_f32_e32 v44, v44, v230\n\t"
  "v_mul_f32_e32 v45, v45, v230\n\t"
  "v_mul_f32_e32 v46, v46, v230\n\t"
  "v_mul_f32_e32 v47, v47, v230\n\t"
  "v_cvt_pk_bf16_f32 v100, v40, v41\n\t"
  "v_cvt_pk_bf16_f32 v101, v42, v43\n\t"
  "v_cvt_pk_bf16_f32 v102, v44, v45\n\t"
  "v_cvt_pk_bf16_f32 v103, v46, v47\n\t"
  "buffer_store_dwordx4 v[100:103], v1, s[32:35], s38 offen offset:1024 sc1\n\t"
  "v_accvgpr_read_b32 v48, a112\n\t"
  "v_accvgpr_read_b32 v49, a113\n\t"
  "v_accvgpr_read_b32 v50, a114\n\t"
  "v_accvgpr_read_b32 v51, a115\n\t"
  "v_accvgpr_read_b32 v52, a116\n\t"
  "v_accvgpr_read_b32 v53, a117\n\t"
  "v_accvgpr_read_b32 v54, a118\n\t"
  "v_accvgpr_read_b32 v55, a119\n\t"
  "v_mul_f32_e32 v48, v48, v230\n\t"
  "v_mul_f32_e32 v49, v49, v230\n\t"
  "v_mul_f32_e32 v50, v50, v230\n\t"
  "v_mul_f32_e32 v51, v51, v230\n\t"
  "v_mul_f32_e32 v52, v52, v230\n\t"
  "v_mul_f32_e32 v53, v53, v230\n\t"
  "v_mul_f32_e32 v54, v54, v230\n\t"
  "v_mul_f32_e32 v55, v55, v230\n\t"
  "v_cvt_pk_bf16_f32 v104, v48, v49\n\t"
  "v_cvt_pk_bf16_f32 v105, v50, v51\n\t"
  "v_cvt_pk_bf16_f32 v106, v52, v53\n\t"
  "v_cvt_pk_bf16_f32 v107, v54, v55\n\t"
  "buffer_store_dwordx4 v[104:107], v1, s[32:35], s38 offen offset:2048 sc1\n\t"
  "v_accvgpr_read_b32 v56, a120\n\t"
  "v_accvgpr_read_b32 v57, a121\n\t"
  "v_accvgpr_read_b32 v58, a122\n\t"
  "v_accvgpr_read_b32 v59, a123\n\t"
  "v_accvgpr_read_b32 v60, a124\n\t"
  "v_accvgpr_read_b32 v61, a125\n\t"
  "v_accvgpr_read_b32 v62, a126\n\t"
  "v_accvgpr_read_b32 v63, a127\n\t"
  "v_mul_f32_e32 v56, v56, v230\n\t"
  "v_mul_f32_e32 v57, v57, v230\n\t"
  "v_mul_f32_e32 v58, v58, v230\n\t"
  "v_mul_f32_e32 v59, v59, v230\n\t"
  "v_mul_f32_e32 v60, v60, v230\n\t"
  "v_mul_f32_e32 v61, v61, v230\n\t"
  "v_mul_f32_e32 v62, v62, v230\n\t"
  "v_mul_f32_e32 v63, v63, v230\n\t"
  "v_cvt_pk_bf16_f32 v108, v56, v57\n\t"
  "v_cvt_pk_bf16_f32 v109, v58, v59\n\t"
  "v_cvt_pk_bf16_f32 v110, v60, v61\n\t"
  "v_cvt_pk_bf16_f32 v111, v62, v63\n\t"
  "buffer_store_dwordx4 v[108:111], v1, s[32:35], s38 offen offset:3072 sc1\n\t"
  "s_waitcnt vmcnt(24)\n\t"
  "s_barrier\n\t"
  "ds_read_b128 a[128:131], v16 offset:0\n\t"
  "ds_read_b128 a[132:135], v16 offset:1024\n\t"
  "ds_read_b128 a[136:139], v16 offset:2048\n\t"
  "ds_read_b128 a[140:143], v16 offset:3072\n\t"
  "ds_read_b128 a[144:147], v16 offset:4096\n\t"
  "ds_read_b128 a[148:151], v16 offset:5120\n\t"
  "ds_read_b128 a[152:155], v16 offset:6144\n\t"
  "ds_read_b128 a[156:159], v16 offset:7168\n\t"
  "ds_read_b64_tr_b16 v[160:161], v2 offset:0\n\t"
  "ds_read_b64_tr_b16 v[162:163], v2 offset:4096\n\t"
  "ds_read_b64_tr_b16 v[164:165], v2 offset:8192\n\t"
  "ds_read_b64_tr_b16 v[166:167], v2 offset:12288\n\t"
  "ds_read_b64_tr_b16 v[168:169], v3 offset:0\n\t"
  "ds_read_b64_tr_b16 v[170:171], v3 offset:4096\n\t"
  "ds_read_b64_tr_b16 v[172:173], v3 offset:8192\n\t"
  "ds_read_b64_tr_b16 v[174:175], v3 offset:12288\n\t"
  "s_waitcnt lgkmcnt(6)\n\t"
  "s_nop 0\n\t"
  "v_mfma_f32_16x16x32_bf16 v[32:35], v[160:163], a[128:131], 0\n\t"
  "s_waitcnt lgkmcnt(4)\n\t"
  "s_add_u32 m0, s24, 65536\n\t"
  "s_add_u32 s30, s26, 131072\n\t"
  "s_nop 0\n\t"
  "buffer_load_dwordx4 v8, s[8:11], s30 offen offset:0 lds\n\t"
  "v_mfma_f32_16x16x32_bf16 v[32:35], v[164:167], a[132:135], v[32:35]\n\t"
  "ds_read_b64_tr_b16 v[176:177], v2 offset:512\n\t"
  "ds_read_b64_tr_b16 v[178:179], v2 offset:4608\n\t"
  "v_mfma_f32_16x16x32_bf16 v[36:39], v[160:163], a[136:139], 0\n\t"
  "buffer_load_dwordx4 v9, s[8:11], s30 offen offset:1024 lds\n\t"
  "v_mfma_f32_16x16x32_bf16 v[36:39], v[164:167], a[140:143], v[36:39]\n\t"
  "ds_read_b64_tr_b16 v[180:181], v2 offset:8704\n\t"
  "ds_read_b64_tr_b16 v[182:183], v2 offset:12800\n\t"
  "v_mfma_f32_16x16x32_bf16 v[40:43], v[160:163], a[144:147], 0\n\t"
  "s_add_u32 m0, s24, 67584\n\t"
  "s_add_u32 s30, s26, 139264\n\t"
  "buffer_load_dwordx4 v8, s[8:11], s30 offen offset:0 lds\n\t"
  "v_mfma_f32_16x16x32_bf16 v[40:43], v[164:167], a[148:151], v[40:43]\n\t"
  "v_mfma_f32_16x16x32_bf16 v[44:47], v[160:163], a[152:155], 0\n\t"
  "buffer_load_dwordx4 v9, s[8:11], s30 offen offset:1024 lds\n\t"
  "v_mfma_f32_16x16x32_bf16 v[44:47], v[164:167], a[156:159], v[44:47]\n\t"
  "s_waitcnt lgkmcnt(2)\n\t"
  "s_nop 0\n\t"
  "v_mfma_f32_16x16x32_bf16 v[48:51], v[168:171], a[128:131], 0\n\t"
  "s_add_u32 m0, s25, 65536\n\t"
  "s_add_u32 s31, s28, 4096\n\t"
  "buffer_load_dwordx4 v1, s[16:19], s31 offen offset:0 sc1 lds\n\t"
  "v_mfma_f32_16x16x32_bf16 v[48:51], v[172:175], a[132:135], v[48:51]\n\t"
  "ds_read_b64_tr_b16 v[160:161], v3 offset:512\n\t"
  "ds_read_b64_tr_b16 v[162:163], v3 offset:4608\n\t"
  "v_mfma_f32_16x16x32_bf16 v[52:55], v[168:171], a[136:139], 0\n\t"
  "buffer_load_dwordx4 v1, s[16:19], s31 offen offset:1024 sc1 lds\n\t"
  "v_mfma_f32_16x16x32_bf16 v[52:55], v[172:175], a[140:143], v[52:55]\n\t"
  "ds_read_b64_tr_b16 v[164:165], v3 offset:8704\n\t"
  "ds_read_b64_tr_b16 v[166:167], v3 offset:12800\n\t"
  "v_mfma_f32_16x16x32_bf16 v[56:59], v[168:171], a[144:147], 0\n\t"
  "s_add_u32 m0, s25, 67584\n\t"
  "s_add_u32 s31, s28, 192512\n\t"
  "buffer_load_dwordx4 v1, s[16:19], s31 offen offset:0 sc1 lds\n\t"
  "v_mfma_f32_16x16x32_bf16 v[56:59], v[172:175], a[148:151], v[56:59]\n\t"
  "v_mfma_f32_16x16x32_bf16 v[60:63], v[168:171], a[152:155], 0\n\t"
  "buffer_load_dwordx4 v1, s[16:19], s31 offen offset:1024 sc1 lds\n\t"
  "v_mfma_f32_16x16x32_bf16 v[60:63], v[172:175], a[156:159], v[60:63]\n\t"
  "s_waitcnt vmcnt(8)\n\t"
  "s_barrier\n\t"
  "s_lshr_b32 s41, %9, 4\n\t"
  "s_lshl_b32 s41, s41, 2\n\t"
  "s_lshr_b32 s42, %8, 3\n\t"
  "s_add_u32 s41, s41, s42\n\t"
  "s_lshl_b32 s40, s41, 8\n\t"
  "s_add_u32 s40, s40, 2252800\n\t"
  "s_add_u32 s50, s64, s40\n\t"
  "s_addc_u32 s51, s65, 0\n\t"
  "s_cmp_eq_u32 %7, 0\n\t"
  "s_cbranch_scc0 L_nao_%=\n\t"
  "v_mov_b32_e64 v20, 1\n\t"
  "s_mov_b64 exec, 1\n\t"
  "global_atomic_add v21, v20, s[50:51]\n\t"
  "s_mov_b64 exec, -1\n\t"
  "L_nao_%=:\n\t"
  "s_nop 0\n\t"
  "v_mfma_f32_16x16x32_bf16 v[64:67], v[176:179], a[128:131], 0\n\t"
  "s_waitcnt lgkmcnt(0)\n\t"
  "s_add_u32 m0, s24, 98304\n\t"
  "s_add_u32 s30, s26, 196608\n\t"
  "s_nop 0\n\t"
  "buffer_load_dwordx4 v8, s[8:11], s30 offen offset:0 lds\n\t"
  "v_mfma_f32_16x16x32_bf16 v[64:67], v[180:183], a[132:135], v[64:67]\n\t"
  "ds_read_b64_tr_b16 v[168:169], v2 offset:32768\n\t"
  "ds_read_b64_tr_b16 v[170:171], v2 offset:36864\n\t"
  "v_mfma_f32_16x16x32_bf16 v[68:71], v[176:179], a[136:139], 0\n\t"
  "buffer_load_dwordx4 v9, s[8:11], s30 offen offset:1024 lds\n\t"
  "v_mfma_f32_16x16x32_bf16 v[68:71], v[180:183], a[140:143], v[68:71]\n\t"
  "ds_read_b64_tr_b16 v[172:173], v2 offset:40960\n\t"
  "ds_read_b64_tr_b16 v[174:175], v2 offset:45056\n\t"
  "v_mfma_f32_16x16x32_bf16 v[72:75], v[176:179], a[144:147], 0\n\t"
  "s_add_u32 m0, s24, 100352\n\t"
  "s_add_u32 s30, s26, 204800\n\t"
  "buffer_load_dwordx4 v8, s[8:11], s30 offen offset:0 lds\n\t"
  "v_mfma_f32_16x16x32_bf16 v[72:75], v[180:183], a[148:151], v[72:75]\n\t"
  "ds_read_b128 a[160:163], v16 offset:32768\n\t"
  "ds_read_b128 a[164:167], v16 offset:33792\n\t"
  "v_mfma_f32_16x16x32_bf16 v[76:79], v[176:179], a[152:155], 0\n\t"
  "buffer_load_dwordx4 v9, s[8:11], s30 offen offset:1024 lds\n\t"
  "v_mfma_f32_16x16x32_bf16 v[76:79], v[180:183], a[156:159], v[76:79]\n\t"
  "ds_read_b128 a[168:171], v16 offset:34816\n\t"
  "ds_read_b128 a[172:175], v16 offset:35840\n\t"
  "v_mfma_f32_16x16x32_bf16 v[80:83], v[160:163], a[128:131], 0\n\t"
  "s_add_u32 m0, s25, 98304\n\t"
  "s_add_u32 s31, s28, 6144\n\t"
  "buffer_load_dwordx4 v1, s[16:19], s31 offen offset:0 sc1 lds\n\t"
  "v_mfma_f32_16x16x32_bf16 v[80:83], v[164:167], a[132:135], v[80:83]\n\t"
  "ds_read_b64_tr_b16 v[176:177], v3 offset:32768\n\t"
  "ds_read_b64_tr_b16 v[178:179], v3 offset:36864\n\t"
  "v_mfma_f32_16x16x32_bf16 v[84:87], v[160:163], a[136:139], 0\n\t"
  "buffer_load_dwordx4 v1, s[16:19], s31 offen offset:1024 sc1 lds\n\t"
  "v_mfma_f32_16x16x32_bf16 v[84:87], v[164:167], a[140:143], v[84:87]\n\t"
  "ds_read_b64_tr_b16 v[180:181], v3 offset:40960\n\t"
  "ds_read_b64_tr_b16 v[182:183], v3 offset:45056\n\t"
  "v_mfma_f32_16x16x32_bf16 v[88:91], v[160:163], a[144:147], 0\n\t"
  "s_add_u32 m0, s25, 100352\n\t"
  "s_add_u32 s31, s28, 194560\n\t"
  "buffer_load_dwordx4 v1, s[16:19], s31 offen offset:0 sc1 lds\n\t"
  "v_mfma_f32_16x16x32_bf16 v[88:91], v[164:167], a[148:151], v[88:91]\n\t"
  "ds_read_b128 a[176:179], v16 offset:36864\n\t"
  "ds_read_b128 a[180:183], v16 offset:37888\n\t"
  "v_mfma_f32_16x16x32_bf16 v[92:95], v[160:163], a[152:155], 0\n\t"
  "buffer_load_dwordx4 v1, s[16:19], s31 offen offset:1024 sc1 lds\n\t"
  "v_mfma_f32_16x16x32_bf16 v[92:95], v[164:167], a[156:159], v[92:95]\n\t"
  "s_waitcnt lgkmcnt(4)\n\t"
  "s_nop 0\n\t"
  "ds_read_b128 a[184:187], v16 offset:38912\n\t"
  "ds_read_b128 a[188:191], v16 offset:39936\n\t"
  "v_mfma_f32_16x16x32_bf16 v[32:35], v[168:171], a[160:163], v[32:35]\n\t"
  "v_mfma_f32_16x16x32_bf16 v[32:35], v[172:175], a[164:167], v[32:35]\n\t"
  "ds_read_b64_tr_b16 v[160:161], v2 offset:33280\n\t"
  "ds_read_b64_tr_b16 v[162:163], v2 offset:37376\n\t"
  "v_mfma_f32_16x16x32_bf16 v[36:39], v[168:171], a[168:171], v[36:39]\n\t"
  "v_mfma_f32_16x16x32_bf16 v[36:39], v[172:175], a[172:175], v[36:39]\n\t"
  "s_waitcnt lgkmcnt(4)\n\t"
  "s_nop 0\n\t"
  "ds_read_b64_tr_b16 v[164:165], v2 offset:41472\n\t"
  "ds_read_b64_tr_b16 v[166:167], v2 offset:45568\n\t"
  "v_mfma_f32_16x16x32_bf16 v[40:43], v[168:171], a[176:179], v[40:43]\n\t"
  "v_mfma_f32_16x16x32_bf16 v[40:43], v[172:175], a[180:183], v[40:43]\n\t"
  "s_waitcnt lgkmcnt(4)\n\t"
  "s_nop 0\n\t"
  "v_mfma_f32_16x16x32_bf16 v[44:47], v[168:171], a[184:187], v[44:47]\n\t"
  "v_mfma_f32_16x16x32_bf16 v[44:47], v[172:175], a[188:191], v[44:47]\n\t"
  "v_mfma_f32_16x16x32_bf16 v[48:51], v[176:179], a[160:163], v[48:51]\n\t"
  "v_mfma_f32_16x16x32_bf16 v[48:51], v[180:183], a[164:167], v[48:51]\n\t"
  "ds_read_b64_tr_b16 v[168:169], v3 offset:33280\n\t"
  "ds_read_b64_tr_b16 v[170:171], v3 offset:37376\n\t"
  "v_mfma_f32_16x16x32_bf16 v[52:55], v[176:179], a[168:171], v[52:55]\n\t"
  "v_mfma_f32_16x16x32_bf16 v[52:55], v[180:183], a[172:175], v[52:55]\n\t"
  "ds_read_b64_tr_b16 v[172:173], v3 offset:41472\n\t"
  "ds_read_b64_tr_b16 v[174:175], v3 offset:45568\n\t"
  "v_mfma_f32_16x16x32_bf16 v[56:59], v[176:179], a[176:179], v[56:59]\n\t"
  "v_mfma_f32_16x16x32_bf16 v[56:59], v[180:183], a[180:183], v[56:59]\n\t"
  "v_mfma_f32_16x16x32_bf16 v[60:63], v[176:179], a[184:187], v[60:63]\n\t"
  "v_mfma_f32_16x16x32_bf16 v[60:63], v[180:183], a[188:191], v[60:63]\n\t"
  "s_waitcnt vmcnt(8)\n\t"
  "s_barrier\n\t"
  "s_waitcnt lgkmcnt(2)\n\t"
  "s_nop 0\n\t"
  "v_mfma_f32_16x16x32_bf16 v[64:67], v[160:163], a[160:163], v[64:67]\n\t"
  "s_add_u32 m0, s24, 0\n\t"
  "s_add_u32 s30, s26, 262144\n\t"
  "s_nop 0\n\t"
  "buffer_load_dwordx4 v8, s[8:11], s30 offen offset:0 lds\n\t"
  "v_mfma_f32_16x16x32_bf16 v[64:67], v[164:167], a[164:167], v[64:67]\n\t"
  "ds_read_b64_tr_b16 v[176:177], v4 offset:0\n\t"
  "ds_read_b64_tr_b16 v[178:179], v4 offset:4096\n\t"
  "v_mfma_f32_16x16x32_bf16 v[68:71], v[160:163], a[168:171], v[68:71]\n\t"
  "buffer_load_dwordx4 v9, s[8:11], s30 offen offset:1024 lds\n\t"
  "v_mfma_f32_16x16x32_bf16 v[68:71], v[164:167], a[172:175], v[68:71]\n\t"
  "ds_read_b64_tr_b16 v[180:181], v4 offset:8192\n\t"
  "ds_read_b64_tr_b16 v[182:183], v4 offset:12288\n\t"
  "v_mfma_f32_16x16x32_bf16 v[72:75], v[160:163], a[176:179], v[72:75]\n\t"
  "s_add_u32 m0, s24, 2048\n\t"
  "s_add_u32 s30, s26, 270336\n\t"
  "buffer_load_dwordx4 v8, s[8:11], s30 offen offset:0 lds\n\t"
  "v_mfma_f32_16x16x32_bf16 v[72:75], v[164:167], a[180:183], v[72:75]\n\t"
  "ds_read_b128 a[128:131], v17 offset:0\n\t"
  "ds_read_b128 a[132:135], v17 offset:1024\n\t"
  "v_mfma_f32_16x16x32_bf16 v[76:79], v[160:163], a[184:187], v[76:79]\n\t"
  "buffer_load_dwordx4 v9, s[8:11], s30 offen offset:1024 lds\n\t"
  "v_mfma_f32_16x16x32_bf16 v[76:79], v[164:167], a[188:191], v[76:79]\n\t"
  "ds_read_b128 a[136:139], v17 offset:2048\n\t"
  "ds_read_b128 a[140:143], v17 offset:3072\n\t"
  "v_mfma_f32_16x16x32_bf16 v[80:83], v[168:171], a[160:163], v[80:83]\n\t"
  "s_waitcnt lgkmcnt(4)\n\t"
  "s_add_u32 m0, s25, 0\n\t"
  "s_add_u32 s31, s28, 8192\n\t"
  "buffer_load_dwordx4 v1, s[16:19], s31 offen offset:0 sc1 lds\n\t"
  "v_mfma_f32_16x16x32_bf16 v[80:83], v[172:175], a[164:167], v[80:83]\n\t"
  "ds_read_b64_tr_b16 v[160:161], v5 offset:0\n\t"
  "ds_read_b64_tr_b16 v[162:163], v5 offset:4096\n\t"
  "v_mfma_f32_16x16x32_bf16 v[84:87], v[168:171], a[168:171], v[84:87]\n\t"
  "buffer_load_dwordx4 v1, s[16:19], s31 offen offset:1024 sc1 lds\n\t"
  "v_mfma_f32_16x16x32_bf16 v[84:87], v[172:175], a[172:175], v[84:87]\n\t"
  "ds_read_b64_tr_b16 v[164:165], v5 offset:8192\n\t"
  "ds_read_b64_tr_b16 v[166:167], v5 offset:12288\n\t"
  "v_mfma_f32_16x16x32_bf16 v[88:91], v[168:171], a[176:179], v[88:91]\n\t"
  "s_add_u32 m0, s25, 2048\n\t"
  "s_add_u32 s31, s28, 196608\n\t"
  "buffer_load_dwordx4 v1, s[16:19], s31 offen offset:0 sc1 lds\n\t"
  "v_mfma_f32_16x16x32_bf16 v[88:91], v[172:175], a[180:183], v[88:91]\n\t"
  "ds_read_b128 a[144:147], v17 offset:4096\n\t"
  "ds_read_b128 a[148:151], v17 offset:5120\n\t"
  "v_mfma_f32_16x16x32_bf16 v[92:95], v[168:171], a[184:187], v[92:95]\n\t"
  "buffer_load_dwordx4 v1, s[16:19], s31 offen offset:1024 sc1 lds\n\t"
  "v_mfma_f32_16x16x32_bf16 v[92:95], v[172:175], a[188:191], v[92:95]\n\t"
  "s_waitcnt lgkmcnt(4)\n\t"
  "s_nop 0\n\t"
  "ds_read_b128 a[152:155], v17 offset:6144\n\t"
  "ds_read_b128 a[156:159], v17 offset:7168\n\t"
  "v_mfma_f32_16x16x32_bf16 v[32:35], v[176:179], a[128:131], v[32:35]\n\t"
  "v_mfma_f32_16x16x32_bf16 v[32:35], v[180:183], a[132:135], v[32:35]\n\t"
  "ds_read_b64_tr_b16 v[168:169], v4 offset:512\n\t"
  "ds_read_b64_tr_b16 v[170:171], v4 offset:4608\n\t"
  "v_mfma_f32_16x16x32_bf16 v[36:39], v[176:179], a[136:139], v[36:39]\n\t"
  "v_mfma_f32_16x16x32_bf16 v[36:39], v[180:183], a[140:143], v[36:39]\n\t"
  "s_waitcnt lgkmcnt(4)\n\t"
  "s_nop 0\n\t"
  "ds_read_b64_tr_b16 v[172:173], v4 offset:8704\n\t"
  "ds_read_b64_tr_b16 v[174:175], v4 offset:12800\n\t"
  "v_mfma_f32_16x16x32_bf16 v[40:43], v[176:179], a[144:147], v[40:43]\n\t"
  "v_mfma_f32_16x16x32_bf16 v[40:43], v[180:183], a[148:151], v[40:43]\n\t"
  "s_waitcnt lgkmcnt(4)\n\t"
  "s_nop 0\n\t"
  "v_mfma_f32_16x16x32_bf16 v[44:47], v[176:179], a[152:155], v[44:47]\n\t"
  "v_mfma_f32_16x16x32_bf16 v[44:47], v[180:183], a[156:159], v[44:47]\n\t"
  "v_mfma_f32_16x16x32_bf16 v[48:51], v[160:163], a[128:131], v[48:51]\n\t"
  "v_mfma_f32_16x16x32_bf16 v[48:51], v[164:167], a[132:135], v[48:51]\n\t"
  "ds_read_b64_tr_b16 v[176:177], v5 offset:512\n\t"
  "ds_read_b64_tr_b16 v[178:179], v5 offset:4608\n\t"
  "v_mfma_f32_16x16x32_bf16 v[52:55], v[160:163], a[136:139], v[52:55]\n\t"
  "v_mfma_f32_16x16x32_bf16 v[52:55], v[164:167], a[140:143], v[52:55]\n\t"
  "ds_read_b64_tr_b16 v[180:181], v5 offset:8704\n\t"
  "ds_read_b64_tr_b16 v[182:183], v5 offset:12800\n\t"
  "v_mfma_f32_16x16x32_bf16 v[56:59], v[160:163], a[144:147], v[56:59]\n\t"
  "v_mfma_f32_16x16x32_bf16 v[56:59], v[164:167], a[148:151], v[56:59]\n\t"
  "v_mfma_f32_16x16x32_bf16 v[60:63], v[160:163], a[152:155], v[60:63]\n\t"
  "v_mfma_f32_16x16x32_bf16 v[60:63], v[164:167], a[156:159], v[60:63]\n\t"
  "s_waitcnt vmcnt(8)\n\t"
  "s_barrier\n\t"
  "s_waitcnt lgkmcnt(2)\n\t"
  "s_nop 0\n\t"
  "v_mfma_f32_16x16x32_bf16 v[64:67], v[168:171], a[128:131], v[64:67]\n\t"
  "s_add_u32 m0, s24, 32768\n\t"
  "s_add_u32 s30, s26, 327680\n\t"
  "buffer_load_dwordx4 v8, s[8:11], s30 offen offset:0 lds\n\t"
  "v_mfma_f32_16x16x32_bf16 v[64:67], v[172:175], a[132:135], v[64:67]\n\t"
  "ds_read_b64_tr_b16 v[160:161], v4 offset:32768\n\t"
  "ds_read_b64_tr_b16 v[162:163], v4 offset:36864\n\t"
  "v_mfma_f32_16x16x32_bf16 v[68:71], v[168:171], a[136:139], v[68:71]\n\t"
  "buffer_load_dwordx4 v9, s[8:11], s30 offen offset:1024 lds\n\t"
  "v_mfma_f32_16x16x32_bf16 v[68:71], v[172:175], a[140:143], v[68:71]\n\t"
  "ds_read_b64_tr_b16 v[164:165], v4 offset:40960\n\t"
  "ds_read_b64_tr_b16 v[166:167], v4 offset:45056\n\t"
  "v_mfma_f32_16x16x32_bf16 v[72:75], v[168:171], a[144:147], v[72:75]\n\t"
  "s_add_u32 m0, s24, 34816\n\t"
  "s_add_u32 s30, s26, 335872\n\t"
  "buffer_load_dwordx4 v8, s[8:11], s30 offen offset:0 lds\n\t"
  "v_mfma_f32_16x16x32_bf16 v[72:75], v[172:175], a[148:151], v[72:75]\n\t"
  "ds_read_b128 a[160:163], v17 offset:32768\n\t"
  "ds_read_b128 a[164:167], v17 offset:33792\n\t"
  "v_mfma_f32_16x16x32_bf16 v[76:79], v[168:171], a[152:155], v[76:79]\n\t"
  "buffer_load_dwordx4 v9, s[8:11], s30 offen offset:1024 lds\n\t"
  "v_mfma_f32_16x16x32_bf16 v[76:79], v[172:175], a[156:159], v[76:79]\n\t"
  "ds_read_b128 a[168:171], v17 offset:34816\n\t"
  "ds_read_b128 a[172:175], v17 offset:35840\n\t"
  "v_mfma_f32_16x16x32_bf16 v[80:83], v[176:179], a[128:131], v[80:83]\n\t"
  "s_waitcnt lgkmcnt(4)\n\t"
  "s_add_u32 m0, s25, 32768\n\t"
  "s_add_u32 s31, s28, 10240\n\t"
  "s_nop 0\n\t"
  "buffer_load_dwordx4 v1, s[16:19], s31 offen offset:0 sc1 lds\n\t"
  "v_mfma_f32_16x16x32_bf16 v[80:83], v[180:183], a[132:135], v[80:83]\n\t"
  "ds_read_b64_tr_b16 v[168:169], v5 offset:32768\n\t"
  "ds_read_b64_tr_b16 v[170:171], v5 offset:36864\n\t"
  "v_mfma_f32_16x16x32_bf16 v[84:87], v[176:179], a[136:139], v[84:87]\n\t"
  "buffer_load_dwordx4 v1, s[16:19], s31 offen offset:1024 sc1 lds\n\t"
  "v_mfma_f32_16x16x32_bf16 v[84:87], v[180:183], a[140:143], v[84:87]\n\t"
  "ds_read_b64_tr_b16 v[172:173], v5 offset:40960\n\t"
  "ds_read_b64_tr_b16 v[174:175], v5 offset:45056\n\t"
  "v_mfma_f32_16x16x32_bf16 v[88:91], v[176:179], a[144:147], v[88:91]\n\t"
  "s_add_u32 m0, s25, 34816\n\t"
  "s_add_u32 s31, s28, 198656\n\t"
  "buffer_load_dwordx4 v1, s[16:19], s31 offen offset:0 sc1 lds\n\t"
  "v_mfma_f32_16x16x32_bf16 v[88:91], v[180:183], a[148:151], v[88:91]\n\t"
  "ds_read_b128 a[176:179], v17 offset:36864\n\t"
  "ds_read_b128 a[180:183], v17 offset:37888\n\t"
  "v_mfma_f32_16x16x32_bf16 v[92:95], v[176:179], a[152:155], v[92:95]\n\t"
  "buffer_load_dwordx4 v1, s[16:19], s31 offen offset:1024 sc1 lds\n\t"
  "v_mfma_f32_16x16x32_bf16 v[92:95], v[180:183], a[156:159], v[92:95]\n\t"
  "s_waitcnt lgkmcnt(4)\n\t"
  "s_nop 0\n\t"
  "ds_read_b128 a[184:187], v17 offset:38912\n\t"
  "ds_read_b128 a[188:191], v17 offset:39936\n\t"
  "v_mfma_f32_16x16x32_bf16 v[32:35], v[160:163], a[160:163], v[32:35]\n\t"
  "v_mfma_f32_16x16x32_bf16 v[32:35], v[164:167], a[164:167], v[32:35]\n\t"
  "ds_read_b64_tr_b16 v[176:177], v4 offset:33280\n\t"
  "ds_read_b64_tr_b16 v[178:179], v4 offset:37376\n\t"
  "v_mfma_f32_16x16x32_bf16 v[36:39], v[160:163], a[168:171], v[36:39]\n\t"
  "v_mfma_f32_16x16x32_bf16 v[36:39], v[164:167], a[172:175], v[36:39]\n\t"
  "s_waitcnt lgkmcnt(4)\n\t"
  "s_nop 0\n\t"
  "ds_read_b64_tr_b16 v[180:181], v4 offset:41472\n\t"
  "ds_read_b64_tr_b16 v[182:183], v4 offset:45568\n\t"
  "v_mfma_f32_16x16x32_bf16 v[40:43], v[160:163], a[176:179], v[40:43]\n\t"
  "v_mfma_f32_16x16x32_bf16 v[40:43], v[164:167], a[180:183], v[40:43]\n\t"
  "s_waitcnt lgkmcnt(4)\n\t"
  "s_nop 0\n\t"
  "v_mfma_f32_16x16x32_bf16 v[44:47], v[160:163], a[184:187], v[44:47]\n\t"
  "v_mfma_f32_16x16x32_bf16 v[44:47], v[164:167], a[188:191], v[44:47]\n\t"
  "v_mfma_f32_16x16x32_bf16 v[48:51], v[168:171], a[160:163], v[48:51]\n\t"
  "v_mfma_f32_16x16x32_bf16 v[48:51], v[172:175], a[164:167], v[48:51]\n\t"
  "ds_read_b64_tr_b16 v[160:161], v5 offset:33280\n\t"
  "ds_read_b64_tr_b16 v[162:163], v5 offset:37376\n\t"
  "v_mfma_f32_16x16x32_bf16 v[52:55], v[168:171], a[168:171], v[52:55]\n\t"
  "v_mfma_f32_16x16x32_bf16 v[52:55], v[172:175], a[172:175], v[52:55]\n\t"
  "ds_read_b64_tr_b16 v[164:165], v5 offset:41472\n\t"
  "ds_read_b64_tr_b16 v[166:167], v5 offset:45568\n\t"
  "v_mfma_f32_16x16x32_bf16 v[56:59], v[168:171], a[176:179], v[56:59]\n\t"
  "v_mfma_f32_16x16x32_bf16 v[56:59], v[172:175], a[180:183], v[56:59]\n\t"
  "v_mfma_f32_16x16x32_bf16 v[60:63], v[168:171], a[184:187], v[60:63]\n\t"
  "v_mfma_f32_16x16x32_bf16 v[60:63], v[172:175], a[188:191], v[60:63]\n\t"
  "s_waitcnt vmcnt(8)\n\t"
  "s_barrier\n\t"
  "s_lshr_b32 s41, %12, 1\n\t"
  "s_lshl_b32 s41, s41, 2\n\t"
  "s_add_u32 s41, s41, %11\n\t"
  "s_lshl_b32 s40, s41, 8\n\t"
  "s_add_u32 s40, s40, 2252800\n\t"
  "s_add_u32 s50, s64, s40\n\t"
  "s_addc_u32 s51, s65, 0\n\t"
  "global_load_dword v20, v21, s[50:51] sc1\n\t"
  "s_waitcnt lgkmcnt(2)\n\t"
  "s_nop 0\n\t"
  "v_mfma_f32_16x16x32_bf16 v[64:67], v[176:179], a[160:163], v[64:67]\n\t"
  "s_add_u32 m0, s24, 65536\n\t"
  "s_add_u32 s30, s26, 393216\n\t"
  "buffer_load_dwordx4 v8, s[8:11], s30 offen offset:0 lds\n\t"
  "v_mfma_f32_16x16x32_bf16 v[64:67], v[180:183], a[164:167], v[64:67]\n\t"
  "ds_read_b64_tr_b16 v[168:169], v2 offset:0\n\t"
  "ds_read_b64_tr_b16 v[170:171], v2 offset:4096\n\t"
  "v_mfma_f32_16x16x32_bf16 v[68:71], v[176:179], a[168:171], v[68:71]\n\t"
  "buffer_load_dwordx4 v9, s[8:11], s30 offen offset:1024 lds\n\t"
  "v_mfma_f32_16x16x32_bf16 v[68:71], v[180:183], a[172:175], v[68:71]\n\t"
  "ds_read_b64_tr_b16 v[172:173], v2 offset:8192\n\t"
  "ds_read_b64_tr_b16 v[174:175], v2 offset:12288\n\t"
  "v_mfma_f32_16x16x32_bf16 v[72:75], v[176:179], a[176:179], v[72:75]\n\t"
  "s_add_u32 m0, s24, 67584\n\t"
  "s_add_u32 s30, s26, 401408\n\t"
  "buffer_load_dwordx4 v8, s[8:11], s30 offen offset:0 lds\n\t"
  "v_mfma_f32_16x16x32_bf16 v[72:75], v[180:183], a[180:183], v[72:75]\n\t"
  "ds_read_b128 a[128:131], v16 offset:0\n\t"
  "ds_read_b128 a[132:135], v16 offset:1024\n\t"
  "v_mfma_f32_16x16x32_bf16 v[76:79], v[176:179], a[184:187], v[76:79]\n\t"
  "buffer_load_dwordx4 v9, s[8:11], s30 offen offset:1024 lds\n\t"
  "v_mfma_f32_16x16x32_bf16 v[76:79], v[180:183], a[188:191], v[76:79]\n\t"
  "ds_read_b128 a[136:139], v16 offset:2048\n\t"
  "ds_read_b128 a[140:143], v16 offset:3072\n\t"
  "v_mfma_f32_16x16x32_bf16 v[80:83], v[160:163], a[160:163], v[80:83]\n\t"
  "s_waitcnt lgkmcnt(4)\n\t"
  "s_add_u32 m0, s25, 65536\n\t"
  "s_add_u32 s31, s28, 12288\n\t"
  "s_nop 0\n\t"
  "buffer_load_dwordx4 v1, s[16:19], s31 offen offset:0 sc1 lds\n\t"
  "v_mfma_f32_16x16x32_bf16 v[80:83], v[164:167], a[164:167], v[80:83]\n\t"
  "ds_read_b64_tr_b16 v[176:177], v3 offset:0\n\t"
  "ds_read_b64_tr_b16 v[178:179], v3 offset:4096\n\t"
  "v_mfma_f32_16x16x32_bf16 v[84:87], v[160:163], a[168:171], v[84:87]\n\t"
  "buffer_load_dwordx4 v1, s[16:19], s31 offen offset:1024 sc1 lds\n\t"
  "v_mfma_f32_16x16x32_bf16 v[84:87], v[164:167], a[172:175], v[84:87]\n\t"
  "ds_read_b64_tr_b16 v[180:181], v3 offset:8192\n\t"
  "ds_read_b64_tr_b16 v[182:183], v3 offset:12288\n\t"
  "v_mfma_f32_16x16x32_bf16 v[88:91], v[160:163], a[176:179], v[88:91]\n\t"
  "s_add_u32 m0, s25, 67584\n\t"
  "s_add_u32 s31, s28, 200704\n\t"
  "buffer_load_dwordx4 v1, s[16:19], s31 offen offset:0 sc1 lds\n\t"
  "v_mfma_f32_16x16x32_bf16 v[88:91], v[164:167], a[180:183], v[88:91]\n\t"
  "ds_read_b128 a[144:147], v16 offset:4096\n\t"
  "ds_read_b128 a[148:151], v16 offset:5120\n\t"
  "v_mfma_f32_16x16x32_bf16 v[92:95], v[160:163], a[184:187], v[92:95]\n\t"
  "buffer_load_dwordx4 v1, s[16:19], s31 offen offset:1024 sc1 lds\n\t"
  "v_mfma_f32_16x16x32_bf16 v[92:95], v[164:167], a[188:191], v[92:95]\n\t"
  "s_waitcnt lgkmcnt(4)\n\t"
  "s_nop 0\n\t"
  "ds_read_b128 a[152:155], v16 offset:6144\n\t"
  "ds_read_b128 a[156:159], v16 offset:7168\n\t"
  "v_mfma_f32_16x16x32_bf16 v[32:35], v[168:171], a[128:131], v[32:35]\n\t"
  "v_mfma_f32_16x16x32_bf16 v[32:35], v[172:175], a[132:135], v[32:35]\n\t"
  "ds_read_b64_tr_b16 v[160:161], v2 offset:512\n\t"
  "ds_read_b64_tr_b16 v[162:163], v2 offset:4608\n\t"
  "v_mfma_f32_16x16x32_bf16 v[36:39], v[168:171], a[136:139], v[36:39]\n\t"
  "v_mfma_f32_16x16x32_bf16 v[36:39], v[172:175], a[140:143], v[36:39]\n\t"
  "s_waitcnt lgkmcnt(4)\n\t"
  "s_nop 0\n\t"
  "ds_read_b64_tr_b16 v[164:165], v2 offset:8704\n\t"
  "ds_read_b64_tr_b16 v[166:167], v2 offset:12800\n\t"
  "v_mfma_f32_16x16x32_bf16 v[40:43], v[168:171], a[144:147], v[40:43]\n\t"
  "v_mfma_f32_16x16x32_bf16 v[40:43], v[172:175], a[148:151], v[40:43]\n\t"
  "s_waitcnt lgkmcnt(4)\n\t"
  "s_nop 0\n\t"
  "v_mfma_f32_16x16x32_bf16 v[44:47], v[168:171], a[152:155], v[44:47]\n\t"
  "v_mfma_f32_16x16x32_bf16 v[44:47], v[172:175], a[156:159], v[44:47]\n\t"
  "v_mfma_f32_16x16x32_bf16 v[48:51], v[176:179], a[128:131], v[48:51]\n\t"
  "v_mfma_f32_16x16x32_bf16 v[48:51], v[180:183], a[132:135], v[48:51]\n\t"
  "ds_read_b64_tr_b16 v[168:169], v3 offset:512\n\t"
  "ds_read_b64_tr_b16 v[170:171], v3 offset:4608\n\t"
  "v_mfma_f32_16x16x32_bf16 v[52:55], v[176:179], a[136:139], v[52:55]\n\t"
  "v_mfma_f32_16x16x32_bf16 v[52:55], v[180:183], a[140:143], v[52:55]\n\t"
  "ds_read_b64_tr_b16 v[172:173], v3 offset:8704\n\t"
  "ds_read_b64_tr_b16 v[174:175], v3 offset:12800\n\t"
  "v_mfma_f32_16x16x32_bf16 v[56:59], v[176:179], a[144:147], v[56:59]\n\t"
  "v_mfma_f32_16x16x32_bf16 v[56:59], v[180:183], a[148:151], v[56:59]\n\t"
  "v_mfma_f32_16x16x32_bf16 v[60:63], v[176:179], a[152:155], v[60:63]\n\t"
  "v_mfma_f32_16x16x32_bf16 v[60:63], v[180:183], a[156:159], v[60:63]\n\t"
  "s_waitcnt vmcnt(8)\n\t"
  "s_barrier\n\t"
  "v_readfirstlane_b32 s40, v20\n\t"
  "s_nop 0\n\t"
  "s_cmp_ge_u32 s40, 8\n\t"
  "s_cbranch_scc1 L_oko_%=\n\t"
  "s_mov_b32 s41, 0x40000\n\t"
  "L_spino_%=:\n\t"
  "s_sleep 1\n\t"
  "s_nop 0\n\t"
  "global_load_dword v20, v21, s[50:51] sc1\n\t"
  "s_waitcnt vmcnt(0)\n\t"
  "v_readfirstlane_b32 s40, v20\n\t"
  "s_nop 0\n\t"
  "s_cmp_ge_u32 s40, 8\n\t"
  "s_cbranch_scc1 L_oko_%=\n\t"
  "s_sub_u32 s41, s41, 1\n\t"
  "s_cmp_lg_u32 s41, 0\n\t"
  "s_cbranch_scc1 L_spino_%=\n\t"
  "L_oko_%=:\n\t"
  "s_waitcnt lgkmcnt(2)\n\t"
  "s_nop 0\n\t"
  "v_mfma_f32_16x16x32_bf16 v[64:67], v[160:163], a[128:131], v[64:67]\n\t"
  "s_add_u32 m0, s24, 98304\n\t"
  "s_add_u32 s30, s27, 0\n\t"
  "s_nop 0\n\t"
  "buffer_load_dwordx4 v8, s[12:15], s30 offen offset:0 lds\n\t"
  "v_mfma_f32_16x16x32_bf16 v[64:67], v[164:167], a[132:135], v[64:67]\n\t"
  "ds_read_b64_tr_b16 v[176:177], v2 offset:32768\n\t"
  "ds_read_b64_tr_b16 v[178:179], v2 offset:36864\n\t"
  "v_mfma_f32_16x16x32_bf16 v[68:71], v[160:163], a[136:139], v[68:71]\n\t"
  "buffer_load_dwordx4 v9, s[12:15], s30 offen offset:1024 lds\n\t"
  "v_mfma_f32_16x16x32_bf16 v[68:71], v[164:167], a[140:143], v[68:71]\n\t"
  "ds_read_b64_tr_b16 v[180:181], v2 offset:40960\n\t"
  "ds_read_b64_tr_b16 v[182:183], v2 offset:45056\n\t"
  "v_mfma_f32_16x16x32_bf16 v[72:75], v[160:163], a[144:147], v[72:75]\n\t"
  "s_add_u32 m0, s24, 100352\n\t"
  "s_add_u32 s30, s27, 8192\n\t"
  "buffer_load_dwordx4 v8, s[12:15], s30 offen offset:0 lds\n\t"
  "v_mfma_f32_16x16x32_bf16 v[72:75], v[164:167], a[148:151], v[72:75]\n\t"
  "ds_read_b128 a[160:163], v16 offset:32768\n\t"
  "ds_read_b128 a[164:167], v16 offset:33792\n\t"
  "v_mfma_f32_16x16x32_bf16 v[76:79], v[160:163], a[152:155], v[76:79]\n\t"
  "buffer_load_dwordx4 v9, s[12:15], s30 offen offset:1024 lds\n\t"
  "v_mfma_f32_16x16x32_bf16 v[76:79], v[164:167], a[156:159], v[76:79]\n\t"
  "ds_read_b128 a[168:171], v16 offset:34816\n\t"
  "ds_read_b128 a[172:175], v16 offset:35840\n\t"
  "v_mfma_f32_16x16x32_bf16 v[80:83], v[168:171], a[128:131], v[80:83]\n\t"
  "s_waitcnt lgkmcnt(4)\n\t"
  "s_add_u32 m0, s25, 98304\n\t"
  "s_add_u32 s31, s29, 0\n\t"
  "buffer_load_dwordx4 v1, s[16:19], s31 offen offset:0 sc1 lds\n\t"
  "v_mfma_f32_16x16x32_bf16 v[80:83], v[172:175], a[132:135], v[80:83]\n\t"
  "ds_read_b64_tr_b16 v[160:161], v3 offset:32768\n\t"
  "ds_read_b64_tr_b16 v[162:163], v3 offset:36864\n\t"
  "v_mfma_f32_16x16x32_bf16 v[84:87], v[168:171], a[136:139], v[84:87]\n\t"
  "buffer_load_dwordx4 v1, s[16:19], s31 offen offset:1024 sc1 lds\n\t"
  "v_mfma_f32_16x16x32_bf16 v[84:87], v[172:175], a[140:143], v[84:87]\n\t"
  "ds_read_b64_tr_b16 v[164:165], v3 offset:40960\n\t"
  "ds_read_b64_tr_b16 v[166:167], v3 offset:45056\n\t"
  "v_mfma_f32_16x16x32_bf16 v[88:91], v[168:171], a[144:147], v[88:91]\n\t"
  "s_add_u32 m0, s25, 100352\n\t"
  "s_add_u32 s31, s29, 188416\n\t"
  "buffer_load_dwordx4 v1, s[16:19], s31 offen offset:0 sc1 lds\n\t"
  "v_mfma_f32_16x16x32_bf16 v[88:91], v[172:175], a[148:151], v[88:91]\n\t"
  "ds_read_b128 a[176:179], v16 offset:36864\n\t"
  "ds_read_b128 a[180:183], v16 offset:37888\n\t"
  "v_mfma_f32_16x16x32_bf16 v[92:95], v[168:171], a[152:155], v[92:95]\n\t"
  "buffer_load_dwordx4 v1, s[16:19], s31 offen offset:1024 sc1 lds\n\t"
  "v_mfma_f32_16x16x32_bf16 v[92:95], v[172:175], a[156:159], v[92:95]\n\t"
  "s_waitcnt lgkmcnt(4)\n\t"
  "s_nop 0\n\t"
  "ds_read_b128 a[184:187], v16 offset:38912\n\t"
  "ds_read_b128 a[188:191], v16 offset:39936\n\t"
  "v_mfma_f32_16x16x32_bf16 v[32:35], v[176:179], a[160:163], v[32:35]\n\t"
  "v_mfma_f32_16x16x32_bf16 v[32:35], v[180:183], a[164:167], v[32:35]\n\t"
  "ds_read_b64_tr_b16 v[168:169], v2 offset:33280\n\t"
  "ds_read_b64_tr_b16 v[170:171], v2 offset:37376\n\t"
  "v_mfma_f32_16x16x32_bf16 v[36:39], v[176:179], a[168:171], v[36:39]\n\t"
  "v_mfma_f32_16x16x32_bf16 v[36:39], v[180:183], a[172:175], v[36:39]\n\t"
  "s_waitcnt lgkmcnt(4)\n\t"
  "s_nop 0\n\t"
  "ds_read_b64_tr_b16 v[172:173], v2 offset:41472\n\t"
  "ds_read_b64_tr_b16 v[174:175], v2 offset:45568\n\t"
  "v_mfma_f32_16x16x32_bf16 v[40:43], v[176:179], a[176:179], v[40:43]\n\t"
  "v_mfma_f32_16x16x32_bf16 v[40:43], v[180:183], a[180:183], v[40:43]\n\t"
  "s_waitcnt lgkmcnt(4)\n\t"
  "s_nop 0\n\t"
  "v_mfma_f32_16x16x32_bf16 v[44:47], v[176:179], a[184:187], v[44:47]\n\t"
  "v_mfma_f32_16x16x32_bf16 v[44:47], v[180:183], a[188:191], v[44:47]\n\t"
  "v_mfma_f32_16x16x32_bf16 v[48:51], v[160:163], a[160:163], v[48:51]\n\t"
  "v_mfma_f32_16x16x32_bf16 v[48:51], v[164:167], a[164:167], v[48:51]\n\t"
  "ds_read_b64_tr_b16 v[176:177], v3 offset:33280\n\t"
  "ds_read_b64_tr_b16 v[178:179], v3 offset:37376\n\t"
  "v_mfma_f32_16x16x32_bf16 v[52:55], v[160:163], a[168:171], v[52:55]\n\t"
  "v_mfma_f32_16x16x32_bf16 v[52:55], v[164:167], a[172:175], v[52:55]\n\t"
  "ds_read_b64_tr_b16 v[180:181], v3 offset:41472\n\t"
  "ds_read_b64_tr_b16 v[182:183], v3 offset:45568\n\t"
  "v_mfma_f32_16x16x32_bf16 v[56:59], v[160:163], a[176:179], v[56:59]\n\t"
  "v_mfma_f32_16x16x32_bf16 v[56:59], v[164:167], a[180:183], v[56:59]\n\t"
  "v_mfma_f32_16x16x32_bf16 v[60:63], v[160:163], a[184:187], v[60:63]\n\t"
  "v_mfma_f32_16x16x32_bf16 v[60:63], v[164:167], a[188:191], v[60:63]\n\t"
  "s_waitcnt vmcnt(8)\n\t"
  "s_barrier\n\t"
  "s_waitcnt lgkmcnt(2)\n\t"
  "s_nop 0\n\t"
  "v_mfma_f32_16x16x32_bf16 v[64:67], v[168:171], a[160:163], v[64:67]\n\t"
  "s_add_u32 m0, s24, 0\n\t"
  "s_add_u32 s30, s27, 65536\n\t"
  "s_nop 0\n\t"
  "buffer_load_dwordx4 v8, s[12:15], s30 offen offset:0 lds\n\t"
  "v_mfma_f32_16x16x32_bf16 v[64:67], v[172:175], a[164:167], v[64:67]\n\t"
  "ds_read_b64_tr_b16 v[160:161], v4 offset:0\n\t"
  "ds_read_b64_tr_b16 v[162:163], v4 offset:4096\n\t"
  "v_mfma_f32_16x16x32_bf16 v[68:71], v[168:171], a[168:171], v[68:71]\n\t"
  "buffer_load_dwordx4 v9, s[12:15], s30 offen offset:1024 lds\n\t"
  "v_mfma_f32_16x16x32_bf16 v[68:71], v[172:175], a[172:175], v[68:71]\n\t"
  "ds_read_b64_tr_b16 v[164:165], v4 offset:8192\n\t"
  "ds_read_b64_tr_b16 v[166:167], v4 offset:12288\n\t"
  "v_mfma_f32_16x16x32_bf16 v[72:75], v[168:171], a[176:179], v[72:75]\n\t"
  "s_add_u32 m0, s24, 2048\n\t"
  "s_add_u32 s30, s27, 73728\n\t"
  "buffer_load_dwordx4 v8, s[12:15], s30 offen offset:0 lds\n\t"
  "v_mfma_f32_16x16x32_bf16 v[72:75], v[172:175], a[180:183], v[72:75]\n\t"
  "ds_read_b128 a[128:131], v17 offset:0\n\t"
  "ds_read_b128 a[132:135], v17 offset:1024\n\t"
  "v_mfma_f32_16x16x32_bf16 v[76:79], v[168:171], a[184:187], v[76:79]\n\t"
  "buffer_load_dwordx4 v9, s[12:15], s30 offen offset:1024 lds\n\t"
  "v_mfma_f32_16x16x32_bf16 v[76:79], v[172:175], a[188:191], v[76:79]\n\t"
  "ds_read_b128 a[136:139], v17 offset:2048\n\t"
  "ds_read_b128 a[140:143], v17 offset:3072\n\t"
  "v_mfma_f32_16x16x32_bf16 v[80:83], v[176:179], a[160:163], v[80:83]\n\t"
  "s_waitcnt lgkmcnt(4)\n\t"
  "s_add_u32 m0, s25, 0\n\t"
  "s_add_u32 s31, s29, 2048\n\t"
  "buffer_load_dwordx4 v1, s[16:19], s31 offen offset:0 sc1 lds\n\t"
  "v_mfma_f32_16x16x32_bf16 v[80:83], v[180:183], a[164:167], v[80:83]\n\t"
  "ds_read_b64_tr_b16 v[168:169], v5 offset:0\n\t"
  "ds_read_b64_tr_b16 v[170:171], v5 offset:4096\n\t"
  "v_mfma_f32_16x16x32_bf16 v[84:87], v[176:179], a[168:171], v[84:87]\n\t"
  "buffer_load_dwordx4 v1, s[16:19], s31 offen offset:1024 sc1 lds\n\t"
  "v_mfma_f32_16x16x32_bf16 v[84:87], v[180:183], a[172:175], v[84:87]\n\t"
  "ds_read_b64_tr_b16 v[172:173], v5 offset:8192\n\t"
  "ds_read_b64_tr_b16 v[174:175], v5 offset:12288\n\t"
  "v_mfma_f32_16x16x32_bf16 v[88:91], v[176:179], a[176:179], v[88:91]\n\t"
  "s_add_u32 m0, s25, 2048\n\t"
  "s_add_u32 s31, s29, 190464\n\t"
  "buffer_load_dwordx4 v1, s[16:19], s31 offen offset:0 sc1 lds\n\t"
  "v_mfma_f32_16x16x32_bf16 v[88:91], v[180:183], a[180:183], v[88:91]\n\t"
  "ds_read_b128 a[144:147], v17 offset:4096\n\t"
  "ds_read_b128 a[148:151], v17 offset:5120\n\t"
  "v_mfma_f32_16x16x32_bf16 v[92:95], v[176:179], a[184:187], v[92:95]\n\t"
  "buffer_load_dwordx4 v1, s[16:19], s31 offen offset:1024 sc1 lds\n\t"
  "v_mfma_f32_16x16x32_bf16 v[92:95], v[180:183], a[188:191], v[92:95]\n\t"
  "s_waitcnt lgkmcnt(4)\n\t"
  "s_nop 0\n\t"
  "ds_read_b128 a[152:155], v17 offset:6144\n\t"
  "ds_read_b128 a[156:159], v17 offset:7168\n\t"
  "v_mfma_f32_16x16x32_bf16 v[32:35], v[160:163], a[128:131], v[32:35]\n\t"
  "v_mfma_f32_16x16x32_bf16 v[32:35], v[164:167], a[132:135], v[32:35]\n\t"
  "ds_read_b64_tr_b16 v[176:177], v4 offset:512\n\t"
  "ds_read_b64_tr_b16 v[178:179], v4 offset:4608\n\t"
  "v_mfma_f32_16x16x32_bf16 v[36:39], v[160:163], a[136:139], v[36:39]\n\t"
  "v_mfma_f32_16x16x32_bf16 v[36:39], v[164:167], a[140:143], v[36:39]\n\t"
  "s_waitcnt lgkmcnt(4)\n\t"
  "s_nop 0\n\t"
  "ds_read_b64_tr_b16 v[180:181], v4 offset:8704\n\t"
  "ds_read_b64_tr_b16 v[182:183], v4 offset:12800\n\t"
  "v_mfma_f32_16x16x32_bf16 v[40:43], v[160:163], a[144:147], v[40:43]\n\t"
  "v_mfma_f32_16x16x32_bf16 v[40:43], v[164:167], a[148:151], v[40:43]\n\t"
  "s_waitcnt lgkmcnt(4)\n\t"
  "s_nop 0\n\t"
  "v_mfma_f32_16x16x32_bf16 v[44:47], v[160:163], a[152:155], v[44:47]\n\t"
  "v_mfma_f32_16x16x32_bf16 v[44:47], v[164:167], a[156:159], v[44:47]\n\t"
  "v_mfma_f32_16x16x32_bf16 v[48:51], v[168:171], a[128:131], v[48:51]\n\t"
  "v_mfma_f32_16x16x32_bf16 v[48:51], v[172:175], a[132:135], v[48:51]\n\t"
  "ds_read_b64_tr_b16 v[160:161], v5 offset:512\n\t"
  "ds_read_b64_tr_b16 v[162:163], v5 offset:4608\n\t"
  "v_mfma_f32_16x16x32_bf16 v[52:55], v[168:171], a[136:139], v[52:55]\n\t"
  "v_mfma_f32_16x16x32_bf16 v[52:55], v[172:175], a[140:143], v[52:55]\n\t"
  "ds_read_b64_tr_b16 v[164:165], v5 offset:8704\n\t"
  "ds_read_b64_tr_b16 v[166:167], v5 offset:12800\n\t"
  "v_mfma_f32_16x16x32_bf16 v[56:59], v[168:171], a[144:147], v[56:59]\n\t"
  "v_mfma_f32_16x16x32_bf16 v[56:59], v[172:175], a[148:151], v[56:59]\n\t"
  "v_mfma_f32_16x16x32_bf16 v[60:63], v[168:171], a[152:155], v[60:63]\n\t"
  "v_mfma_f32_16x16x32_bf16 v[60:63], v[172:175], a[156:159], v[60:63]\n\t"
  "s_waitcnt vmcnt(8)\n\t"
  "s_barrier\n\t"
  "s_waitcnt lgkmcnt(2)\n\t"
  "s_nop 0\n\t"
  "v_mfma_f32_16x16x32_bf16 v[64:67], v[176:179], a[128:131], v[64:67]\n\t"
  "s_add_u32 m0, s24, 32768\n\t"
  "s_add_u32 s30, s27, 131072\n\t"
  "buffer_load_dwordx4 v8, s[12:15], s30 offen offset:0 lds\n\t"
  "v_mfma_f32_16x16x32_bf16 v[64:67], v[180:183], a[132:135], v[64:67]\n\t"
  "ds_read_b64_tr_b16 v[168:169], v4 offset:32768\n\t"
  "ds_read_b64_tr_b16 v[170:171], v4 offset:36864\n\t"
  "v_mfma_f32_16x16x32_bf16 v[68:71], v[176:179], a[136:139], v[68:71]\n\t"
  "buffer_load_dwordx4 v9, s[12:15], s30 offen offset:1024 lds\n\t"
  "v_mfma_f32_16x16x32_bf16 v[68:71], v[180:183], a[140:143], v[68:71]\n\t"
  "ds_read_b64_tr_b16 v[172:173], v4 offset:40960\n\t"
  "ds_read_b64_tr_b16 v[174:175], v4 offset:45056\n\t"
  "v_mfma_f32_16x16x32_bf16 v[72:75], v[176:179], a[144:147], v[72:75]\n\t"
  "s_add_u32 m0, s24, 34816\n\t"
  "s_add_u32 s30, s27, 139264\n\t"
  "buffer_load_dwordx4 v8, s[12:15], s30 offen offset:0 lds\n\t"
  "v_mfma_f32_16x16x32_bf16 v[72:75], v[180:183], a[148:151], v[72:75]\n\t"
  "ds_read_b128 a[160:163], v17 offset:32768\n\t"
  "ds_read_b128 a[164:167], v17 offset:33792\n\t"
  "v_mfma_f32_16x16x32_bf16 v[76:79], v[176:179], a[152:155], v[76:79]\n\t"
  "buffer_load_dwordx4 v9, s[12:15], s30 offen offset:1024 lds\n\t"
  "v_mfma_f32_16x16x32_bf16 v[76:79], v[180:183], a[156:159], v[76:79]\n\t"
  "ds_read_b128 a[168:171], v17 offset:34816\n\t"
  "ds_read_b128 a[172:175], v17 offset:35840\n\t"
  "v_mfma_f32_16x16x32_bf16 v[80:83], v[160:163], a[128:131], v[80:83]\n\t"
  "s_waitcnt lgkmcnt(4)\n\t"
  "s_add_u32 m0, s25, 32768\n\t"
  "s_add_u32 s31, s29, 4096\n\t"
  "s_nop 0\n\t"
  "buffer_load_dwordx4 v1, s[16:19], s31 offen offset:0 sc1 lds\n\t"
  "v_mfma_f32_16x16x32_bf16 v[80:83], v[164:167], a[132:135], v[80:83]\n\t"
  "ds_read_b64_tr_b16 v[176:177], v5 offset:32768\n\t"
  "ds_read_b64_tr_b16 v[178:179], v5 offset:36864\n\t"
  "v_mfma_f32_16x16x32_bf16 v[84:87], v[160:163], a[136:139], v[84:87]\n\t"
  "buffer_load_dwordx4 v1, s[16:19], s31 offen offset:1024 sc1 lds\n\t"
  "v_mfma_f32_16x16x32_bf16 v[84:87], v[164:167], a[140:143], v[84:87]\n\t"
  "ds_read_b64_tr_b16 v[180:181], v5 offset:40960\n\t"
  "ds_read_b64_tr_b16 v[182:183], v5 offset:45056\n\t"
  "v_mfma_f32_16x16x32_bf16 v[88:91], v[160:163], a[144:147], v[88:91]\n\t"
  "s_add_u32 m0, s25, 34816\n\t"
  "s_add_u32 s31, s29, 192512\n\t"
  "buffer_load_dwordx4 v1, s[16:19], s31 offen offset:0 sc1 lds\n\t"
  "v_mfma_f32_16x16x32_bf16 v[88:91], v[164:167], a[148:151], v[88:91]\n\t"
  "ds_read_b128 a[176:179], v17 offset:36864\n\t"
  "ds_read_b128 a[180:183], v17 offset:37888\n\t"
  "v_mfma_f32_16x16x32_bf16 v[92:95], v[160:163], a[152:155], v[92:95]\n\t"
  "buffer_load_dwordx4 v1, s[16:19], s31 offen offset:1024 sc1 lds\n\t"
  "v_mfma_f32_16x16x32_bf16 v[92:95], v[164:167], a[156:159], v[92:95]\n\t"
  "s_waitcnt lgkmcnt(4)\n\t"
  "s_nop 0\n\t"
  "ds_read_b128 a[184:187], v17 offset:38912\n\t"
  "ds_read_b128 a[188:191], v17 offset:39936\n\t"
  "v_mfma_f32_16x16x32_bf16 v[96:99], v[168:171], a[160:163], 0\n\t"
  "v_mfma_f32_16x16x32_bf16 v[96:99], v[172:175], a[164:167], v[96:99]\n\t"
  "ds_read_b64_tr_b16 v[160:161], v4 offset:33280\n\t"
  "ds_read_b64_tr_b16 v[162:163], v4 offset:37376\n\t"
  "v_mfma_f32_16x16x32_bf16 v[100:103], v[168:171], a[168:171], 0\n\t"
  "v_mfma_f32_16x16x32_bf16 v[100:103], v[172:175], a[172:175], v[100:103]\n\t"
  "s_waitcnt lgkmcnt(4)\n\t"
  "s_nop 0\n\t"
  "ds_read_b64_tr_b16 v[164:165], v4 offset:41472\n\t"
  "ds_read_b64_tr_b16 v[166:167], v4 offset:45568\n\t"
  "v_mfma_f32_16x16x32_bf16 v[104:107], v[168:171], a[176:179], 0\n\t"
  "v_mfma_f32_16x16x32_bf16 v[104:107], v[172:175], a[180:183], v[104:107]\n\t"
  "s_waitcnt lgkmcnt(4)\n\t"
  "s_nop 0\n\t"
  "v_mfma_f32_16x16x32_bf16 v[108:111], v[168:171], a[184:187], 0\n\t"
  "v_mfma_f32_16x16x32_bf16 v[108:111], v[172:175], a[188:191], v[108:111]\n\t"
  "v_mfma_f32_16x16x32_bf16 v[112:115], v[176:179], a[160:163], 0\n\t"
  "v_mfma_f32_16x16x32_bf16 v[112:115], v[180:183], a[164:167], v[112:115]\n\t"
  "ds_read_b64_tr_b16 v[168:169], v5 offset:33280\n\t"
  "ds_read_b64_tr_b16 v[170:171], v5 offset:37376\n\t"
  "v_mfma_f32_16x16x32_bf16 v[116:119], v[176:179], a[168:171], 0\n\t"
  "v_mfma_f32_16x16x32_bf16 v[116:119], v[180:183], a[172:175], v[116:119]\n\t"
  "ds_read_b64_tr_b16 v[172:173], v5 offset:41472\n\t"
  "ds_read_b64_tr_b16 v[174:175], v5 offset:45568\n\t"
  "v_mfma_f32_16x16x32_bf16 v[120:123], v[176:179], a[176:179], 0\n\t"
  "v_mfma_f32_16x16x32_bf16 v[120:123], v[180:183], a[180:183], v[120:123]\n\t"
  "v_mfma_f32_16x16x32_bf16 v[124:127], v[176:179], a[184:187], 0\n\t"
  "v_mfma_f32_16x16x32_bf16 v[124:127], v[180:183], a[188:191], v[124:127]\n\t"
  "s_waitcnt vmcnt(8)\n\t"
  "s_barrier\n\t"
  "s_waitcnt lgkmcnt(2)\n\t"
  "s_nop 0\n\t"
  "v_mfma_f32_16x16x32_bf16 v[128:131], v[160:163], a[160:163], 0\n\t"
  "s_add_u32 m0, s24, 65536\n\t"
  "s_add_u32 s30, s27, 196608\n\t"
  "buffer_load_dwordx4 v8, s[12:15], s30 offen offset:0 lds\n\t"
  "v_mfma_f32_16x16x32_bf16 v[128:131], v[164:167], a[164:167], v[128:131]\n\t"
  "ds_read_b64_tr_b16 v[176:177], v2 offset:0\n\t"
  "ds_read_b64_tr_b16 v[178:179], v2 offset:4096\n\t"
  "v_mfma_f32_16x16x32_bf16 v[132:135], v[160:163], a[168:171], 0\n\t"
  "buffer_load_dwordx4 v9, s[12:15], s30 offen offset:1024 lds\n\t"
  "v_mfma_f32_16x16x32_bf16 v[132:135], v[164:167], a[172:175], v[132:135]\n\t"
  "ds_read_b64_tr_b16 v[180:181], v2 offset:8192\n\t"
  "ds_read_b64_tr_b16 v[182:183], v2 offset:12288\n\t"
  "v_mfma_f32_16x16x32_bf16 v[136:139], v[160:163], a[176:179], 0\n\t"
  "s_add_u32 m0, s24, 67584\n\t"
  "s_add_u32 s30, s27, 204800\n\t"
  "buffer_load_dwordx4 v8, s[12:15], s30 offen offset:0 lds\n\t"
  "v_mfma_f32_16x16x32_bf16 v[136:139], v[164:167], a[180:183], v[136:139]\n\t"
  "ds_read_b128 a[128:131], v16 offset:0\n\t"
  "ds_read_b128 a[132:135], v16 offset:1024\n\t"
  "v_mfma_f32_16x16x32_bf16 v[140:143], v[160:163], a[184:187], 0\n\t"
  "buffer_load_dwordx4 v9, s[12:15], s30 offen offset:1024 lds\n\t"
  "v_mfma_f32_16x16x32_bf16 v[140:143], v[164:167], a[188:191], v[140:143]\n\t"
  "ds_read_b128 a[136:139], v16 offset:2048\n\t"
  "ds_read_b128 a[140:143], v16 offset:3072\n\t"
  "v_mfma_f32_16x16x32_bf16 v[144:147], v[168:171], a[160:163], 0\n\t"
  "s_waitcnt lgkmcnt(4)\n\t"
  "s_add_u32 m0, s25, 65536\n\t"
  "s_add_u32 s31, s29, 6144\n\t"
  "s_nop 0\n\t"
  "buffer_load_dwordx4 v1, s[16:19], s31 offen offset:0 sc1 lds\n\t"
  "v_mfma_f32_16x16x32_bf16 v[144:147], v[172:175], a[164:167], v[144:147]\n\t"
  "ds_read_b64_tr_b16 v[160:161], v3 offset:0\n\t"
  "ds_read_b64_tr_b16 v[162:163], v3 offset:4096\n\t"
  "v_mfma_f32_16x16x32_bf16 v[148:151], v[168:171], a[168:171], 0\n\t"
  "buffer_load_dwordx4 v1, s[16:19], s31 offen offset:1024 sc1 lds\n\t"
  "v_mfma_f32_16x16x32_bf16 v[148:151], v[172:175], a[172:175], v[148:151]\n\t"
  "ds_read_b64_tr_b16 v[164:165], v3 offset:8192\n\t"
  "ds_read_b64_tr_b16 v[166:167], v3 offset:12288\n\t"
  "v_mfma_f32_16x16x32_bf16 v[152:155], v[168:171], a[176:179], 0\n\t"
  "s_add_u32 m0, s25, 67584\n\t"
  "s_add_u32 s31, s29, 194560\n\t"
  "buffer_load_dwordx4 v1, s[16:19], s31 offen offset:0 sc1 lds\n\t"
  "v_mfma_f32_16x16x32_bf16 v[152:155], v[172:175], a[180:183], v[152:155]\n\t"
  "ds_read_b128 a[144:147], v16 offset:4096\n\t"
  "ds_read_b128 a[148:151], v16 offset:5120\n\t"
  "v_mfma_f32_16x16x32_bf16 v[156:159], v[168:171], a[184:187], 0\n\t"
  "buffer_load_dwordx4 v1, s[16:19], s31 offen offset:1024 sc1 lds\n\t"
  "v_mfma_f32_16x16x32_bf16 v[156:159], v[172:175], a[188:191], v[156:159]\n\t"
  "s_waitcnt lgkmcnt(4)\n\t"
  "s_nop 0\n\t"
  "ds_read_b128 a[152:155], v16 offset:6144\n\t"
  "ds_read_b128 a[156:159], v16 offset:7168\n\t"
  "v_mfma_f32_16x16x32_bf16 v[96:99], v[176:179], a[128:131], v[96:99]\n\t"
  "v_cvt_pk_bf16_f32 v32, v32, v33\n\t"
  "v_cvt_pk_bf16_f32 v33, v34, v35\n\t"
  "v_mfma_f32_16x16x32_bf16 v[96:99], v[180:183], a[132:135], v[96:99]\n\t"
  "ds_read_b64_tr_b16 v[168:169], v2 offset:512\n\t"
  "ds_read_b64_tr_b16 v[170:171], v2 offset:4608\n\t"
  "v_mfma_f32_16x16x32_bf16 v[100:103], v[176:179], a[136:139], v[100:103]\n\t"
  "v_cvt_pk_bf16_f32 v34, v48, v49\n\t"
  "v_cvt_pk_bf16_f32 v35, v50, v51\n\t"
  "v_mfma_f32_16x16x32_bf16 v[100:103], v[180:183], a[140:143], v[100:103]\n\t"
  "s_waitcnt lgkmcnt(4)\n\t"
  "s_nop 0\n\t"
  "ds_read_b64_tr_b16 v[172:173], v2 offset:8704\n\t"
  "ds_read_b64_tr_b16 v[174:175], v2 offset:12800\n\t"
  "v_mfma_f32_16x16x32_bf16 v[104:107], v[176:179], a[144:147], v[104:107]\n\t"
  "v_cvt_pk_bf16_f32 v64, v64, v65\n\t"
  "v_cvt_pk_bf16_f32 v65, v66, v67\n\t"
  "v_mfma_f32_16x16x32_bf16 v[104:107], v[180:183], a[148:151], v[104:107]\n\t"
  "s_waitcnt lgkmcnt(4)\n\t"
  "s_nop 0\n\t"
  "v_cvt_pk_bf16_f32 v66, v80, v81\n\t"
  "v_cvt_pk_bf16_f32 v67, v82, v83\n\t"
  "v_mfma_f32_16x16x32_bf16 v[108:111], v[176:179], a[152:155], v[108:111]\n\t"
  "v_cvt_pk_bf16_f32 v36, v36, v37\n\t"
  "v_cvt_pk_bf16_f32 v37, v38, v39\n\t"
  "v_mfma_f32_16x16x32_bf16 v[108:111], v[180:183], a[156:159], v[108:111]\n\t"
  "v_cvt_pk_bf16_f32 v38, v52, v53\n\t"
  "v_cvt_pk_bf16_f32 v39, v54, v55\n\t"
  "v_mfma_f32_16x16x32_bf16 v[112:115], v[160:163], a[128:131], v[112:115]\n\t"
  "v_cvt_pk_bf16_f32 v68, v68, v69\n\t"
  "v_cvt_pk_bf16_f32 v69, v70, v71\n\t"
  "v_mfma_f32_16x16x32_bf16 v[112:115], v[164:167], a[132:135], v[112:115]\n\t"
  "ds_read_b64_tr_b16 v[176:177], v3 offset:512\n\t"
  "ds_read_b64_tr_b16 v[178:179], v3 offset:4608\n\t"
  "v_mfma_f32_16x16x32_bf16 v[116:119], v[160:163], a[136:139], v[116:119]\n\t"
  "v_cvt_pk_bf16_f32 v70, v84, v85\n\t"
  "v_cvt_pk_bf16_f32 v71, v86, v87\n\t"
  "v_mfma_f32_16x16x32_bf16 v[116:119], v[164:167], a[140:143], v[116:119]\n\t"
  "ds_read_b64_tr_b16 v[180:181], v3 offset:8704\n\t"
  "ds_read_b64_tr_b16 v[182:183], v3 offset:12800\n\t"
  "v_mfma_f32_16x16x32_bf16 v[120:123], v[160:163], a[144:147], v[120:123]\n\t"
  "v_cvt_pk_bf16_f32 v40, v40, v41\n\t"
  "v_cvt_pk_bf16_f32 v41, v42, v43\n\t"
  "v_mfma_f32_16x16x32_bf16 v[120:123], v[164:167], a[148:151], v[120:123]\n\t"
  "v_cvt_pk_bf16_f32 v42, v56, v57\n\t"
  "v_cvt_pk_bf16_f32 v43, v58, v59\n\t"
  "v_mfma_f32_16x16x32_bf16 v[124:127], v[160:163], a[152:155], v[124:127]\n\t"
  "v_cvt_pk_bf16_f32 v72, v72, v73\n\t"
  "v_cvt_pk_bf16_f32 v73, v74, v75\n\t"
  "v_mfma_f32_16x16x32_bf16 v[124:127], v[164:167], a[156:159], v[124:127]\n\t"
  "v_cvt_pk_bf16_f32 v74, v88, v89\n\t"
  "v_cvt_pk_bf16_f32 v75, v90, v91\n\t"
  "s_waitcnt vmcnt(8)\n\t"
  "s_barrier\n\t"
  "s_waitcnt lgkmcnt(2)\n\t"
  "s_nop 0\n\t"
  "v_mfma_f32_16x16x32_bf16 v[128:131], v[168:171], a[128:131], v[128:131]\n\t"
  "s_add_u32 m0, s24, 98304\n\t"
  "s_add_u32 s30, s27, 262144\n\t"
  "buffer_load_dwordx4 v8, s[12:15], s30 offen offset:0 lds\n\t"
  "v_cvt_pk_bf16_f32 v44, v44, v45\n\t"
  "v_mfma_f32_16x16x32_bf16 v[128:131], v[172:175], a[132:135], v[128:131]\n\t"
  "ds_read_b64_tr_b16 v[160:161], v2 offset:32768\n\t"
  "ds_read_b64_tr_b16 v[162:163], v2 offset:36864\n\t"
  "v_mfma_f32_16x16x32_bf16 v[132:135], v[168:171], a[136:139], v[132:135]\n\t"
  "buffer_load_dwordx4 v9, s[12:15], s30 offen offset:1024 lds\n\t"
  "v_cvt_pk_bf16_f32 v45, v46, v47\n\t"
  "v_mfma_f32_16x16x32_bf16 v[132:135], v[172:175], a[140:143], v[132:135]\n\t"
  "ds_read_b64_tr_b16 v[164:165], v2 offset:40960\n\t"
  "ds_read_b64_tr_b16 v[166:167], v2 offset:45056\n\t"
  "v_mfma_f32_16x16x32_bf16 v[136:139], v[168:171], a[144:147], v[136:139]\n\t"
  "s_add_u32 m0, s24, 100352\n\t"
  "s_add_u32 s30, s27, 270336\n\t"
  "buffer_load_dwordx4 v8, s[12:15], s30 offen offset:0 lds\n\t"
  "v_cvt_pk_bf16_f32 v46, v60, v61\n\t"
  "v_mfma_f32_16x16x32_bf16 v[136:139], v[172:175], a[148:151], v[136:139]\n\t"
  "ds_read_b128 a[160:163], v16 offset:32768\n\t"
  "ds_read_b128 a[164:167], v16 offset:33792\n\t"
  "v_mfma_f32_16x16x32_bf16 v[140:143], v[168:171], a[152:155], v[140:143]\n\t"
  "buffer_load_dwordx4 v9, s[12:15], s30 offen offset:1024 lds\n\t"
  "v_cvt_pk_bf16_f32 v47, v62, v63\n\t"
  "v_mfma_f32_16x16x32_bf16 v[140:143], v[172:175], a[156:159], v[140:143]\n\t"
  "ds_read_b128 a[168:171], v16 offset:34816\n\t"
  "ds_read_b128 a[172:175], v16 offset:35840\n\t"
  "v_mfma_f32_16x16x32_bf16 v[144:147], v[176:179], a[128:131], v[144:147]\n\t"
  "s_waitcnt lgkmcnt(4)\n\t"
  "s_add_u32 m0, s25, 98304\n\t"
  "s_add_u32 s31, s29, 8192\n\t"
  "s_nop 0\n\t"
  "buffer_load_dwordx4 v1, s[16:19], s31 offen offset:0 sc1 lds\n\t"
  "v_cvt_pk_bf16_f32 v76, v76, v77\n\t"
  "v_mfma_f32_16x16x32_bf16 v[144:147], v[180:183], a[132:135], v[144:147]\n\t"
  "ds_read_b64_tr_b16 v[168:169], v3 offset:32768\n\t"
  "ds_read_b64_tr_b16 v[170:171], v3 offset:36864\n\t"
  "v_mfma_f32_16x16x32_bf16 v[148:151], v[176:179], a[136:139], v[148:151]\n\t"
  "buffer_load_dwordx4 v1, s[16:19], s31 offen offset:1024 sc1 lds\n\t"
  "v_cvt_pk_bf16_f32 v77, v78, v79\n\t"
  "v_mfma_f32_16x16x32_bf16 v[148:151], v[180:183], a[140:143], v[148:151]\n\t"
  "ds_read_b64_tr_b16 v[172:173], v3 offset:40960\n\t"
  "ds_read_b64_tr_b16 v[174:175], v3 offset:45056\n\t"
  "v_mfma_f32_16x16x32_bf16 v[152:155], v[176:179], a[144:147], v[152:155]\n\t"
  "s_add_u32 m0, s25, 100352\n\t"
  "s_add_u32 s31, s29, 196608\n\t"
  "buffer_load_dwordx4 v1, s[16:19], s31 offen offset:0 sc1 lds\n\t"
  "v_cvt_pk_bf16_f32 v78, v92, v93\n\t"
  "v_mfma_f32_16x16x32_bf16 v[152:155], v[180:183], a[148:151], v[152:155]\n\t"
  "ds_read_b128 a[176:179], v16 offset:36864\n\t"
  "ds_read_b128 a[180:183], v16 offset:37888\n\t"
  "v_mfma_f32_16x16x32_bf16 v[156:159], v[176:179], a[152:155], v[156:159]\n\t"
  "buffer_load_dwordx4 v1, s[16:19], s31 offen offset:1024 sc1 lds\n\t"
  "v_cvt_pk_bf16_f32 v79, v94, v95\n\t"
  "v_mfma_f32_16x16x32_bf16 v[156:159], v[180:183], a[156:159], v[156:159]\n\t"
  "ds_read_b128 a[184:187], v16 offset:38912\n\t"
  "ds_read_b128 a[188:191], v16 offset:39936\n\t"
  "s_waitcnt lgkmcnt(0)\n\t"
  "s_add_u32 s30, s37, 0\n\t"
  "s_cmp_eq_u32 s30, %11\n\t"
  "s_cbranch_scc1 L_owna0_%=\n\t"
  "s_add_u32 s30, s36, 0\n\t"
  "s_nop 0\n\t"
  "buffer_store_dwordx4 v[32:35], v1, s[20:23], s30 offen offset:0 sc1\n\t"
  "buffer_store_dwordx4 v[64:67], v1, s[20:23], s30 offen offset:1024 sc1\n\t"
  "s_add_u32 s30, s36, 4096\n\t"
  "buffer_store_dwordx4 v[36:39], v1, s[20:23], s30 offen offset:0 sc1\n\t"
  "buffer_store_dwordx4 v[68:71], v1, s[20:23], s30 offen offset:1024 sc1\n\t"
  "s_branch L_enda0_%=\n\t"
  "L_owna0_%=:\n\t"
  "s_nop 0\n\t"
  "ds_write_b128 v14, v[32:35] offset:0\n\t"
  "ds_write_b128 v14, v[64:67] offset:1024\n\t"
  "ds_write_b128 v14, v[36:39] offset:4096\n\t"
  "ds_write_b128 v14, v[68:71] offset:5120\n\t"
  "L_enda0_%=:\n\t"
  "s_add_u32 s30, s37, 1\n\t"
  "s_cmp_eq_u32 s30, %11\n\t"
  "s_cbranch_scc1 L_owna1_%=\n\t"
  "s_add_u32 s30, s36, 65536\n\t"
  "s_nop 0\n\t"
  "buffer_store_dwordx4 v[40:43], v1, s[20:23], s30 offen offset:0 sc1\n\t"
  "buffer_store_dwordx4 v[72:75], v1, s[20:23], s30 offen offset:1024 sc1\n\t"
  "s_add_u32 s30, s36, 69632\n\t"
  "buffer_store_dwordx4 v[44:47], v1, s[20:23], s30 offen offset:0 sc1\n\t"
  "buffer_store_dwordx4 v[76:79], v1, s[20:23], s30 offen offset:1024 sc1\n\t"
  "s_branch L_enda1_%=\n\t"
  "L_owna1_%=:\n\t"
  "s_nop 0\n\t"
  "ds_write_b128 v14, v[40:43] offset:0\n\t"
  "ds_write_b128 v14, v[72:75] offset:1024\n\t"
  "ds_write_b128 v14, v[44:47] offset:4096\n\t"
  "ds_write_b128 v14, v[76:79] offset:5120\n\t"
  "L_enda1_%=:\n\t"
  "s_waitcnt lgkmcnt(0)\n\t"
  "s_nop 0\n\t"
  "v_mfma_f32_16x16x32_bf16 v[96:99], v[160:163], a[160:163], v[96:99]\n\t"
  "v_mfma_f32_16x16x32_bf16 v[96:99], v[164:167], a[164:167], v[96:99]\n\t"
  "ds_read_b64_tr_b16 v[176:177], v2 offset:33280\n\t"
  "ds_read_b64_tr_b16 v[178:179], v2 offset:37376\n\t"
  "v_mfma_f32_16x16x32_bf16 v[100:103], v[160:163], a[168:171], v[100:103]\n\t"
  "v_mfma_f32_16x16x32_bf16 v[100:103], v[164:167], a[172:175], v[100:103]\n\t"
  "ds_read_b64_tr_b16 v[180:181], v2 offset:41472\n\t"
  "ds_read_b64_tr_b16 v[182:183], v2 offset:45568\n\t"
  "v_mfma_f32_16x16x32_bf16 v[104:107], v[160:163], a[176:179], v[104:107]\n\t"
  "v_mfma_f32_16x16x32_bf16 v[104:107], v[164:167], a[180:183], v[104:107]\n\t"
  "v_mfma_f32_16x16x32_bf16 v[108:111], v[160:163], a[184:187], v[108:111]\n\t"
  "v_mfma_f32_16x16x32_bf16 v[108:111], v[164:167], a[188:191], v[108:111]\n\t"
  "v_mfma_f32_16x16x32_bf16 v[112:115], v[168:171], a[160:163], v[112:115]\n\t"
  "v_mfma_f32_16x16x32_bf16 v[112:115], v[172:175], a[164:167], v[112:115]\n\t"
  "ds_read_b64_tr_b16 v[160:161], v3 offset:33280\n\t"
  "ds_read_b64_tr_b16 v[162:163], v3 offset:37376\n\t"
  "v_mfma_f32_16x16x32_bf16 v[116:119], v[168:171], a[168:171], v[116:119]\n\t"
  "v_mfma_f32_16x16x32_bf16 v[116:119], v[172:175], a[172:175], v[116:119]\n\t"
  "ds_read_b64_tr_b16 v[164:165], v3 offset:41472\n\t"
  "ds_read_b64_tr_b16 v[166:167], v3 offset:45568\n\t"
  "v_mfma_f32_16x16x32_bf16 v[120:123], v[168:171], a[176:179], v[120:123]\n\t"
  "v_mfma_f32_16x16x32_bf16 v[120:123], v[172:175], a[180:183], v[120:123]\n\t"
  "v_mfma_f32_16x16x32_bf16 v[124:127], v[168:171], a[184:187], v[124:127]\n\t"
  "v_mfma_f32_16x16x32_bf16 v[124:127], v[172:175], a[188:191], v[124:127]\n\t"
  "s_waitcnt vmcnt(8)\n\t"
  "s_barrier\n\t"
  "s_waitcnt lgkmcnt(2)\n\t"
  "s_nop 0\n\t"
  "v_mfma_f32_16x16x32_bf16 v[128:131], v[176:179], a[160:163], v[128:131]\n\t"
  "s_add_u32 m0, s24, 0\n\t"
  "s_add_u32 s30, s27, 327680\n\t"
  "s_nop 0\n\t"
  "buffer_load_dwordx4 v8, s[12:15], s30 offen offset:0 lds\n\t"
  "v_mfma_f32_16x16x32_bf16 v[128:131], v[180:183], a[164:167], v[128:131]\n\t"
  "ds_read_b64_tr_b16 v[168:169], v4 offset:0\n\t"
  "ds_read_b64_tr_b16 v[170:171], v4 offset:4096\n\t"
  "v_mfma_f32_16x16x32_bf16 v[132:135], v[176:179], a[168:171], v[132:135]\n\t"
  "buffer_load_dwordx4 v9, s[12:15], s30 offen offset:1024 lds\n\t"
  "v_mfma_f32_16x16x32_bf16 v[132:135], v[180:183], a[172:175], v[132:135]\n\t"
  "ds_read_b64_tr_b16 v[172:173], v4 offset:8192\n\t"
  "ds_read_b64_tr_b16 v[174:175], v4 offset:12288\n\t"
  "v_mfma_f32_16x16x32_bf16 v[136:139], v[176:179], a[176:179], v[136:139]\n\t"
  "s_add_u32 m0, s24, 2048\n\t"
  "s_add_u32 s30, s27, 335872\n\t"
  "buffer_load_dwordx4 v8, s[12:15], s30 offen offset:0 lds\n\t"
  "v_mfma_f32_16x16x32_bf16 v[136:139], v[180:183], a[180:183], v[136:139]\n\t"
  "ds_read_b128 a[128:131], v17 offset:0\n\t"
  "ds_read_b128 a[132:135], v17 offset:1024\n\t"
  "v_mfma_f32_16x16x32_bf16 v[140:143], v[176:179], a[184:187], v[140:143]\n\t"
  "buffer_load_dwordx4 v9, s[12:15], s30 offen offset:1024 lds\n\t"
  "v_mfma_f32_16x16x32_bf16 v[140:143], v[180:183], a[188:191], v[140:143]\n\t"
  "ds_read_b128 a[136:139], v17 offset:2048\n\t"
  "ds_read_b128 a[140:143], v17 offset:3072\n\t"
  "v_mfma_f32_16x16x32_bf16 v[144:147], v[160:163], a[160:163], v[144:147]\n\t"
  "s_waitcnt lgkmcnt(4)\n\t"
  "s_add_u32 m0, s25, 0\n\t"
  "s_add_u32 s31, s29, 10240\n\t"
  "buffer_load_dwordx4 v1, s[16:19], s31 offen offset:0 sc1 lds\n\t"
  "v_mfma_f32_16x16x32_bf16 v[144:147], v[164:167], a[164:167], v[144:147]\n\t"
  "ds_read_b64_tr_b16 v[176:177], v5 offset:0\n\t"
  "ds_read_b64_tr_b16 v[178:179], v5 offset:4096\n\t"
  "v_mfma_f32_16x16x32_bf16 v[148:151], v[160:163], a[168:171], v[148:151]\n\t"
  "buffer_load_dwordx4 v1, s[16:19], s31 offen offset:1024 sc1 lds\n\t"
  "v_mfma_f32_16x16x32_bf16 v[148:151], v[164:167], a[172:175], v[148:151]\n\t"
  "ds_read_b64_tr_b16 v[180:181], v5 offset:8192\n\t"
  "ds_read_b64_tr_b16 v[182:183], v5 offset:12288\n\t"
  "v_mfma_f32_16x16x32_bf16 v[152:155], v[160:163], a[176:179], v[152:155]\n\t"
  "s_add_u32 m0, s25, 2048\n\t"
  "s_add_u32 s31, s29, 198656\n\t"
  "buffer_load_dwordx4 v1, s[16:19], s31 offen offset:0 sc1 lds\n\t"
  "v_mfma_f32_16x16x32_bf16 v[152:155], v[164:167], a[180:183], v[152:155]\n\t"
  "ds_read_b128 a[144:147], v17 offset:4096\n\t"
  "ds_read_b128 a[148:151], v17 offset:5120\n\t"
  "v_mfma_f32_16x16x32_bf16 v[156:159], v[160:163], a[184:187], v[156:159]\n\t"
  "buffer_load_dwordx4 v1, s[16:19], s31 offen offset:1024 sc1 lds\n\t"
  "v_mfma_f32_16x16x32_bf16 v[156:159], v[164:167], a[188:191], v[156:159]\n\t"
  "s_waitcnt lgkmcnt(4)\n\t"
  "s_nop 0\n\t"
  "ds_read_b128 a[152:155], v17 offset:6144\n\t"
  "ds_read_b128 a[156:159], v17 offset:7168\n\t"
  "v_mfma_f32_16x16x32_bf16 v[96:99], v[168:171], a[128:131], v[96:99]\n\t"
  "v_mfma_f32_16x16x32_bf16 v[96:99], v[172:175], a[132:135], v[96:99]\n\t"
  "ds_read_b64_tr_b16 v[160:161], v4 offset:512\n\t"
  "ds_read_b64_tr_b16 v[162:163], v4 offset:4608\n\t"
  "v_mfma_f32_16x16x32_bf16 v[100:103], v[168:171], a[136:139], v[100:103]\n\t"
  "v_mfma_f32_16x16x32_bf16 v[100:103], v[172:175], a[140:143], v[100:103]\n\t"
  "s_waitcnt lgkmcnt(4)\n\t"
  "s_nop 0\n\t"
  "ds_read_b64_tr_b16 v[164:165], v4 offset:8704\n\t"
  "ds_read_b64_tr_b16 v[166:167], v4 offset:12800\n\t"
  "v_mfma_f32_16x16x32_bf16 v[104:107], v[168:171], a[144:147], v[104:107]\n\t"
  "v_mfma_f32_16x16x32_bf16 v[104:107], v[172:175], a[148:151], v[104:107]\n\t"
  "s_waitcnt lgkmcnt(4)\n\t"
  "s_nop 0\n\t"
  "v_mfma_f32_16x16x32_bf16 v[108:111], v[168:171], a[152:155], v[108:111]\n\t"
  "v_mfma_f32_16x16x32_bf16 v[108:111], v[172:175], a[156:159], v[108:111]\n\t"
  "v_mfma_f32_16x16x32_bf16 v[112:115], v[176:179], a[128:131], v[112:115]\n\t"
  "v_mfma_f32_16x16x32_bf16 v[112:115], v[180:183], a[132:135], v[112:115]\n\t"
  "ds_read_b64_tr_b16 v[168:169], v5 offset:512\n\t"
  "ds_read_b64_tr_b16 v[170:171], v5 offset:4608\n\t"
  "v_mfma_f32_16x16x32_bf16 v[116:119], v[176:179], a[136:139], v[116:119]\n\t"
  "v_mfma_f32_16x16x32_bf16 v[116:119], v[180:183], a[140:143], v[116:119]\n\t"
  "ds_read_b64_tr_b16 v[172:173], v5 offset:8704\n\t"
  "ds_read_b64_tr_b16 v[174:175], v5 offset:12800\n\t"
  "v_mfma_f32_16x16x32_bf16 v[120:123], v[176:179], a[144:147], v[120:123]\n\t"
  "v_mfma_f32_16x16x32_bf16 v[120:123], v[180:183], a[148:151], v[120:123]\n\t"
  "v_mfma_f32_16x16x32_bf16 v[124:127], v[176:179], a[152:155], v[124:127]\n\t"
  "v_mfma_f32_16x16x32_bf16 v[124:127], v[180:183], a[156:159], v[124:127]\n\t"
  "s_waitcnt vmcnt(8)\n\t"
  "s_barrier\n\t"
  "s_waitcnt lgkmcnt(2)\n\t"
  "s_nop 0\n\t"
  "v_mfma_f32_16x16x32_bf16 v[128:131], v[160:163], a[128:131], v[128:131]\n\t"
  "s_add_u32 m0, s24, 32768\n\t"
  "s_add_u32 s30, s27, 393216\n\t"
  "buffer_load_dwordx4 v8, s[12:15], s30 offen offset:0 lds\n\t"
  "v_mfma_f32_16x16x32_bf16 v[128:131], v[164:167], a[132:135], v[128:131]\n\t"
  "ds_read_b64_tr_b16 v[176:177], v4 offset:32768\n\t"
  "ds_read_b64_tr_b16 v[178:179], v4 offset:36864\n\t"
  "v_mfma_f32_16x16x32_bf16 v[132:135], v[160:163], a[136:139], v[132:135]\n\t"
  "buffer_load_dwordx4 v9, s[12:15], s30 offen offset:1024 lds\n\t"
  "v_mfma_f32_16x16x32_bf16 v[132:135], v[164:167], a[140:143], v[132:135]\n\t"
  "ds_read_b64_tr_b16 v[180:181], v4 offset:40960\n\t"
  "ds_read_b64_tr_b16 v[182:183], v4 offset:45056\n\t"
  "v_mfma_f32_16x16x32_bf16 v[136:139], v[160:163], a[144:147], v[136:139]\n\t"
  "s_add_u32 m0, s24, 34816\n\t"
  "s_add_u32 s30, s27, 401408\n\t"
  "buffer_load_dwordx4 v8, s[12:15], s30 offen offset:0 lds\n\t"
  "v_mfma_f32_16x16x32_bf16 v[136:139], v[164:167], a[148:151], v[136:139]\n\t"
  "ds_read_b128 a[160:163], v17 offset:32768\n\t"
  "ds_read_b128 a[164:167], v17 offset:33792\n\t"
  "v_mfma_f32_16x16x32_bf16 v[140:143], v[160:163], a[152:155], v[140:143]\n\t"
  "buffer_load_dwordx4 v9, s[12:15], s30 offen offset:1024 lds\n\t"
  "v_mfma_f32_16x16x32_bf16 v[140:143], v[164:167], a[156:159], v[140:143]\n\t"
  "ds_read_b128 a[168:171], v17 offset:34816\n\t"
  "ds_read_b128 a[172:175], v17 offset:35840\n\t"
  "v_mfma_f32_16x16x32_bf16 v[144:147], v[168:171], a[128:131], v[144:147]\n\t"
  "s_waitcnt lgkmcnt(4)\n\t"
  "s_add_u32 m0, s25, 32768\n\t"
  "s_add_u32 s31, s29, 12288\n\t"
  "s_nop 0\n\t"
  "buffer_load_dwordx4 v1, s[16:19], s31 offen offset:0 sc1 lds\n\t"
  "v_mfma_f32_16x16x32_bf16 v[144:147], v[172:175], a[132:135], v[144:147]\n\t"
  "ds_read_b64_tr_b16 v[160:161], v5 offset:32768\n\t"
  "ds_read_b64_tr_b16 v[162:163], v5 offset:36864\n\t"
  "v_mfma_f32_16x16x32_bf16 v[148:151], v[168:171], a[136:139], v[148:151]\n\t"
  "buffer_load_dwordx4 v1, s[16:19], s31 offen offset:1024 sc1 lds\n\t"
  "v_mfma_f32_16x16x32_bf16 v[148:151], v[172:175], a[140:143], v[148:151]\n\t"
  "ds_read_b64_tr_b16 v[164:165], v5 offset:40960\n\t"
  "ds_read_b64_tr_b16 v[166:167], v5 offset:45056\n\t"
  "v_mfma_f32_16x16x32_bf16 v[152:155], v[168:171], a[144:147], v[152:155]\n\t"
  "s_add_u32 m0, s25, 34816\n\t"
  "s_add_u32 s31, s29, 200704\n\t"
  "buffer_load_dwordx4 v1, s[16:19], s31 offen offset:0 sc1 lds\n\t"
  "v_mfma_f32_16x16x32_bf16 v[152:155], v[172:175], a[148:151], v[152:155]\n\t"
  "ds_read_b128 a[176:179], v17 offset:36864\n\t"
  "ds_read_b128 a[180:183], v17 offset:37888\n\t"
  "v_mfma_f32_16x16x32_bf16 v[156:159], v[168:171], a[152:155], v[156:159]\n\t"
  "buffer_load_dwordx4 v1, s[16:19], s31 offen offset:1024 sc1 lds\n\t"
  "v_mfma_f32_16x16x32_bf16 v[156:159], v[172:175], a[156:159], v[156:159]\n\t"
  "s_waitcnt lgkmcnt(4)\n\t"
  "s_nop 0\n\t"
  "ds_read_b128 a[184:187], v17 offset:38912\n\t"
  "ds_read_b128 a[188:191], v17 offset:39936\n\t"
  "v_mfma_f32_16x16x32_bf16 v[96:99], v[176:179], a[160:163], v[96:99]\n\t"
  "v_mfma_f32_16x16x32_bf16 v[96:99], v[180:183], a[164:167], v[96:99]\n\t"
  "ds_read_b64_tr_b16 v[168:169], v4 offset:33280\n\t"
  "ds_read_b64_tr_b16 v[170:171], v4 offset:37376\n\t"
  "v_mfma_f32_16x16x32_bf16 v[100:103], v[176:179], a[168:171], v[100:103]\n\t"
  "v_mfma_f32_16x16x32_bf16 v[100:103], v[180:183], a[172:175], v[100:103]\n\t"
  "s_waitcnt lgkmcnt(4)\n\t"
  "s_nop 0\n\t"
  "ds_read_b64_tr_b16 v[172:173], v4 offset:41472\n\t"
  "ds_read_b64_tr_b16 v[174:175], v4 offset:45568\n\t"
  "v_mfma_f32_16x16x32_bf16 v[104:107], v[176:179], a[176:179], v[104:107]\n\t"
  "v_mfma_f32_16x16x32_bf16 v[104:107], v[180:183], a[180:183], v[104:107]\n\t"
  "s_waitcnt lgkmcnt(4)\n\t"
  "s_nop 0\n\t"
  "v_mfma_f32_16x16x32_bf16 v[108:111], v[176:179], a[184:187], v[108:111]\n\t"
  "v_mfma_f32_16x16x32_bf16 v[108:111], v[180:183], a[188:191], v[108:111]\n\t"
  "v_mfma_f32_16x16x32_bf16 v[112:115], v[160:163], a[160:163], v[112:115]\n\t"
  "v_mfma_f32_16x16x32_bf16 v[112:115], v[164:167], a[164:167], v[112:115]\n\t"
  "ds_read_b64_tr_b16 v[176:177], v5 offset:33280\n\t"
  "ds_read_b64_tr_b16 v[178:179], v5 offset:37376\n\t"
  "v_mfma_f32_16x16x32_bf16 v[116:119], v[160:163], a[168:171], v[116:119]\n\t"
  "v_mfma_f32_16x16x32_bf16 v[116:119], v[164:167], a[172:175], v[116:119]\n\t"
  "ds_read_b64_tr_b16 v[180:181], v5 offset:41472\n\t"
  "ds_read_b64_tr_b16 v[182:183], v5 offset:45568\n\t"
  "v_mfma_f32_16x16x32_bf16 v[120:123], v[160:163], a[176:179], v[120:123]\n\t"
  "v_mfma_f32_16x16x32_bf16 v[120:123], v[164:167], a[180:183], v[120:123]\n\t"
  "v_mfma_f32_16x16x32_bf16 v[124:127], v[160:163], a[184:187], v[124:127]\n\t"
  "v_mfma_f32_16x16x32_bf16 v[124:127], v[164:167], a[188:191], v[124:127]\n\t"
  "s_waitcnt vmcnt(8)\n\t"
  "s_barrier\n\t"
  "s_waitcnt lgkmcnt(2)\n\t"
  "s_nop 0\n\t"
  "v_mfma_f32_16x16x32_bf16 v[128:131], v[168:171], a[160:163], v[128:131]\n\t"
  "s_add_u32 m0, s24, 65536\n\t"
  "s_add_u32 s30, s27, 458752\n\t"
  "buffer_load_dwordx4 v8, s[12:15], s30 offen offset:0 lds\n\t"
  "v_mfma_f32_16x16x32_bf16 v[128:131], v[172:175], a[164:167], v[128:131]\n\t"
  "ds_read_b64_tr_b16 v[160:161], v2 offset:0\n\t"
  "ds_read_b64_tr_b16 v[162:163], v2 offset:4096\n\t"
  "v_mfma_f32_16x16x32_bf16 v[132:135], v[168:171], a[168:171], v[132:135]\n\t"
  "buffer_load_dwordx4 v9, s[12:15], s30 offen offset:1024 lds\n\t"
  "v_mfma_f32_16x16x32_bf16 v[132:135], v[172:175], a[172:175], v[132:135]\n\t"
  "ds_read_b64_tr_b16 v[164:165], v2 offset:8192\n\t"
  "ds_read_b64_tr_b16 v[166:167], v2 offset:12288\n\t"
  "v_mfma_f32_16x16x32_bf16 v[136:139], v[168:171], a[176:179], v[136:139]\n\t"
  "s_add_u32 m0, s24, 67584\n\t"
  "s_add_u32 s30, s27, 466944\n\t"
  "buffer_load_dwordx4 v8, s[12:15], s30 offen offset:0 lds\n\t"
  "v_mfma_f32_16x16x32_bf16 v[136:139], v[172:175], a[180:183], v[136:139]\n\t"
  "ds_read_b128 a[128:131], v16 offset:0\n\t"
  "ds_read_b128 a[132:135], v16 offset:1024\n\t"
  "v_mfma_f32_16x16x32_bf16 v[140:143], v[168:171], a[184:187], v[140:143]\n\t"
  "buffer_load_dwordx4 v9, s[12:15], s30 offen offset:1024 lds\n\t"
  "v_mfma_f32_16x16x32_bf16 v[140:143], v[172:175], a[188:191], v[140:143]\n\t"
  "ds_read_b128 a[136:139], v16 offset:2048\n\t"
  "ds_read_b128 a[140:143], v16 offset:3072\n\t"
  "v_mfma_f32_16x16x32_bf16 v[144:147], v[176:179], a[160:163], v[144:147]\n\t"
  "s_waitcnt lgkmcnt(4)\n\t"
  "s_add_u32 m0, s25, 65536\n\t"
  "s_add_u32 s31, s29, 14336\n\t"
  "s_nop 0\n\t"
  "buffer_load_dwordx4 v1, s[16:19], s31 offen offset:0 sc1 lds\n\t"
  "v_mfma_f32_16x16x32_bf16 v[144:147], v[180:183], a[164:167], v[144:147]\n\t"
  "ds_read_b64_tr_b16 v[168:169], v3 offset:0\n\t"
  "ds_read_b64_tr_b16 v[170:171], v3 offset:4096\n\t"
  "v_mfma_f32_16x16x32_bf16 v[148:151], v[176:179], a[168:171], v[148:151]\n\t"
  "buffer_load_dwordx4 v1, s[16:19], s31 offen offset:1024 sc1 lds\n\t"
  "v_mfma_f32_16x16x32_bf16 v[148:151], v[180:183], a[172:175], v[148:151]\n\t"
  "ds_read_b64_tr_b16 v[172:173], v3 offset:8192\n\t"
  "ds_read_b64_tr_b16 v[174:175], v3 offset:12288\n\t"
  "v_mfma_f32_16x16x32_bf16 v[152:155], v[176:179], a[176:179], v[152:155]\n\t"
  "s_add_u32 m0, s25, 67584\n\t"
  "s_add_u32 s31, s29, 202752\n\t"
  "buffer_load_dwordx4 v1, s[16:19], s31 offen offset:0 sc1 lds\n\t"
  "v_mfma_f32_16x16x32_bf16 v[152:155], v[180:183], a[180:183], v[152:155]\n\t"
  "ds_read_b128 a[144:147], v16 offset:4096\n\t"
  "ds_read_b128 a[148:151], v16 offset:5120\n\t"
  "v_mfma_f32_16x16x32_bf16 v[156:159], v[176:179], a[184:187], v[156:159]\n\t"
  "buffer_load_dwordx4 v1, s[16:19], s31 offen offset:1024 sc1 lds\n\t"
  "v_mfma_f32_16x16x32_bf16 v[156:159], v[180:183], a[188:191], v[156:159]\n\t"
  "s_waitcnt lgkmcnt(4)\n\t"
  "s_nop 0\n\t"
  "ds_read_b128 a[152:155], v16 offset:6144\n\t"
  "ds_read_b128 a[156:159], v16 offset:7168\n\t"
  "v_mfma_f32_16x16x32_bf16 v[96:99], v[160:163], a[128:131], v[96:99]\n\t"
  "v_mfma_f32_16x16x32_bf16 v[96:99], v[164:167], a[132:135], v[96:99]\n\t"
  "ds_read_b64_tr_b16 v[176:177], v2 offset:512\n\t"
  "ds_read_b64_tr_b16 v[178:179], v2 offset:4608\n\t"
  "v_mfma_f32_16x16x32_bf16 v[100:103], v[160:163], a[136:139], v[100:103]\n\t"
  "v_mfma_f32_16x16x32_bf16 v[100:103], v[164:167], a[140:143], v[100:103]\n\t"
  "s_waitcnt lgkmcnt(4)\n\t"
  "s_nop 0\n\t"
  "ds_read_b64_tr_b16 v[180:181], v2 offset:8704\n\t"
  "ds_read_b64_tr_b16 v[182:183], v2 offset:12800\n\t"
  "v_mfma_f32_16x16x32_bf16 v[104:107], v[160:163], a[144:147], v[104:107]\n\t"
  "v_mfma_f32_16x16x32_bf16 v[104:107], v[164:167], a[148:151], v[104:107]\n\t"
  "s_waitcnt lgkmcnt(4)\n\t"
  "s_nop 0\n\t"
  "v_mfma_f32_16x16x32_bf16 v[108:111], v[160:163], a[152:155], v[108:111]\n\t"
  "v_mfma_f32_16x16x32_bf16 v[108:111], v[164:167], a[156:159], v[108:111]\n\t"
  "v_mfma_f32_16x16x32_bf16 v[112:115], v[168:171], a[128:131], v[112:115]\n\t"
  "v_mfma_f32_16x16x32_bf16 v[112:115], v[172:175], a[132:135], v[112:115]\n\t"
  "ds_read_b64_tr_b16 v[160:161], v3 offset:512\n\t"
  "ds_read_b64_tr_b16 v[162:163], v3 offset:4608\n\t"
  "v_mfma_f32_16x16x32_bf16 v[116:119], v[168:171], a[136:139], v[116:119]\n\t"
  "v_mfma_f32_16x16x32_bf16 v[116:119], v[172:175], a[140:143], v[116:119]\n\t"
  "ds_read_b64_tr_b16 v[164:165], v3 offset:8704\n\t"
  "ds_read_b64_tr_b16 v[166:167], v3 offset:12800\n\t"
  "v_mfma_f32_16x16x32_bf16 v[120:123], v[168:171], a[144:147], v[120:123]\n\t"
  "v_mfma_f32_16x16x32_bf16 v[120:123], v[172:175], a[148:151], v[120:123]\n\t"
  "v_mfma_f32_16x16x32_bf16 v[124:127], v[168:171], a[152:155], v[124:127]\n\t"
  "v_mfma_f32_16x16x32_bf16 v[124:127], v[172:175], a[156:159], v[124:127]\n\t"
  "s_waitcnt vmcnt(8)\n\t"
  "s_barrier\n\t"
  "s_waitcnt lgkmcnt(2)\n\t"
  "s_nop 0\n\t"
  "v_mfma_f32_16x16x32_bf16 v[128:131], v[176:179], a[128:131], v[128:131]\n\t"
  "s_add_u32 m0, s24, 98304\n\t"
  "s_add_u32 s30, s27, 524288\n\t"
  "buffer_load_dwordx4 v8, s[12:15], s30 offen offset:0 lds\n\t"
  "v_mfma_f32_16x16x32_bf16 v[128:131], v[180:183], a[132:135], v[128:131]\n\t"
  "ds_read_b64_tr_b16 v[168:169], v2 offset:32768\n\t"
  "ds_read_b64_tr_b16 v[170:171], v2 offset:36864\n\t"
  "v_mfma_f32_16x16x32_bf16 v[132:135], v[176:179], a[136:139], v[132:135]\n\t"
  "buffer_load_dwordx4 v9, s[12:15], s30 offen offset:1024 lds\n\t"
  "v_mfma_f32_16x16x32_bf16 v[132:135], v[180:183], a[140:143], v[132:135]\n\t"
  "ds_read_b64_tr_b16 v[172:173], v2 offset:40960\n\t"
  "ds_read_b64_tr_b16 v[174:175], v2 offset:45056\n\t"
  "v_mfma_f32_16x16x32_bf16 v[136:139], v[176:179], a[144:147], v[136:139]\n\t"
  "s_add_u32 m0, s24, 100352\n\t"
  "s_add_u32 s30, s27, 532480\n\t"
  "buffer_load_dwordx4 v8, s[12:15], s30 offen offset:0 lds\n\t"
  "v_mfma_f32_16x16x32_bf16 v[136:139], v[180:183], a[148:151], v[136:139]\n\t"
  "ds_read_b128 a[160:163], v16 offset:32768\n\t"
  "ds_read_b128 a[164:167], v16 offset:33792\n\t"
  "v_mfma_f32_16x16x32_bf16 v[140:143], v[176:179], a[152:155], v[140:143]\n\t"
  "buffer_load_dwordx4 v9, s[12:15], s30 offen offset:1024 lds\n\t"
  "v_mfma_f32_16x16x32_bf16 v[140:143], v[180:183], a[156:159], v[140:143]\n\t"
  "ds_read_b128 a[168:171], v16 offset:34816\n\t"
  "ds_read_b128 a[172:175], v16 offset:35840\n\t"
  "v_mfma_f32_16x16x32_bf16 v[144:147], v[160:163], a[128:131], v[144:147]\n\t"
  "s_waitcnt lgkmcnt(4)\n\t"
  "s_add_u32 m0, s25, 98304\n\t"
  "s_add_u32 s31, s29, 16384\n\t"
  "s_nop 0\n\t"
  "buffer_load_dwordx4 v1, s[16:19], s31 offen offset:0 sc1 lds\n\t"
  "v_mfma_f32_16x16x32_bf16 v[144:147], v[164:167], a[132:135], v[144:147]\n\t"
  "ds_read_b64_tr_b16 v[176:177], v3 offset:32768\n\t"
  "ds_read_b64_tr_b16 v[178:179], v3 offset:36864\n\t"
  "v_mfma_f32_16x16x32_bf16 v[148:151], v[160:163], a[136:139], v[148:151]\n\t"
  "buffer_load_dwordx4 v1, s[16:19], s31 offen offset:1024 sc1 lds\n\t"
  "v_mfma_f32_16x16x32_bf16 v[148:151], v[164:167], a[140:143], v[148:151]\n\t"
  "ds_read_b64_tr_b16 v[180:181], v3 offset:40960\n\t"
  "ds_read_b64_tr_b16 v[182:183], v3 offset:45056\n\t"
  "v_mfma_f32_16x16x32_bf16 v[152:155], v[160:163], a[144:147], v[152:155]\n\t"
  "s_add_u32 m0, s25, 100352\n\t"
  "s_add_u32 s31, s29, 204800\n\t"
  "buffer_load_dwordx4 v1, s[16:19], s31 offen offset:0 sc1 lds\n\t"
  "v_mfma_f32_16x16x32_bf16 v[152:155], v[164:167], a[148:151], v[152:155]\n\t"
  "ds_read_b128 a[176:179], v16 offset:36864\n\t"
  "ds_read_b128 a[180:183], v16 offset:37888\n\t"
  "v_mfma_f32_16x16x32_bf16 v[156:159], v[160:163], a[152:155], v[156:159]\n\t"
  "buffer_load_dwordx4 v1, s[16:19], s31 offen offset:1024 sc1 lds\n\t"
  "v_mfma_f32_16x16x32_bf16 v[156:159], v[164:167], a[156:159], v[156:159]\n\t"
  "s_waitcnt lgkmcnt(4)\n\t"
  "s_nop 0\n\t"
  "ds_read_b128 a[184:187], v16 offset:38912\n\t"
  "ds_read_b128 a[188:191], v16 offset:39936\n\t"
  "v_mfma_f32_16x16x32_bf16 v[96:99], v[168:171], a[160:163], v[96:99]\n\t"
  "v_mfma_f32_16x16x32_bf16 v[96:99], v[172:175], a[164:167], v[96:99]\n\t"
  "ds_read_b64_tr_b16 v[160:161], v2 offset:33280\n\t"
  "ds_read_b64_tr_b16 v[162:163], v2 offset:37376\n\t"
  "v_mfma_f32_16x16x32_bf16 v[100:103], v[168:171], a[168:171], v[100:103]\n\t"
  "v_mfma_f32_16x16x32_bf16 v[100:103], v[172:175], a[172:175], v[100:103]\n\t"
  "s_waitcnt lgkmcnt(4)\n\t"
  "s_nop 0\n\t"
  "ds_read_b64_tr_b16 v[164:165], v2 offset:41472\n\t"
  "ds_read_b64_tr_b16 v[166:167], v2 offset:45568\n\t"
  "v_mfma_f32_16x16x32_bf16 v[104:107], v[168:171], a[176:179], v[104:107]\n\t"
  "v_mfma_f32_16x16x32_bf16 v[104:107], v[172:175], a[180:183], v[104:107]\n\t"
  "s_waitcnt lgkmcnt(4)\n\t"
  "s_nop 0\n\t"
  "v_mfma_f32_16x16x32_bf16 v[108:111], v[168:171], a[184:187], v[108:111]\n\t"
  "v_mfma_f32_16x16x32_bf16 v[108:111], v[172:175], a[188:191], v[108:111]\n\t"
  "v_mfma_f32_16x16x32_bf16 v[112:115], v[176:179], a[160:163], v[112:115]\n\t"
  "v_mfma_f32_16x16x32_bf16 v[112:115], v[180:183], a[164:167], v[112:115]\n\t"
  "ds_read_b64_tr_b16 v[168:169], v3 offset:33280\n\t"
  "ds_read_b64_tr_b16 v[170:171], v3 offset:37376\n\t"
  "v_mfma_f32_16x16x32_bf16 v[116:119], v[176:179], a[168:171], v[116:119]\n\t"
  "v_mfma_f32_16x16x32_bf16 v[116:119], v[180:183], a[172:175], v[116:119]\n\t"
  "ds_read_b64_tr_b16 v[172:173], v3 offset:41472\n\t"
  "ds_read_b64_tr_b16 v[174:175], v3 offset:45568\n\t"
  "v_mfma_f32_16x16x32_bf16 v[120:123], v[176:179], a[176:179], v[120:123]\n\t"
  "v_mfma_f32_16x16x32_bf16 v[120:123], v[180:183], a[180:183], v[120:123]\n\t"
  "v_mfma_f32_16x16x32_bf16 v[124:127], v[176:179], a[184:187], v[124:127]\n\t"
  "v_mfma_f32_16x16x32_bf16 v[124:127], v[180:183], a[188:191], v[124:127]\n\t"
  "s_waitcnt vmcnt(8)\n\t"
  "s_barrier\n\t"
  "s_waitcnt lgkmcnt(2)\n\t"
  "s_nop 0\n\t"
  "v_mfma_f32_16x16x32_bf16 v[128:131], v[160:163], a[160:163], v[128:131]\n\t"
  "s_add_u32 m0, s24, 0\n\t"
  "s_add_u32 s30, s27, 589824\n\t"
  "s_nop 0\n\t"
  "buffer_load_dwordx4 v8, s[12:15], s30 offen offset:0 lds\n\t"
  "v_mfma_f32_16x16x32_bf16 v[128:131], v[164:167], a[164:167], v[128:131]\n\t"
  "ds_read_b64_tr_b16 v[176:177], v4 offset:0\n\t"
  "ds_read_b64_tr_b16 v[178:179], v4 offset:4096\n\t"
  "v_mfma_f32_16x16x32_bf16 v[132:135], v[160:163], a[168:171], v[132:135]\n\t"
  "buffer_load_dwordx4 v9, s[12:15], s30 offen offset:1024 lds\n\t"
  "v_mfma_f32_16x16x32_bf16 v[132:135], v[164:167], a[172:175], v[132:135]\n\t"
  "ds_read_b64_tr_b16 v[180:181], v4 offset:8192\n\t"
  "ds_read_b64_tr_b16 v[182:183], v4 offset:12288\n\t"
  "v_mfma_f32_16x16x32_bf16 v[136:139], v[160:163], a[176:179], v[136:139]\n\t"
  "s_add_u32 m0, s24, 2048\n\t"
  "s_add_u32 s30, s27, 598016\n\t"
  "buffer_load_dwordx4 v8, s[12:15], s30 offen offset:0 lds\n\t"
  "v_mfma_f32_16x16x32_bf16 v[136:139], v[164:167], a[180:183], v[136:139]\n\t"
  "ds_read_b128 a[128:131], v17 offset:0\n\t"
  "ds_read_b128 a[132:135], v17 offset:1024\n\t"
  "v_mfma_f32_16x16x32_bf16 v[140:143], v[160:163], a[184:187], v[140:143]\n\t"
  "buffer_load_dwordx4 v9, s[12:15], s30 offen offset:1024 lds\n\t"
  "v_mfma_f32_16x16x32_bf16 v[140:143], v[164:167], a[188:191], v[140:143]\n\t"
  "ds_read_b128 a[136:139], v17 offset:2048\n\t"
  "ds_read_b128 a[140:143], v17 offset:3072\n\t"
  "v_mfma_f32_16x16x32_bf16 v[144:147], v[168:171], a[160:163], v[144:147]\n\t"
  "s_waitcnt lgkmcnt(4)\n\t"
  "s_add_u32 m0, s25, 0\n\t"
  "s_add_u32 s31, s29, 18432\n\t"
  "buffer_load_dwordx4 v1, s[16:19], s31 offen offset:0 sc1 lds\n\t"
  "v_mfma_f32_16x16x32_bf16 v[144:147], v[172:175], a[164:167], v[144:147]\n\t"
  "ds_read_b64_tr_b16 v[160:161], v5 offset:0\n\t"
  "ds_read_b64_tr_b16 v[162:163], v5 offset:4096\n\t"
  "v_mfma_f32_16x16x32_bf16 v[148:151], v[168:171], a[168:171], v[148:151]\n\t"
  "buffer_load_dwordx4 v1, s[16:19], s31 offen offset:1024 sc1 lds\n\t"
  "v_mfma_f32_16x16x32_bf16 v[148:151], v[172:175], a[172:175], v[148:151]\n\t"
  "ds_read_b64_tr_b16 v[164:165], v5 offset:8192\n\t"
  "ds_read_b64_tr_b16 v[166:167], v5 offset:12288\n\t"
  "v_mfma_f32_16x16x32_bf16 v[152:155], v[168:171], a[176:179], v[152:155]\n\t"
  "s_add_u32 m0, s25, 2048\n\t"
  "s_add_u32 s31, s29, 206848\n\t"
  "buffer_load_dwordx4 v1, s[16:19], s31 offen offset:0 sc1 lds\n\t"
  "v_mfma_f32_16x16x32_bf16 v[152:155], v[172:175], a[180:183], v[152:155]\n\t"
  "ds_read_b128 a[144:147], v17 offset:4096\n\t"
  "ds_read_b128 a[148:151], v17 offset:5120\n\t"
  "v_mfma_f32_16x16x32_bf16 v[156:159], v[168:171], a[184:187], v[156:159]\n\t"
  "buffer_load_dwordx4 v1, s[16:19], s31 offen offset:1024 sc1 lds\n\t"
  "v_mfma_f32_16x16x32_bf16 v[156:159], v[172:175], a[188:191], v[156:159]\n\t"
  "s_waitcnt lgkmcnt(4)\n\t"
  "s_nop 0\n\t"
  "ds_read_b128 a[152:155], v17 offset:6144\n\t"
  "ds_read_b128 a[156:159], v17 offset:7168\n\t"
  "v_mfma_f32_16x16x32_bf16 v[96:99], v[176:179], a[128:131], v[96:99]\n\t"
  "v_mfma_f32_16x16x32_bf16 v[96:99], v[180:183], a[132:135], v[96:99]\n\t"
  "ds_read_b64_tr_b16 v[168:169], v4 offset:512\n\t"
  "ds_read_b64_tr_b16 v[170:171], v4 offset:4608\n\t"
  "v_mfma_f32_16x16x32_bf16 v[100:103], v[176:179], a[136:139], v[100:103]\n\t"
  "v_mfma_f32_16x16x32_bf16 v[100:103], v[180:183], a[140:143], v[100:103]\n\t"
  "s_waitcnt lgkmcnt(4)\n\t"
  "s_nop 0\n\t"
  "ds_read_b64_tr_b16 v[172:173], v4 offset:8704\n\t"
  "ds_read_b64_tr_b16 v[174:175], v4 offset:12800\n\t"
  "v_mfma_f32_16x16x32_bf16 v[104:107], v[176:179], a[144:147], v[104:107]\n\t"
  "v_mfma_f32_16x16x32_bf16 v[104:107], v[180:183], a[148:151], v[104:107]\n\t"
  "s_waitcnt lgkmcnt(4)\n\t"
  "s_nop 0\n\t"
  "v_mfma_f32_16x16x32_bf16 v[108:111], v[176:179], a[152:155], v[108:111]\n\t"
  "v_mfma_f32_16x16x32_bf16 v[108:111], v[180:183], a[156:159], v[108:111]\n\t"
  "v_mfma_f32_16x16x32_bf16 v[112:115], v[160:163], a[128:131], v[112:115]\n\t"
  "v_mfma_f32_16x16x32_bf16 v[112:115], v[164:167], a[132:135], v[112:115]\n\t"
  "ds_read_b64_tr_b16 v[176:177], v5 offset:512\n\t"
  "ds_read_b64_tr_b16 v[178:179], v5 offset:4608\n\t"
  "v_mfma_f32_16x16x32_bf16 v[116:119], v[160:163], a[136:139], v[116:119]\n\t"
  "v_mfma_f32_16x16x32_bf16 v[116:119], v[164:167], a[140:143], v[116:119]\n\t"
  "ds_read_b64_tr_b16 v[180:181], v5 offset:8704\n\t"
  "ds_read_b64_tr_b16 v[182:183], v5 offset:12800\n\t"
  "v_mfma_f32_16x16x32_bf16 v[120:123], v[160:163], a[144:147], v[120:123]\n\t"
  "v_mfma_f32_16x16x32_bf16 v[120:123], v[164:167], a[148:151], v[120:123]\n\t"
  "v_mfma_f32_16x16x32_bf16 v[124:127], v[160:163], a[152:155], v[124:127]\n\t"
  "v_mfma_f32_16x16x32_bf16 v[124:127], v[164:167], a[156:159], v[124:127]\n\t"
  "s_waitcnt vmcnt(8)\n\t"
  "s_barrier\n\t"
  "s_waitcnt lgkmcnt(2)\n\t"
  "s_nop 0\n\t"
  "v_mfma_f32_16x16x32_bf16 v[128:131], v[168:171], a[128:131], v[128:131]\n\t"
  "s_add_u32 m0, s24, 32768\n\t"
  "s_add_u32 s30, s27, 655360\n\t"
  "buffer_load_dwordx4 v8, s[12:15], s30 offen offset:0 lds\n\t"
  "v_mfma_f32_16x16x32_bf16 v[128:131], v[172:175], a[132:135], v[128:131]\n\t"
  "ds_read_b64_tr_b16 v[160:161], v4 offset:32768\n\t"
  "ds_read_b64_tr_b16 v[162:163], v4 offset:36864\n\t"
  "v_mfma_f32_16x16x32_bf16 v[132:135], v[168:171], a[136:139], v[132:135]\n\t"
  "buffer_load_dwordx4 v9, s[12:15], s30 offen offset:1024 lds\n\t"
  "v_mfma_f32_16x16x32_bf16 v[132:135], v[172:175], a[140:143], v[132:135]\n\t"
  "ds_read_b64_tr_b16 v[164:165], v4 offset:40960\n\t"
  "ds_read_b64_tr_b16 v[166:167], v4 offset:45056\n\t"
  "v_mfma_f32_16x16x32_bf16 v[136:139], v[168:171], a[144:147], v[136:139]\n\t"
  "s_add_u32 m0, s24, 34816\n\t"
  "s_add_u32 s30, s27, 663552\n\t"
  "buffer_load_dwordx4 v8, s[12:15], s30 offen offset:0 lds\n\t"
  "v_mfma_f32_16x16x32_bf16 v[136:139], v[172:175], a[148:151], v[136:139]\n\t"
  "ds_read_b128 a[160:163], v17 offset:32768\n\t"
  "ds_read_b128 a[164:167], v17 offset:33792\n\t"
  "v_mfma_f32_16x16x32_bf16 v[140:143], v[168:171], a[152:155], v[140:143]\n\t"
  "buffer_load_dwordx4 v9, s[12:15], s30 offen offset:1024 lds\n\t"
  "v_mfma_f32_16x16x32_bf16 v[140:143], v[172:175], a[156:159], v[140:143]\n\t"
  "ds_read_b128 a[168:171], v17 offset:34816\n\t"
  "ds_read_b128 a[172:175], v17 offset:35840\n\t"
  "v_mfma_f32_16x16x32_bf16 v[144:147], v[176:179], a[128:131], v[144:147]\n\t"
  "s_waitcnt lgkmcnt(4)\n\t"
  "s_add_u32 m0, s25, 32768\n\t"
  "s_add_u32 s31, s29, 20480\n\t"
  "s_nop 0\n\t"
  "buffer_load_dwordx4 v1, s[16:19], s31 offen offset:0 sc1 lds\n\t"
  "v_mfma_f32_16x16x32_bf16 v[144:147], v[180:183], a[132:135], v[144:147]\n\t"
  "ds_read_b64_tr_b16 v[168:169], v5 offset:32768\n\t"
  "ds_read_b64_tr_b16 v[170:171], v5 offset:36864\n\t"
  "v_mfma_f32_16x16x32_bf16 v[148:151], v[176:179], a[136:139], v[148:151]\n\t"
  "buffer_load_dwordx4 v1, s[16:19], s31 offen offset:1024 sc1 lds\n\t"
  "v_mfma_f32_16x16x32_bf16 v[148:151], v[180:183], a[140:143], v[148:151]\n\t"
  "ds_read_b64_tr_b16 v[172:173], v5 offset:40960\n\t"
  "ds_read_b64_tr_b16 v[174:175], v5 offset:45056\n\t"
  "v_mfma_f32_16x16x32_bf16 v[152:155], v[176:179], a[144:147], v[152:155]\n\t"
  "s_add_u32 m0, s25, 34816\n\t"
  "s_add_u32 s31, s29, 208896\n\t"
  "buffer_load_dwordx4 v1, s[16:19], s31 offen offset:0 sc1 lds\n\t"
  "v_mfma_f32_16x16x32_bf16 v[152:155], v[180:183], a[148:151], v[152:155]\n\t"
  "ds_read_b128 a[176:179], v17 offset:36864\n\t"
  "ds_read_b128 a[180:183], v17 offset:37888\n\t"
  "v_mfma_f32_16x16x32_bf16 v[156:159], v[176:179], a[152:155], v[156:159]\n\t"
  "buffer_load_dwordx4 v1, s[16:19], s31 offen offset:1024 sc1 lds\n\t"
  "v_mfma_f32_16x16x32_bf16 v[156:159], v[180:183], a[156:159], v[156:159]\n\t"
  "s_waitcnt lgkmcnt(4)\n\t"
  "s_nop 0\n\t"
  "ds_read_b128 a[184:187], v17 offset:38912\n\t"
  "ds_read_b128 a[188:191], v17 offset:39936\n\t"
  "v_mfma_f32_16x16x32_bf16 v[96:99], v[160:163], a[160:163], v[96:99]\n\t"
  "v_mfma_f32_16x16x32_bf16 v[96:99], v[164:167], a[164:167], v[96:99]\n\t"
  "ds_read_b64_tr_b16 v[176:177], v4 offset:33280\n\t"
  "ds_read_b64_tr_b16 v[178:179], v4 offset:37376\n\t"
  "v_mfma_f32_16x16x32_bf16 v[100:103], v[160:163], a[168:171], v[100:103]\n\t"
  "v_mfma_f32_16x16x32_bf16 v[100:103], v[164:167], a[172:175], v[100:103]\n\t"
  "s_waitcnt lgkmcnt(4)\n\t"
  "s_nop 0\n\t"
  "ds_read_b64_tr_b16 v[180:181], v4 offset:41472\n\t"
  "ds_read_b64_tr_b16 v[182:183], v4 offset:45568\n\t"
  "v_mfma_f32_16x16x32_bf16 v[104:107], v[160:163], a[176:179], v[104:107]\n\t"
  "v_mfma_f32_16x16x32_bf16 v[104:107], v[164:167], a[180:183], v[104:107]\n\t"
  "s_waitcnt lgkmcnt(4)\n\t"
  "s_nop 0\n\t"
  "v_mfma_f32_16x16x32_bf16 v[108:111], v[160:163], a[184:187], v[108:111]\n\t"
  "v_mfma_f32_16x16x32_bf16 v[108:111], v[164:167], a[188:191], v[108:111]\n\t"
  "v_mfma_f32_16x16x32_bf16 v[112:115], v[168:171], a[160:163], v[112:115]\n\t"
  "v_mfma_f32_16x16x32_bf16 v[112:115], v[172:175], a[164:167], v[112:115]\n\t"
  "ds_read_b64_tr_b16 v[160:161], v5 offset:33280\n\t"
  "ds_read_b64_tr_b16 v[162:163], v5 offset:37376\n\t"
  "v_mfma_f32_16x16x32_bf16 v[116:119], v[168:171], a[168:171], v[116:119]\n\t"
  "v_mfma_f32_16x16x32_bf16 v[116:119], v[172:175], a[172:175], v[116:119]\n\t"
  "ds_read_b64_tr_b16 v[164:165], v5 offset:41472\n\t"
  "ds_read_b64_tr_b16 v[166:167], v5 offset:45568\n\t"
  "v_mfma_f32_16x16x32_bf16 v[120:123], v[168:171], a[176:179], v[120:123]\n\t"
  "v_mfma_f32_16x16x32_bf16 v[120:123], v[172:175], a[180:183], v[120:123]\n\t"
  "v_mfma_f32_16x16x32_bf16 v[124:127], v[168:171], a[184:187], v[124:127]\n\t"
  "v_mfma_f32_16x16x32_bf16 v[124:127], v[172:175], a[188:191], v[124:127]\n\t"
  "s_waitcnt vmcnt(8)\n\t"
  "s_barrier\n\t"
  "s_waitcnt lgkmcnt(2)\n\t"
  "s_nop 0\n\t"
  "v_mfma_f32_16x16x32_bf16 v[128:131], v[176:179], a[160:163], v[128:131]\n\t"
  "s_add_u32 m0, s24, 65536\n\t"
  "s_add_u32 s30, s27, 720896\n\t"
  "buffer_load_dwordx4 v8, s[12:15], s30 offen offset:0 lds\n\t"
  "v_mfma_f32_16x16x32_bf16 v[128:131], v[180:183], a[164:167], v[128:131]\n\t"
  "ds_read_b64_tr_b16 v[168:169], v2 offset:0\n\t"
  "ds_read_b64_tr_b16 v[170:171], v2 offset:4096\n\t"
  "v_mfma_f32_16x16x32_bf16 v[132:135], v[176:179], a[168:171], v[132:135]\n\t"
  "buffer_load_dwordx4 v9, s[12:15], s30 offen offset:1024 lds\n\t"
  "v_mfma_f32_16x16x32_bf16 v[132:135], v[180:183], a[172:175], v[132:135]\n\t"
  "ds_read_b64_tr_b16 v[172:173], v2 offset:8192\n\t"
  "ds_read_b64_tr_b16 v[174:175], v2 offset:12288\n\t"
  "v_mfma_f32_16x16x32_bf16 v[136:139], v[176:179], a[176:179], v[136:139]\n\t"
  "s_add_u32 m0, s24, 67584\n\t"
  "s_add_u32 s30, s27, 729088\n\t"
  "buffer_load_dwordx4 v8, s[12:15], s30 offen offset:0 lds\n\t"
  "v_mfma_f32_16x16x32_bf16 v[136:139], v[180:183], a[180:183], v[136:139]\n\t"
  "ds_read_b128 a[128:131], v16 offset:0\n\t"
  "ds_read_b128 a[132:135], v16 offset:1024\n\t"
  "v_mfma_f32_16x16x32_bf16 v[140:143], v[176:179], a[184:187], v[140:143]\n\t"
  "buffer_load_dwordx4 v9, s[12:15], s30 offen offset:1024 lds\n\t"
  "v_mfma_f32_16x16x32_bf16 v[140:143], v[180:183], a[188:191], v[140:143]\n\t"
  "ds_read_b128 a[136:139], v16 offset:2048\n\t"
  "ds_read_b128 a[140:143], v16 offset:3072\n\t"
  "v_mfma_f32_16x16x32_bf16 v[144:147], v[160:163], a[160:163], v[144:147]\n\t"
  "s_waitcnt lgkmcnt(4)\n\t"
  "s_add_u32 m0, s25, 65536\n\t"
  "s_add_u32 s31, s29, 22528\n\t"
  "s_nop 0\n\t"
  "buffer_load_dwordx4 v1, s[16:19], s31 offen offset:0 sc1 lds\n\t"
  "v_mfma_f32_16x16x32_bf16 v[144:147], v[164:167], a[164:167], v[144:147]\n\t"
  "ds_read_b64_tr_b16 v[176:177], v3 offset:0\n\t"
  "ds_read_b64_tr_b16 v[178:179], v3 offset:4096\n\t"
  "v_mfma_f32_16x16x32_bf16 v[148:151], v[160:163], a[168:171], v[148:151]\n\t"
  "buffer_load_dwordx4 v1, s[16:19], s31 offen offset:1024 sc1 lds\n\t"
  "v_mfma_f32_16x16x32_bf16 v[148:151], v[164:167], a[172:175], v[148:151]\n\t"
  "ds_read_b64_tr_b16 v[180:181], v3 offset:8192\n\t"
  "ds_read_b64_tr_b16 v[182:183], v3 offset:12288\n\t"
  "v_mfma_f32_16x16x32_bf16 v[152:155], v[160:163], a[176:179], v[152:155]\n\t"
  "s_add_u32 m0, s25, 67584\n\t"
  "s_add_u32 s31, s29, 210944\n\t"
  "buffer_load_dwordx4 v1, s[16:19], s31 offen offset:0 sc1 lds\n\t"
  "v_mfma_f32_16x16x32_bf16 v[152:155], v[164:167], a[180:183], v[152:155]\n\t"
  "ds_read_b128 a[144:147], v16 offset:4096\n\t"
  "ds_read_b128 a[148:151], v16 offset:5120\n\t"
  "v_mfma_f32_16x16x32_bf16 v[156:159], v[160:163], a[184:187], v[156:159]\n\t"
  "buffer_load_dwordx4 v1, s[16:19], s31 offen offset:1024 sc1 lds\n\t"
  "v_mfma_f32_16x16x32_bf16 v[156:159], v[164:167], a[188:191], v[156:159]\n\t"
  "s_waitcnt lgkmcnt(4)\n\t"
  "s_nop 0\n\t"
  "ds_read_b128 a[152:155], v16 offset:6144\n\t"
  "ds_read_b128 a[156:159], v16 offset:7168\n\t"
  "v_mfma_f32_16x16x32_bf16 v[96:99], v[168:171], a[128:131], v[96:99]\n\t"
  "v_mfma_f32_16x16x32_bf16 v[96:99], v[172:175], a[132:135], v[96:99]\n\t"
  "ds_read_b64_tr_b16 v[160:161], v2 offset:512\n\t"
  "ds_read_b64_tr_b16 v[162:163], v2 offset:4608\n\t"
  "v_mfma_f32_16x16x32_bf16 v[100:103], v[168:171], a[136:139], v[100:103]\n\t"
  "v_mfma_f32_16x16x32_bf16 v[100:103], v[172:175], a[140:143], v[100:103]\n\t"
  "s_waitcnt lgkmcnt(4)\n\t"
  "s_nop 0\n\t"
  "ds_read_b64_tr_b16 v[164:165], v2 offset:8704\n\t"
  "ds_read_b64_tr_b16 v[166:167], v2 offset:12800\n\t"
  "v_mfma_f32_16x16x32_bf16 v[104:107], v[168:171], a[144:147], v[104:107]\n\t"
  "v_mfma_f32_16x16x32_bf16 v[104:107], v[172:175], a[148:151], v[104:107]\n\t"
  "s_waitcnt lgkmcnt(4)\n\t"
  "s_nop 0\n\t"
  "v_mfma_f32_16x16x32_bf16 v[108:111], v[168:171], a[152:155], v[108:111]\n\t"
  "v_mfma_f32_16x16x32_bf16 v[108:111], v[172:175], a[156:159], v[108:111]\n\t"
  "v_mfma_f32_16x16x32_bf16 v[112:115], v[176:179], a[128:131], v[112:115]\n\t"
  "v_mfma_f32_16x16x32_bf16 v[112:115], v[180:183], a[132:135], v[112:115]\n\t"
  "ds_read_b64_tr_b16 v[168:169], v3 offset:512\n\t"
  "ds_read_b64_tr_b16 v[170:171], v3 offset:4608\n\t"
  "v_mfma_f32_16x16x32_bf16 v[116:119], v[176:179], a[136:139], v[116:119]\n\t"
  "v_mfma_f32_16x16x32_bf16 v[116:119], v[180:183], a[140:143], v[116:119]\n\t"
  "ds_read_b64_tr_b16 v[172:173], v3 offset:8704\n\t"
  "ds_read_b64_tr_b16 v[174:175], v3 offset:12800\n\t"
  "v_mfma_f32_16x16x32_bf16 v[120:123], v[176:179], a[144:147], v[120:123]\n\t"
  "v_mfma_f32_16x16x32_bf16 v[120:123], v[180:183], a[148:151], v[120:123]\n\t"
  "v_mfma_f32_16x16x32_bf16 v[124:127], v[176:179], a[152:155], v[124:127]\n\t"
  "v_mfma_f32_16x16x32_bf16 v[124:127], v[180:183], a[156:159], v[124:127]\n\t"
  "s_waitcnt vmcnt(8)\n\t"
  "s_barrier\n\t"
  "s_waitcnt lgkmcnt(2)\n\t"
  "s_nop 0\n\t"
  "v_mfma_f32_16x16x32_bf16 v[128:131], v[160:163], a[128:131], v[128:131]\n\t"
  "s_add_u32 m0, s24, 98304\n\t"
  "s_add_u32 s30, s27, 786432\n\t"
  "buffer_load_dwordx4 v8, s[12:15], s30 offen offset:0 lds\n\t"
  "v_mfma_f32_16x16x32_bf16 v[128:131], v[164:167], a[132:135], v[128:131]\n\t"
  "ds_read_b64_tr_b16 v[176:177], v2 offset:32768\n\t"
  "ds_read_b64_tr_b16 v[178:179], v2 offset:36864\n\t"
  "v_mfma_f32_16x16x32_bf16 v[132:135], v[160:163], a[136:139], v[132:135]\n\t"
  "buffer_load_dwordx4 v9, s[12:15], s30 offen offset:1024 lds\n\t"
  "v_mfma_f32_16x16x32_bf16 v[132:135], v[164:167], a[140:143], v[132:135]\n\t"
  "ds_read_b64_tr_b16 v[180:181], v2 offset:40960\n\t"
  "ds_read_b64_tr_b16 v[182:183], v2 offset:45056\n\t"
  "v_mfma_f32_16x16x32_bf16 v[136:139], v[160:163], a[144:147], v[136:139]\n\t"
  "s_add_u32 m0, s24, 100352\n\t"
  "s_add_u32 s30, s27, 794624\n\t"
  "buffer_load_dwordx4 v8, s[12:15], s30 offen offset:0 lds\n\t"
  "v_mfma_f32_16x16x32_bf16 v[136:139], v[164:167], a[148:151], v[136:139]\n\t"
  "ds_read_b128 a[160:163], v16 offset:32768\n\t"
  "ds_read_b128 a[164:167], v16 offset:33792\n\t"
  "v_mfma_f32_16x16x32_bf16 v[140:143], v[160:163], a[152:155], v[140:143]\n\t"
  "buffer_load_dwordx4 v9, s[12:15], s30 offen offset:1024 lds\n\t"
  "v_mfma_f32_16x16x32_bf16 v[140:143], v[164:167], a[156:159], v[140:143]\n\t"
  "ds_read_b128 a[168:171], v16 offset:34816\n\t"
  "ds_read_b128 a[172:175], v16 offset:35840\n\t"
  "v_mfma_f32_16x16x32_bf16 v[144:147], v[168:171], a[128:131], v[144:147]\n\t"
  "s_waitcnt lgkmcnt(4)\n\t"
  "s_add_u32 m0, s25, 98304\n\t"
  "s_add_u32 s31, s29, 24576\n\t"
  "s_nop 0\n\t"
  "buffer_load_dwordx4 v1, s[16:19], s31 offen offset:0 sc1 lds\n\t"
  "v_mfma_f32_16x16x32_bf16 v[144:147], v[172:175], a[132:135], v[144:147]\n\t"
  "ds_read_b64_tr_b16 v[160:161], v3 offset:32768\n\t"
  "ds_read_b64_tr_b16 v[162:163], v3 offset:36864\n\t"
  "v_mfma_f32_16x16x32_bf16 v[148:151], v[168:171], a[136:139], v[148:151]\n\t"
  "buffer_load_dwordx4 v1, s[16:19], s31 offen offset:1024 sc1 lds\n\t"
  "v_mfma_f32_16x16x32_bf16 v[148:151], v[172:175], a[140:143], v[148:151]\n\t"
  "ds_read_b64_tr_b16 v[164:165], v3 offset:40960\n\t"
  "ds_read_b64_tr_b16 v[166:167], v3 offset:45056\n\t"
  "v_mfma_f32_16x16x32_bf16 v[152:155], v[168:171], a[144:147], v[152:155]\n\t"
  "s_add_u32 m0, s25, 100352\n\t"
  "s_add_u32 s31, s29, 212992\n\t"
  "buffer_load_dwordx4 v1, s[16:19], s31 offen offset:0 sc1 lds\n\t"
  "v_mfma_f32_16x16x32_bf16 v[152:155], v[172:175], a[148:151], v[152:155]\n\t"
  "ds_read_b128 a[176:179], v16 offset:36864\n\t"
  "ds_read_b128 a[180:183], v16 offset:37888\n\t"
  "v_mfma_f32_16x16x32_bf16 v[156:159], v[168:171], a[152:155], v[156:159]\n\t"
  "buffer_load_dwordx4 v1, s[16:19], s31 offen offset:1024 sc1 lds\n\t"
  "v_mfma_f32_16x16x32_bf16 v[156:159], v[172:175], a[156:159], v[156:159]\n\t"
  "s_waitcnt lgkmcnt(4)\n\t"
  "s_nop 0\n\t"
  "ds_read_b128 a[184:187], v16 offset:38912\n\t"
  "ds_read_b128 a[188:191], v16 offset:39936\n\t"
  "v_mfma_f32_16x16x32_bf16 v[96:99], v[176:179], a[160:163], v[96:99]\n\t"
  "v_mfma_f32_16x16x32_bf16 v[96:99], v[180:183], a[164:167], v[96:99]\n\t"
  "ds_read_b64_tr_b16 v[168:169], v2 offset:33280\n\t"
  "ds_read_b64_tr_b16 v[170:171], v2 offset:37376\n\t"
  "v_mfma_f32_16x16x32_bf16 v[100:103], v[176:179], a[168:171], v[100:103]\n\t"
  "v_mfma_f32_16x16x32_bf16 v[100:103], v[180:183], a[172:175], v[100:103]\n\t"
  "s_waitcnt lgkmcnt(4)\n\t"
  "s_nop 0\n\t"
  "ds_read_b64_tr_b16 v[172:173], v2 offset:41472\n\t"
  "ds_read_b64_tr_b16 v[174:175], v2 offset:45568\n\t"
  "v_mfma_f32_16x16x32_bf16 v[104:107], v[176:179], a[176:179], v[104:107]\n\t"
  "v_mfma_f32_16x16x32_bf16 v[104:107], v[180:183], a[180:183], v[104:107]\n\t"
  "s_waitcnt lgkmcnt(4)\n\t"
  "s_nop 0\n\t"
  "v_mfma_f32_16x16x32_bf16 v[108:111], v[176:179], a[184:187], v[108:111]\n\t"
  "v_mfma_f32_16x16x32_bf16 v[108:111], v[180:183], a[188:191], v[108:111]\n\t"
  "v_mfma_f32_16x16x32_bf16 v[112:115], v[160:163], a[160:163], v[112:115]\n\t"
  "v_mfma_f32_16x16x32_bf16 v[112:115], v[164:167], a[164:167], v[112:115]\n\t"
  "ds_read_b64_tr_b16 v[176:177], v3 offset:33280\n\t"
  "ds_read_b64_tr_b16 v[178:179], v3 offset:37376\n\t"
  "v_mfma_f32_16x16x32_bf16 v[116:119], v[160:163], a[168:171], v[116:119]\n\t"
  "v_mfma_f32_16x16x32_bf16 v[116:119], v[164:167], a[172:175], v[116:119]\n\t"
  "ds_read_b64_tr_b16 v[180:181], v3 offset:41472\n\t"
  "ds_read_b64_tr_b16 v[182:183], v3 offset:45568\n\t"
  "v_mfma_f32_16x16x32_bf16 v[120:123], v[160:163], a[176:179], v[120:123]\n\t"
  "v_mfma_f32_16x16x32_bf16 v[120:123], v[164:167], a[180:183], v[120:123]\n\t"
  "v_mfma_f32_16x16x32_bf16 v[124:127], v[160:163], a[184:187], v[124:127]\n\t"
  "v_mfma_f32_16x16x32_bf16 v[124:127], v[164:167], a[188:191], v[124:127]\n\t"
  "s_waitcnt vmcnt(8)\n\t"
  "s_barrier\n\t"
  "s_waitcnt lgkmcnt(2)\n\t"
  "s_nop 0\n\t"
  "v_mfma_f32_16x16x32_bf16 v[128:131], v[168:171], a[160:163], v[128:131]\n\t"
  "s_add_u32 m0, s24, 0\n\t"
  "s_add_u32 s30, s27, 851968\n\t"
  "s_nop 0\n\t"
  "buffer_load_dwordx4 v8, s[12:15], s30 offen offset:0 lds\n\t"
  "v_mfma_f32_16x16x32_bf16 v[128:131], v[172:175], a[164:167], v[128:131]\n\t"
  "ds_read_b64_tr_b16 v[160:161], v4 offset:0\n\t"
  "ds_read_b64_tr_b16 v[162:163], v4 offset:4096\n\t"
  "v_mfma_f32_16x16x32_bf16 v[132:135], v[168:171], a[168:171], v[132:135]\n\t"
  "buffer_load_dwordx4 v9, s[12:15], s30 offen offset:1024 lds\n\t"
  "v_mfma_f32_16x16x32_bf16 v[132:135], v[172:175], a[172:175], v[132:135]\n\t"
  "ds_read_b64_tr_b16 v[164:165], v4 offset:8192\n\t"
  "ds_read_b64_tr_b16 v[166:167], v4 offset:12288\n\t"
  "v_mfma_f32_16x16x32_bf16 v[136:139], v[168:171], a[176:179], v[136:139]\n\t"
  "s_add_u32 m0, s24, 2048\n\t"
  "s_add_u32 s30, s27, 860160\n\t"
  "buffer_load_dwordx4 v8, s[12:15], s30 offen offset:0 lds\n\t"
  "v_mfma_f32_16x16x32_bf16 v[136:139], v[172:175], a[180:183], v[136:139]\n\t"
  "ds_read_b128 a[128:131], v17 offset:0\n\t"
  "ds_read_b128 a[132:135], v17 offset:1024\n\t"
  "v_mfma_f32_16x16x32_bf16 v[140:143], v[168:171], a[184:187], v[140:143]\n\t"
  "buffer_load_dwordx4 v9, s[12:15], s30 offen offset:1024 lds\n\t"
  "v_mfma_f32_16x16x32_bf16 v[140:143], v[172:175], a[188:191], v[140:143]\n\t"
  "ds_read_b128 a[136:139], v17 offset:2048\n\t"
  "ds_read_b128 a[140:143], v17 offset:3072\n\t"
  "v_mfma_f32_16x16x32_bf16 v[144:147], v[176:179], a[160:163], v[144:147]\n\t"
  "s_waitcnt lgkmcnt(4)\n\t"
  "s_add_u32 m0, s25, 0\n\t"
  "s_add_u32 s31, s29, 26624\n\t"
  "buffer_load_dwordx4 v1, s[16:19], s31 offen offset:0 sc1 lds\n\t"
  "v_mfma_f32_16x16x32_bf16 v[144:147], v[180:183], a[164:167], v[144:147]\n\t"
  "ds_read_b64_tr_b16 v[168:169], v5 offset:0\n\t"
  "ds_read_b64_tr_b16 v[170:171], v5 offset:4096\n\t"
  "v_mfma_f32_16x16x32_bf16 v[148:151], v[176:179], a[168:171], v[148:151]\n\t"
  "buffer_load_dwordx4 v1, s[16:19], s31 offen offset:1024 sc1 lds\n\t"
  "v_mfma_f32_16x16x32_bf16 v[148:151], v[180:183], a[172:175], v[148:151]\n\t"
  "ds_read_b64_tr_b16 v[172:173], v5 offset:8192\n\t"
  "ds_read_b64_tr_b16 v[174:175], v5 offset:12288\n\t"
  "v_mfma_f32_16x16x32_bf16 v[152:155], v[176:179], a[176:179], v[152:155]\n\t"
  "s_add_u32 m0, s25, 2048\n\t"
  "s_add_u32 s31, s29, 215040\n\t"
  "buffer_load_dwordx4 v1, s[16:19], s31 offen offset:0 sc1 lds\n\t"
  "v_mfma_f32_16x16x32_bf16 v[152:155], v[180:183], a[180:183], v[152:155]\n\t"
  "ds_read_b128 a[144:147], v17 offset:4096\n\t"
  "ds_read_b128 a[148:151], v17 offset:5120\n\t"
  "v_mfma_f32_16x16x32_bf16 v[156:159], v[176:179], a[184:187], v[156:159]\n\t"
  "buffer_load_dwordx4 v1, s[16:19], s31 offen offset:1024 sc1 lds\n\t"
  "v_mfma_f32_16x16x32_bf16 v[156:159], v[180:183], a[188:191], v[156:159]\n\t"
  "s_waitcnt lgkmcnt(4)\n\t"
  "s_nop 0\n\t"
  "ds_read_b128 a[152:155], v17 offset:6144\n\t"
  "ds_read_b128 a[156:159], v17 offset:7168\n\t"
  "v_mfma_f32_16x16x32_bf16 v[96:99], v[160:163], a[128:131], v[96:99]\n\t"
  "v_mfma_f32_16x16x32_bf16 v[96:99], v[164:167], a[132:135], v[96:99]\n\t"
  "ds_read_b64_tr_b16 v[176:177], v4 offset:512\n\t"
  "ds_read_b64_tr_b16 v[178:179], v4 offset:4608\n\t"
  "v_mfma_f32_16x16x32_bf16 v[100:103], v[160:163], a[136:139], v[100:103]\n\t"
  "v_mfma_f32_16x16x32_bf16 v[100:103], v[164:167], a[140:143], v[100:103]\n\t"
  "s_waitcnt lgkmcnt(4)\n\t"
  "s_nop 0\n\t"
  "ds_read_b64_tr_b16 v[180:181], v4 offset:8704\n\t"
  "ds_read_b64_tr_b16 v[182:183], v4 offset:12800\n\t"
  "v_mfma_f32_16x16x32_bf16 v[104:107], v[160:163], a[144:147], v[104:107]\n\t"
  "v_mfma_f32_16x16x32_bf16 v[104:107], v[164:167], a[148:151], v[104:107]\n\t"
  "s_waitcnt lgkmcnt(4)\n\t"
  "s_nop 0\n\t"
  "v_mfma_f32_16x16x32_bf16 v[108:111], v[160:163], a[152:155], v[108:111]\n\t"
  "v_mfma_f32_16x16x32_bf16 v[108:111], v[164:167], a[156:159], v[108:111]\n\t"
  "v_mfma_f32_16x16x32_bf16 v[112:115], v[168:171], a[128:131], v[112:115]\n\t"
  "v_mfma_f32_16x16x32_bf16 v[112:115], v[172:175], a[132:135], v[112:115]\n\t"
  "ds_read_b64_tr_b16 v[160:161], v5 offset:512\n\t"
  "ds_read_b64_tr_b16 v[162:163], v5 offset:4608\n\t"
  "v_mfma_f32_16x16x32_bf16 v[116:119], v[168:171], a[136:139], v[116:119]\n\t"
  "v_mfma_f32_16x16x32_bf16 v[116:119], v[172:175], a[140:143], v[116:119]\n\t"
  "ds_read_b64_tr_b16 v[164:165], v5 offset:8704\n\t"
  "ds_read_b64_tr_b16 v[166:167], v5 offset:12800\n\t"
  "v_mfma_f32_16x16x32_bf16 v[120:123], v[168:171], a[144:147], v[120:123]\n\t"
  "v_mfma_f32_16x16x32_bf16 v[120:123], v[172:175], a[148:151], v[120:123]\n\t"
  "v_mfma_f32_16x16x32_bf16 v[124:127], v[168:171], a[152:155], v[124:127]\n\t"
  "v_mfma_f32_16x16x32_bf16 v[124:127], v[172:175], a[156:159], v[124:127]\n\t"
  "s_waitcnt vmcnt(8)\n\t"
  "s_barrier\n\t"
  "s_waitcnt lgkmcnt(2)\n\t"
  "s_nop 0\n\t"
  "v_mfma_f32_16x16x32_bf16 v[128:131], v[176:179], a[128:131], v[128:131]\n\t"
  "s_add_u32 m0, s24, 32768\n\t"
  "s_add_u32 s30, s27, 917504\n\t"
  "buffer_load_dwordx4 v8, s[12:15], s30 offen offset:0 lds\n\t"
  "v_mfma_f32_16x16x32_bf16 v[128:131], v[180:183], a[132:135], v[128:131]\n\t"
  "ds_read_b64_tr_b16 v[168:169], v4 offset:32768\n\t"
  "ds_read_b64_tr_b16 v[170:171], v4 offset:36864\n\t"
  "v_mfma_f32_16x16x32_bf16 v[132:135], v[176:179], a[136:139], v[132:135]\n\t"
  "buffer_load_dwordx4 v9, s[12:15], s30 offen offset:1024 lds\n\t"
  "v_mfma_f32_16x16x32_bf16 v[132:135], v[180:183], a[140:143], v[132:135]\n\t"
  "ds_read_b64_tr_b16 v[172:173], v4 offset:40960\n\t"
  "ds_read_b64_tr_b16 v[174:175], v4 offset:45056\n\t"
  "v_mfma_f32_16x16x32_bf16 v[136:139], v[176:179], a[144:147], v[136:139]\n\t"
  "s_add_u32 m0, s24, 34816\n\t"
  "s_add_u32 s30, s27, 925696\n\t"
  "buffer_load_dwordx4 v8, s[12:15], s30 offen offset:0 lds\n\t"
  "v_mfma_f32_16x16x32_bf16 v[136:139], v[180:183], a[148:151], v[136:139]\n\t"
  "ds_read_b128 a[160:163], v17 offset:32768\n\t"
  "ds_read_b128 a[164:167], v17 offset:33792\n\t"
  "v_mfma_f32_16x16x32_bf16 v[140:143], v[176:179], a[152:155], v[140:143]\n\t"
  "buffer_load_dwordx4 v9, s[12:15], s30 offen offset:1024 lds\n\t"
  "v_mfma_f32_16x16x32_bf16 v[140:143], v[180:183], a[156:159], v[140:143]\n\t"
  "ds_read_b128 a[168:171], v17 offset:34816\n\t"
  "ds_read_b128 a[172:175], v17 offset:35840\n\t"
  "v_mfma_f32_16x16x32_bf16 v[144:147], v[160:163], a[128:131], v[144:147]\n\t"
  "s_waitcnt lgkmcnt(4)\n\t"
  "s_add_u32 m0, s25, 32768\n\t"
  "s_add_u32 s31, s29, 28672\n\t"
  "s_nop 0\n\t"
  "buffer_load_dwordx4 v1, s[16:19], s31 offen offset:0 sc1 lds\n\t"
  "v_mfma_f32_16x16x32_bf16 v[144:147], v[164:167], a[132:135], v[144:147]\n\t"
  "ds_read_b64_tr_b16 v[176:177], v5 offset:32768\n\t"
  "ds_read_b64_tr_b16 v[178:179], v5 offset:36864\n\t"
  "v_mfma_f32_16x16x32_bf16 v[148:151], v[160:163], a[136:139], v[148:151]\n\t"
  "buffer_load_dwordx4 v1, s[16:19], s31 offen offset:1024 sc1 lds\n\t"
  "v_mfma_f32_16x16x32_bf16 v[148:151], v[164:167], a[140:143], v[148:151]\n\t"
  "ds_read_b64_tr_b16 v[180:181], v5 offset:40960\n\t"
  "ds_read_b64_tr_b16 v[182:183], v5 offset:45056\n\t"
  "v_mfma_f32_16x16x32_bf16 v[152:155], v[160:163], a[144:147], v[152:155]\n\t"
  "s_add_u32 m0, s25, 34816\n\t"
  "s_add_u32 s31, s29, 217088\n\t"
  "buffer_load_dwordx4 v1, s[16:19], s31 offen offset:0 sc1 lds\n\t"
  "v_mfma_f32_16x16x32_bf16 v[152:155], v[164:167], a[148:151], v[152:155]\n\t"
  "ds_read_b128 a[176:179], v17 offset:36864\n\t"
  "ds_read_b128 a[180:183], v17 offset:37888\n\t"
  "v_mfma_f32_16x16x32_bf16 v[156:159], v[160:163], a[152:155], v[156:159]\n\t"
  "buffer_load_dwordx4 v1, s[16:19], s31 offen offset:1024 sc1 lds\n\t"
  "v_mfma_f32_16x16x32_bf16 v[156:159], v[164:167], a[156:159], v[156:159]\n\t"
  "s_waitcnt lgkmcnt(4)\n\t"
  "s_nop 0\n\t"
  "ds_read_b128 a[184:187], v17 offset:38912\n\t"
  "ds_read_b128 a[188:191], v17 offset:39936\n\t"
  "v_mfma_f32_16x16x32_bf16 v[96:99], v[168:171], a[160:163], v[96:99]\n\t"
  "v_mfma_f32_16x16x32_bf16 v[96:99], v[172:175], a[164:167], v[96:99]\n\t"
  "ds_read_b64_tr_b16 v[160:161], v4 offset:33280\n\t"
  "ds_read_b64_tr_b16 v[162:163], v4 offset:37376\n\t"
  "v_mfma_f32_16x16x32_bf16 v[100:103], v[168:171], a[168:171], v[100:103]\n\t"
  "v_mfma_f32_16x16x32_bf16 v[100:103], v[172:175], a[172:175], v[100:103]\n\t"
  "s_waitcnt lgkmcnt(4)\n\t"
  "s_nop 0\n\t"
  "ds_read_b64_tr_b16 v[164:165], v4 offset:41472\n\t"
  "ds_read_b64_tr_b16 v[166:167], v4 offset:45568\n\t"
  "v_mfma_f32_16x16x32_bf16 v[104:107], v[168:171], a[176:179], v[104:107]\n\t"
  "v_mfma_f32_16x16x32_bf16 v[104:107], v[172:175], a[180:183], v[104:107]\n\t"
  "s_waitcnt lgkmcnt(4)\n\t"
  "s_nop 0\n\t"
  "v_mfma_f32_16x16x32_bf16 v[108:111], v[168:171], a[184:187], v[108:111]\n\t"
  "v_mfma_f32_16x16x32_bf16 v[108:111], v[172:175], a[188:191], v[108:111]\n\t"
  "v_mfma_f32_16x16x32_bf16 v[112:115], v[176:179], a[160:163], v[112:115]\n\t"
  "v_mfma_f32_16x16x32_bf16 v[112:115], v[180:183], a[164:167], v[112:115]\n\t"
  "ds_read_b64_tr_b16 v[168:169], v5 offset:33280\n\t"
  "ds_read_b64_tr_b16 v[170:171], v5 offset:37376\n\t"
  "v_mfma_f32_16x16x32_bf16 v[116:119], v[176:179], a[168:171], v[116:119]\n\t"
  "v_mfma_f32_16x16x32_bf16 v[116:119], v[180:183], a[172:175], v[116:119]\n\t"
  "ds_read_b64_tr_b16 v[172:173], v5 offset:41472\n\t"
  "ds_read_b64_tr_b16 v[174:175], v5 offset:45568\n\t"
  "v_mfma_f32_16x16x32_bf16 v[120:123], v[176:179], a[176:179], v[120:123]\n\t"
  "v_mfma_f32_16x16x32_bf16 v[120:123], v[180:183], a[180:183], v[120:123]\n\t"
  "v_mfma_f32_16x16x32_bf16 v[124:127], v[176:179], a[184:187], v[124:127]\n\t"
  "v_mfma_f32_16x16x32_bf16 v[124:127], v[180:183], a[188:191], v[124:127]\n\t"
  "s_waitcnt vmcnt(8)\n\t"
  "s_barrier\n\t"
  "s_waitcnt lgkmcnt(2)\n\t"
  "s_nop 0\n\t"
  "v_mfma_f32_16x16x32_bf16 v[128:131], v[160:163], a[160:163], v[128:131]\n\t"
  "s_add_u32 m0, s24, 65536\n\t"
  "s_add_u32 s30, s27, 983040\n\t"
  "buffer_load_dwordx4 v8, s[12:15], s30 offen offset:0 lds\n\t"
  "v_mfma_f32_16x16x32_bf16 v[128:131], v[164:167], a[164:167], v[128:131]\n\t"
  "ds_read_b64_tr_b16 v[176:177], v2 offset:0\n\t"
  "ds_read_b64_tr_b16 v[178:179], v2 offset:4096\n\t"
  "v_mfma_f32_16x16x32_bf16 v[132:135], v[160:163], a[168:171], v[132:135]\n\t"
  "buffer_load_dwordx4 v9, s[12:15], s30 offen offset:1024 lds\n\t"
  "v_mfma_f32_16x16x32_bf16 v[132:135], v[164:167], a[172:175], v[132:135]\n\t"
  "ds_read_b64_tr_b16 v[180:181], v2 offset:8192\n\t"
  "ds_read_b64_tr_b16 v[182:183], v2 offset:12288\n\t"
  "v_mfma_f32_16x16x32_bf16 v[136:139], v[160:163], a[176:179], v[136:139]\n\t"
  "s_add_u32 m0, s24, 67584\n\t"
  "s_add_u32 s30, s27, 991232\n\t"
  "buffer_load_dwordx4 v8, s[12:15], s30 offen offset:0 lds\n\t"
  "v_mfma_f32_16x16x32_bf16 v[136:139], v[164:167], a[180:183], v[136:139]\n\t"
  "ds_read_b128 a[128:131], v16 offset:0\n\t"
  "ds_read_b128 a[132:135], v16 offset:1024\n\t"
  "v_mfma_f32_16x16x32_bf16 v[140:143], v[160:163], a[184:187], v[140:143]\n\t"
  "buffer_load_dwordx4 v9, s[12:15], s30 offen offset:1024 lds\n\t"
  "v_mfma_f32_16x16x32_bf16 v[140:143], v[164:167], a[188:191], v[140:143]\n\t"
  "ds_read_b128 a[136:139], v16 offset:2048\n\t"
  "ds_read_b128 a[140:143], v16 offset:3072\n\t"
  "v_mfma_f32_16x16x32_bf16 v[144:147], v[168:171], a[160:163], v[144:147]\n\t"
  "s_waitcnt lgkmcnt(4)\n\t"
  "s_add_u32 m0, s25, 65536\n\t"
  "s_add_u32 s31, s29, 30720\n\t"
  "s_nop 0\n\t"
  "buffer_load_dwordx4 v1, s[16:19], s31 offen offset:0 sc1 lds\n\t"
  "v_mfma_f32_16x16x32_bf16 v[144:147], v[172:175], a[164:167], v[144:147]\n\t"
  "ds_read_b64_tr_b16 v[160:161], v3 offset:0\n\t"
  "ds_read_b64_tr_b16 v[162:163], v3 offset:4096\n\t"
  "v_mfma_f32_16x16x32_bf16 v[148:151], v[168:171], a[168:171], v[148:151]\n\t"
  "buffer_load_dwordx4 v1, s[16:19], s31 offen offset:1024 sc1 lds\n\t"
  "v_mfma_f32_16x16x32_bf16 v[148:151], v[172:175], a[172:175], v[148:151]\n\t"
  "ds_read_b64_tr_b16 v[164:165], v3 offset:8192\n\t"
  "ds_read_b64_tr_b16 v[166:167], v3 offset:12288\n\t"
  "v_mfma_f32_16x16x32_bf16 v[152:155], v[168:171], a[176:179], v[152:155]\n\t"
  "s_add_u32 m0, s25, 67584\n\t"
  "s_add_u32 s31, s29, 219136\n\t"
  "buffer_load_dwordx4 v1, s[16:19], s31 offen offset:0 sc1 lds\n\t"
  "v_mfma_f32_16x16x32_bf16 v[152:155], v[172:175], a[180:183], v[152:155]\n\t"
  "ds_read_b128 a[144:147], v16 offset:4096\n\t"
  "ds_read_b128 a[148:151], v16 offset:5120\n\t"
  "v_mfma_f32_16x16x32_bf16 v[156:159], v[168:171], a[184:187], v[156:159]\n\t"
  "buffer_load_dwordx4 v1, s[16:19], s31 offen offset:1024 sc1 lds\n\t"
  "v_mfma_f32_16x16x32_bf16 v[156:159], v[172:175], a[188:191], v[156:159]\n\t"
  "s_waitcnt lgkmcnt(4)\n\t"
  "s_nop 0\n\t"
  "ds_read_b128 a[152:155], v16 offset:6144\n\t"
  "ds_read_b128 a[156:159], v16 offset:7168\n\t"
  "v_mfma_f32_16x16x32_bf16 v[96:99], v[176:179], a[128:131], v[96:99]\n\t"
  "v_mfma_f32_16x16x32_bf16 v[96:99], v[180:183], a[132:135], v[96:99]\n\t"
  "ds_read_b64_tr_b16 v[168:169], v2 offset:512\n\t"
  "ds_read_b64_tr_b16 v[170:171], v2 offset:4608\n\t"
  "v_mfma_f32_16x16x32_bf16 v[100:103], v[176:179], a[136:139], v[100:103]\n\t"
  "v_mfma_f32_16x16x32_bf16 v[100:103], v[180:183], a[140:143], v[100:103]\n\t"
  "s_waitcnt lgkmcnt(4)\n\t"
  "s_nop 0\n\t"
  "ds_read_b64_tr_b16 v[172:173], v2 offset:8704\n\t"
  "ds_read_b64_tr_b16 v[174:175], v2 offset:12800\n\t"
  "v_mfma_f32_16x16x32_bf16 v[104:107], v[176:179], a[144:147], v[104:107]\n\t"
  "v_mfma_f32_16x16x32_bf16 v[104:107], v[180:183], a[148:151], v[104:107]\n\t"
  "s_waitcnt lgkmcnt(4)\n\t"
  "s_nop 0\n\t"
  "v_mfma_f32_16x16x32_bf16 v[108:111], v[176:179], a[152:155], v[108:111]\n\t"
  "v_mfma_f32_16x16x32_bf16 v[108:111], v[180:183], a[156:159], v[108:111]\n\t"
  "v_mfma_f32_16x16x32_bf16 v[112:115], v[160:163], a[128:131], v[112:115]\n\t"
  "v_mfma_f32_16x16x32_bf16 v[112:115], v[164:167], a[132:135], v[112:115]\n\t"
  "ds_read_b64_tr_b16 v[176:177], v3 offset:512\n\t"
  "ds_read_b64_tr_b16 v[178:179], v3 offset:4608\n\t"
  "v_mfma_f32_16x16x32_bf16 v[116:119], v[160:163], a[136:139], v[116:119]\n\t"
  "v_mfma_f32_16x16x32_bf16 v[116:119], v[164:167], a[140:143], v[116:119]\n\t"
  "ds_read_b64_tr_b16 v[180:181], v3 offset:8704\n\t"
  "ds_read_b64_tr_b16 v[182:183], v3 offset:12800\n\t"
  "v_mfma_f32_16x16x32_bf16 v[120:123], v[160:163], a[144:147], v[120:123]\n\t"
  "v_mfma_f32_16x16x32_bf16 v[120:123], v[164:167], a[148:151], v[120:123]\n\t"
  "v_mfma_f32_16x16x32_bf16 v[124:127], v[160:163], a[152:155], v[124:127]\n\t"
  "v_mfma_f32_16x16x32_bf16 v[124:127], v[164:167], a[156:159], v[124:127]\n\t"
  "s_waitcnt vmcnt(8)\n\t"
  "s_barrier\n\t"
  "s_waitcnt lgkmcnt(2)\n\t"
  "s_nop 0\n\t"
  "v_mfma_f32_16x16x32_bf16 v[128:131], v[168:171], a[128:131], v[128:131]\n\t"
  "v_mfma_f32_16x16x32_bf16 v[128:131], v[172:175], a[132:135], v[128:131]\n\t"
  "ds_read_b64_tr_b16 v[160:161], v2 offset:32768\n\t"
  "ds_read_b64_tr_b16 v[162:163], v2 offset:36864\n\t"
  "v_mfma_f32_16x16x32_bf16 v[132:135], v[168:171], a[136:139], v[132:135]\n\t"
  "v_mfma_f32_16x16x32_bf16 v[132:135], v[172:175], a[140:143], v[132:135]\n\t"
  "ds_read_b64_tr_b16 v[164:165], v2 offset:40960\n\t"
  "ds_read_b64_tr_b16 v[166:167], v2 offset:45056\n\t"
  "v_mfma_f32_16x16x32_bf16 v[136:139], v[168:171], a[144:147], v[136:139]\n\t"
  "v_mfma_f32_16x16x32_bf16 v[136:139], v[172:175], a[148:151], v[136:139]\n\t"
  "ds_read_b128 a[160:163], v16 offset:32768\n\t"
  "ds_read_b128 a[164:167], v16 offset:33792\n\t"
  "v_mfma_f32_16x16x32_bf16 v[140:143], v[168:171], a[152:155], v[140:143]\n\t"
  "v_mfma_f32_16x16x32_bf16 v[140:143], v[172:175], a[156:159], v[140:143]\n\t"
  "ds_read_b128 a[168:171], v16 offset:34816\n\t"
  "ds_read_b128 a[172:175], v16 offset:35840\n\t"
  "v_mfma_f32_16x16x32_bf16 v[144:147], v[176:179], a[128:131], v[144:147]\n\t"
  "s_waitcnt lgkmcnt(4)\n\t"
  "s_nop 0\n\t"
  "v_mfma_f32_16x16x32_bf16 v[144:147], v[180:183], a[132:135], v[144:147]\n\t"
  "ds_read_b64_tr_b16 v[168:169], v3 offset:32768\n\t"
  "ds_read_b64_tr_b16 v[170:171], v3 offset:36864\n\t"
  "v_mfma_f32_16x16x32_bf16 v[148:151], v[176:179], a[136:139], v[148:151]\n\t"
  "v_mfma_f32_16x16x32_bf16 v[148:151], v[180:183], a[140:143], v[148:151]\n\t"
  "ds_read_b64_tr_b16 v[172:173], v3 offset:40960\n\t"
  "ds_read_b64_tr_b16 v[174:175], v3 offset:45056\n\t"
  "v_mfma_f32_16x16x32_bf16 v[152:155], v[176:179], a[144:147], v[152:155]\n\t"
  "v_mfma_f32_16x16x32_bf16 v[152:155], v[180:183], a[148:151], v[152:155]\n\t"
  "ds_read_b128 a[176:179], v16 offset:36864\n\t"
  "ds_read_b128 a[180:183], v16 offset:37888\n\t"
  "v_mfma_f32_16x16x32_bf16 v[156:159], v[176:179], a[152:155], v[156:159]\n\t"
  "v_mfma_f32_16x16x32_bf16 v[156:159], v[180:183], a[156:159], v[156:159]\n\t"
  "s_waitcnt lgkmcnt(4)\n\t"
  "s_nop 0\n\t"
  "ds_read_b128 a[184:187], v16 offset:38912\n\t"
  "ds_read_b128 a[188:191], v16 offset:39936\n\t"
  "v_mfma_f32_16x16x32_bf16 v[96:99], v[160:163], a[160:163], v[96:99]\n\t"
  "v_mfma_f32_16x16x32_bf16 v[96:99], v[164:167], a[164:167], v[96:99]\n\t"
  "ds_read_b64_tr_b16 v[176:177], v2 offset:33280\n\t"
  "ds_read_b64_tr_b16 v[178:179], v2 offset:37376\n\t"
  "v_mfma_f32_16x16x32_bf16 v[100:103], v[160:163], a[168:171], v[100:103]\n\t"
  "v_mfma_f32_16x16x32_bf16 v[100:103], v[164:167], a[172:175], v[100:103]\n\t"
  "s_waitcnt lgkmcnt(4)\n\t"
  "s_nop 0\n\t"
  "ds_read_b64_tr_b16 v[180:181], v2 offset:41472\n\t"
  "ds_read_b64_tr_b16 v[182:183], v2 offset:45568\n\t"
  "v_mfma_f32_16x16x32_bf16 v[104:107], v[160:163], a[176:179], v[104:107]\n\t"
  "v_mfma_f32_16x16x32_bf16 v[104:107], v[164:167], a[180:183], v[104:107]\n\t"
  "s_waitcnt lgkmcnt(4)\n\t"
  "s_nop 0\n\t"
  "v_mfma_f32_16x16x32_bf16 v[108:111], v[160:163], a[184:187], v[108:111]\n\t"
  "v_mfma_f32_16x16x32_bf16 v[108:111], v[164:167], a[188:191], v[108:111]\n\t"
  "v_mfma_f32_16x16x32_bf16 v[112:115], v[168:171], a[160:163], v[112:115]\n\t"
  "v_mfma_f32_16x16x32_bf16 v[112:115], v[172:175], a[164:167], v[112:115]\n\t"
  "ds_read_b64_tr_b16 v[160:161], v3 offset:33280\n\t"
  "ds_read_b64_tr_b16 v[162:163], v3 offset:37376\n\t"
  "v_mfma_f32_16x16x32_bf16 v[116:119], v[168:171], a[168:171], v[116:119]\n\t"
  "v_mfma_f32_16x16x32_bf16 v[116:119], v[172:175], a[172:175], v[116:119]\n\t"
  "ds_read_b64_tr_b16 v[164:165], v3 offset:41472\n\t"
  "ds_read_b64_tr_b16 v[166:167], v3 offset:45568\n\t"
  "v_mfma_f32_16x16x32_bf16 v[120:123], v[168:171], a[176:179], v[120:123]\n\t"
  "v_mfma_f32_16x16x32_bf16 v[120:123], v[172:175], a[180:183], v[120:123]\n\t"
  "v_mfma_f32_16x16x32_bf16 v[124:127], v[168:171], a[184:187], v[124:127]\n\t"
  "v_mfma_f32_16x16x32_bf16 v[124:127], v[172:175], a[188:191], v[124:127]\n\t"
  "s_waitcnt vmcnt(0)\n\t"
  "s_barrier\n\t"
  "s_waitcnt lgkmcnt(2)\n\t"
  "s_nop 0\n\t"
  "v_mfma_f32_16x16x32_bf16 v[128:131], v[176:179], a[160:163], v[128:131]\n\t"
  "v_mfma_f32_16x16x32_bf16 v[128:131], v[180:183], a[164:167], v[128:131]\n\t"
  "ds_read_b64_tr_b16 v[168:169], v4 offset:0\n\t"
  "ds_read_b64_tr_b16 v[170:171], v4 offset:4096\n\t"
  "v_mfma_f32_16x16x32_bf16 v[132:135], v[176:179], a[168:171], v[132:135]\n\t"
  "v_mfma_f32_16x16x32_bf16 v[132:135], v[180:183], a[172:175], v[132:135]\n\t"
  "ds_read_b64_tr_b16 v[172:173], v4 offset:8192\n\t"
  "ds_read_b64_tr_b16 v[174:175], v4 offset:12288\n\t"
  "v_mfma_f32_16x16x32_bf16 v[136:139], v[176:179], a[176:179], v[136:139]\n\t"
  "v_mfma_f32_16x16x32_bf16 v[136:139], v[180:183], a[180:183], v[136:139]\n\t"
  "ds_read_b128 a[128:131], v17 offset:0\n\t"
  "ds_read_b128 a[132:135], v17 offset:1024\n\t"
  "v_mfma_f32_16x16x32_bf16 v[140:143], v[176:179], a[184:187], v[140:143]\n\t"
  "v_mfma_f32_16x16x32_bf16 v[140:143], v[180:183], a[188:191], v[140:143]\n\t"
  "ds_read_b128 a[136:139], v17 offset:2048\n\t"
  "ds_read_b128 a[140:143], v17 offset:3072\n\t"
  "v_mfma_f32_16x16x32_bf16 v[144:147], v[160:163], a[160:163], v[144:147]\n\t"
  "s_waitcnt lgkmcnt(4)\n\t"
  "s_nop 0\n\t"
  "v_mfma_f32_16x16x32_bf16 v[144:147], v[164:167], a[164:167], v[144:147]\n\t"
  "ds_read_b64_tr_b16 v[176:177], v5 offset:0\n\t"
  "ds_read_b64_tr_b16 v[178:179], v5 offset:4096\n\t"
  "v_mfma_f32_16x16x32_bf16 v[148:151], v[160:163], a[168:171], v[148:151]\n\t"
  "v_mfma_f32_16x16x32_bf16 v[148:151], v[164:167], a[172:175], v[148:151]\n\t"
  "ds_read_b64_tr_b16 v[180:181], v5 offset:8192\n\t"
  "ds_read_b64_tr_b16 v[182:183], v5 offset:12288\n\t"
  "v_mfma_f32_16x16x32_bf16 v[152:155], v[160:163], a[176:179], v[152:155]\n\t"
  "v_mfma_f32_16x16x32_bf16 v[152:155], v[164:167], a[180:183], v[152:155]\n\t"
  "ds_read_b128 a[144:147], v17 offset:4096\n\t"
  "ds_read_b128 a[148:151], v17 offset:5120\n\t"
  "v_mfma_f32_16x16x32_bf16 v[156:159], v[160:163], a[184:187], v[156:159]\n\t"
  "v_mfma_f32_16x16x32_bf16 v[156:159], v[164:167], a[188:191], v[156:159]\n\t"
  "s_waitcnt lgkmcnt(4)\n\t"
  "s_nop 0\n\t"
  "ds_read_b128 a[152:155], v17 offset:6144\n\t"
  "ds_read_b128 a[156:159], v17 offset:7168\n\t"
  "v_mfma_f32_16x16x32_bf16 v[96:99], v[168:171], a[128:131], v[96:99]\n\t"
  "v_mfma_f32_16x16x32_bf16 v[96:99], v[172:175], a[132:135], v[96:99]\n\t"
  "ds_read_b64_tr_b16 v[160:161], v4 offset:512\n\t"
  "ds_read_b64_tr_b16 v[162:163], v4 offset:4608\n\t"
  "v_mfma_f32_16x16x32_bf16 v[100:103], v[168:171], a[136:139], v[100:103]\n\t"
  "v_mfma_f32_16x16x32_bf16 v[100:103], v[172:175], a[140:143], v[100:103]\n\t"
  "s_waitcnt lgkmcnt(4)\n\t"
  "s_nop 0\n\t"
  "ds_read_b64_tr_b16 v[164:165], v4 offset:8704\n\t"
  "ds_read_b64_tr_b16 v[166:167], v4 offset:12800\n\t"
  "v_mfma_f32_16x16x32_bf16 v[104:107], v[168:171], a[144:147], v[104:107]\n\t"
  "v_mfma_f32_16x16x32_bf16 v[104:107], v[172:175], a[148:151], v[104:107]\n\t"
  "s_waitcnt lgkmcnt(4)\n\t"
  "s_nop 0\n\t"
  "v_mfma_f32_16x16x32_bf16 v[108:111], v[168:171], a[152:155], v[108:111]\n\t"
  "v_mfma_f32_16x16x32_bf16 v[108:111], v[172:175], a[156:159], v[108:111]\n\t"
  "v_mfma_f32_16x16x32_bf16 v[112:115], v[176:179], a[128:131], v[112:115]\n\t"
  "v_mfma_f32_16x16x32_bf16 v[112:115], v[180:183], a[132:135], v[112:115]\n\t"
  "ds_read_b64_tr_b16 v[168:169], v5 offset:512\n\t"
  "ds_read_b64_tr_b16 v[170:171], v5 offset:4608\n\t"
  "v_mfma_f32_16x16x32_bf16 v[116:119], v[176:179], a[136:139], v[116:119]\n\t"
  "v_mfma_f32_16x16x32_bf16 v[116:119], v[180:183], a[140:143], v[116:119]\n\t"
  "ds_read_b64_tr_b16 v[172:173], v5 offset:8704\n\t"
  "ds_read_b64_tr_b16 v[174:175], v5 offset:12800\n\t"
  "v_mfma_f32_16x16x32_bf16 v[120:123], v[176:179], a[144:147], v[120:123]\n\t"
  "v_mfma_f32_16x16x32_bf16 v[120:123], v[180:183], a[148:151], v[120:123]\n\t"
  "v_mfma_f32_16x16x32_bf16 v[124:127], v[176:179], a[152:155], v[124:127]\n\t"
  "v_mfma_f32_16x16x32_bf16 v[124:127], v[180:183], a[156:159], v[124:127]\n\t"
  "s_waitcnt lgkmcnt(2)\n\t"
  "s_nop 0\n\t"
  "v_mfma_f32_16x16x32_bf16 v[128:131], v[160:163], a[128:131], v[128:131]\n\t"
  "v_mfma_f32_16x16x32_bf16 v[128:131], v[164:167], a[132:135], v[128:131]\n\t"
  "v_mfma_f32_16x16x32_bf16 v[132:135], v[160:163], a[136:139], v[132:135]\n\t"
  "v_mfma_f32_16x16x32_bf16 v[132:135], v[164:167], a[140:143], v[132:135]\n\t"
  "v_mfma_f32_16x16x32_bf16 v[136:139], v[160:163], a[144:147], v[136:139]\n\t"
  "v_mfma_f32_16x16x32_bf16 v[136:139], v[164:167], a[148:151], v[136:139]\n\t"
  "v_mfma_f32_16x16x32_bf16 v[140:143], v[160:163], a[152:155], v[140:143]\n\t"
  "v_mfma_f32_16x16x32_bf16 v[140:143], v[164:167], a[156:159], v[140:143]\n\t"
  "v_mfma_f32_16x16x32_bf16 v[144:147], v[168:171], a[128:131], v[144:147]\n\t"
  "s_waitcnt lgkmcnt(0)\n\t"
  "s_nop 0\n\t"
  "v_mfma_f32_16x16x32_bf16 v[144:147], v[172:175], a[132:135], v[144:147]\n\t"
  "v_mfma_f32_16x16x32_bf16 v[148:151], v[168:171], a[136:139], v[148:151]\n\t"
  "v_mfma_f32_16x16x32_bf16 v[148:151], v[172:175], a[140:143], v[148:151]\n\t"
  "v_mfma_f32_16x16x32_bf16 v[152:155], v[168:171], a[144:147], v[152:155]\n\t"
  "v_mfma_f32_16x16x32_bf16 v[152:155], v[172:175], a[148:151], v[152:155]\n\t"
  "v_mfma_f32_16x16x32_bf16 v[156:159], v[168:171], a[152:155], v[156:159]\n\t"
  "v_mfma_f32_16x16x32_bf16 v[156:159], v[172:175], a[156:159], v[156:159]\n\t"
  "s_nop 7\n\t"
  "s_nop 0\n\t"
  "v_cvt_pk_bf16_f32 v96, v96, v97\n\t"
  "v_cvt_pk_bf16_f32 v97, v98, v99\n\t"
  "v_cvt_pk_bf16_f32 v98, v112, v113\n\t"
  "v_cvt_pk_bf16_f32 v99, v114, v115\n\t"
  "v_cvt_pk_bf16_f32 v128, v128, v129\n\t"
  "v_cvt_pk_bf16_f32 v129, v130, v131\n\t"
  "v_cvt_pk_bf16_f32 v130, v144, v145\n\t"
  "v_cvt_pk_bf16_f32 v131, v146, v147\n\t"
  "v_cvt_pk_bf16_f32 v100, v100, v101\n\t"
  "v_cvt_pk_bf16_f32 v101, v102, v103\n\t"
  "v_cvt_pk_bf16_f32 v102, v116, v117\n\t"
  "v_cvt_pk_bf16_f32 v103, v118, v119\n\t"
  "v_cvt_pk_bf16_f32 v132, v132, v133\n\t"
  "v_cvt_pk_bf16_f32 v133, v134, v135\n\t"
  "v_cvt_pk_bf16_f32 v134, v148, v149\n\t"
  "v_cvt_pk_bf16_f32 v135, v150, v151\n\t"
  "v_cvt_pk_bf16_f32 v104, v104, v105\n\t"
  "v_cvt_pk_bf16_f32 v105, v106, v107\n\t"
  "v_cvt_pk_bf16_f32 v106, v120, v121\n\t"
  "v_cvt_pk_bf16_f32 v107, v122, v123\n\t"
  "v_cvt_pk_bf16_f32 v136, v136, v137\n\t"
  "v_cvt_pk_bf16_f32 v137, v138, v139\n\t"
  "v_cvt_pk_bf16_f32 v138, v152, v153\n\t"
  "v_cvt_pk_bf16_f32 v139, v154, v155\n\t"
  "v_cvt_pk_bf16_f32 v108, v108, v109\n\t"
  "v_cvt_pk_bf16_f32 v109, v110, v111\n\t"
  "v_cvt_pk_bf16_f32 v110, v124, v125\n\t"
  "v_cvt_pk_bf16_f32 v111, v126, v127\n\t"
  "v_cvt_pk_bf16_f32 v140, v140, v141\n\t"
  "v_cvt_pk_bf16_f32 v141, v142, v143\n\t"
  "v_cvt_pk_bf16_f32 v142, v156, v157\n\t"
  "v_cvt_pk_bf16_f32 v143, v158, v159\n\t"
  "s_waitcnt lgkmcnt(0)\n\t"
  "s_add_u32 s30, s37, 0\n\t"
  "s_cmp_eq_u32 s30, %11\n\t"
  "s_cbranch_scc1 L_ownb0_%=\n\t"
  "s_add_u32 s30, s36, 8192\n\t"
  "buffer_store_dwordx4 v[96:99], v1, s[20:23], s30 offen offset:0 sc1\n\t"
  "buffer_store_dwordx4 v[128:131], v1, s[20:23], s30 offen offset:1024 sc1\n\t"
  "s_add_u32 s30, s36, 12288\n\t"
  "buffer_store_dwordx4 v[100:103], v1, s[20:23], s30 offen offset:0 sc1\n\t"
  "buffer_store_dwordx4 v[132:135], v1, s[20:23], s30 offen offset:1024 sc1\n\t"
  "s_branch L_endb0_%=\n\t"
  "L_ownb0_%=:\n\t"
  "s_nop 0\n\t"
  "ds_write_b128 v14, v[96:99] offset:8192\n\t"
  "ds_write_b128 v14, v[128:131] offset:9216\n\t"
  "ds_write_b128 v14, v[100:103] offset:12288\n\t"
  "ds_write_b128 v14, v[132:135] offset:13312\n\t"
  "L_endb0_%=:\n\t"
  "s_add_u32 s30, s37, 1\n\t"
  "s_cmp_eq_u32 s30, %11\n\t"
  "s_cbranch_scc1 L_ownb1_%=\n\t"
  "s_add_u32 s30, s36, 73728\n\t"
  "s_nop 0\n\t"
  "buffer_store_dwordx4 v[104:107], v1, s[20:23], s30 offen offset:0 sc1\n\t"
  "buffer_store_dwordx4 v[136:139], v1, s[20:23], s30 offen offset:1024 sc1\n\t"
  "s_add_u32 s30, s36, 77824\n\t"
  "buffer_store_dwordx4 v[108:111], v1, s[20:23], s30 offen offset:0 sc1\n\t"
  "buffer_store_dwordx4 v[140:143], v1, s[20:23], s30 offen offset:1024 sc1\n\t"
  "s_branch L_endb1_%=\n\t"
  "L_ownb1_%=:\n\t"
  "s_nop 0\n\t"
  "ds_write_b128 v14, v[104:107] offset:8192\n\t"
  "ds_write_b128 v14, v[136:139] offset:9216\n\t"
  "ds_write_b128 v14, v[108:111] offset:12288\n\t"
  "ds_write_b128 v14, v[140:143] offset:13312\n\t"
  "L_endb1_%=:\n\t"
  "s_waitcnt lgkmcnt(0)\n\t"
  "s_waitcnt vmcnt(0) lgkmcnt(0)\n\t"
  "s_branch L_done_%=\n\t"
  "L_fb_%=:\n\t"
  "s_lshl_b32 s63, %8, 19\n\t"
  "s_add_u32 s52, s70, s63\n\t"
  "s_addc_u32 s53, s71, 0\n\t"
  "s_mov_b32 s54, 0x80000\n\t"
  "s_mov_b32 s55, 0x20000\n\t"
  "s_add_u32 s56, s72, s63\n\t"
  "s_addc_u32 s57, s73, 0\n\t"
  "s_sub_u32 s56, s56, 0x400\n\t"
  "s_subb_u32 s57, s57, 0\n\t"
  "s_mov_b32 s58, 0x80400\n\t"
  "s_mov_b32 s59, 0x20000\n\t"
  "s_lshl_b32 s62, %7, 13\n\t"
  "s_add_u32 s62, s62, 131072\n\t"
  "v_mbcnt_lo_u32_b32 v76, -1, 0\n\t"
  "v_mbcnt_hi_u32_b32 v76, -1, v76\n\t"
  "v_and_b32_e32 v74, 15, v76\n\t"
  "v_lshrrev_b32_e32 v75, 4, v76\n\t"
  "v_lshlrev_b32_e32 v64, 8, v74\n\t"
  "v_lshl_add_u32 v64, v75, 4, v64\n\t"
  "v_add_u32_e32 v65, 0x1000, v64\n\t"
  "v_bfe_u32 v74, v76, 2, 3\n\t"
  "v_lshrrev_b32_e32 v75, 2, v74\n\t"
  "v_lshlrev_b32_e32 v75, 1, v75\n\t"
  "v_and_b32_e32 v77, 3, v76\n\t"
  "v_xor_b32_e32 v75, v75, v77\n\t"
  "v_lshlrev_b32_e32 v75, 4, v75\n\t"
  "v_lshl_add_u32 v75, v74, 8, v75\n\t"
  "v_lshrrev_b32_e32 v77, 5, v76\n\t"
  "v_lshl_add_u32 v75, v77, 6, v75\n\t"
  "v_add_u32_e32 v66, 0x400, v75\n\t"
  "v_add_u32_e32 v67, 0x80, v75\n\t"
  "s_sub_u32 s63, s62, 65536\n\t"
  "v_add_u32_e32 v68, s63, v6\n\t"
  "v_add_u32_e32 v69, s63, v7\n\t"
  "v_accvgpr_write_b32 a0, 0\n\t"
  "v_accvgpr_write_b32 a1, 0\n\t"
  "v_accvgpr_write_b32 a2, 0\n\t"
  "v_accvgpr_write_b32 a3, 0\n\t"
  "v_accvgpr_write_b32 a4, 0\n\t"
  "v_accvgpr_write_b32 a5, 0\n\t"
  "v_accvgpr_write_b32 a6, 0\n\t"
  "v_accvgpr_write_b32 a7, 0\n\t"
  "v_accvgpr_write_b32 a8, 0\n\t"
  "v_accvgpr_write_b32 a9, 0\n\t"
  "v_accvgpr_write_b32 a10, 0\n\t"
  "v_accvgpr_write_b32 a11, 0\n\t"
  "v_accvgpr_write_b32 a12, 0\n\t"
  "v_accvgpr_write_b32 a13, 0\n\t"
  "v_accvgpr_write_b32 a14, 0\n\t"
  "v_accvgpr_write_b32 a15, 0\n\t"
  "v_accvgpr_write_b32 a16, 0\n\t"
  "v_accvgpr_write_b32 a17, 0\n\t"
  "v_accvgpr_write_b32 a18, 0\n\t"
  "v_accvgpr_write_b32 a19, 0\n\t"
  "v_accvgpr_write_b32 a20, 0\n\t"
  "v_accvgpr_write_b32 a21, 0\n\t"
  "v_accvgpr_write_b32 a22, 0\n\t"
  "v_accvgpr_write_b32 a23, 0\n\t"
  "v_accvgpr_write_b32 a24, 0\n\t"
  "v_accvgpr_write_b32 a25, 0\n\t"
  "v_accvgpr_write_b32 a26, 0\n\t"
  "v_accvgpr_write_b32 a27, 0\n\t"
  "v_accvgpr_write_b32 a28, 0\n\t"
  "v_accvgpr_write_b32 a29, 0\n\t"
  "v_accvgpr_write_b32 a30, 0\n\t"
  "v_accvgpr_write_b32 a31, 0\n\t"
  "v_accvgpr_write_b32 a32, 0\n\t"
  "v_accvgpr_write_b32 a33, 0\n\t"
  "v_accvgpr_write_b32 a34, 0\n\t"
  "v_accvgpr_write_b32 a35, 0\n\t"
  "v_accvgpr_write_b32 a36, 0\n\t"
  "v_accvgpr_write_b32 a37, 0\n\t"
  "v_accvgpr_write_b32 a38, 0\n\t"
  "v_accvgpr_write_b32 a39, 0\n\t"
  "v_accvgpr_write_b32 a40, 0\n\t"
  "v_accvgpr_write_b32 a41, 0\n\t"
  "v_accvgpr_write_b32 a42, 0\n\t"
  "v_accvgpr_write_b32 a43, 0\n\t"
  "v_accvgpr_write_b32 a44, 0\n\t"
  "v_accvgpr_write_b32 a45, 0\n\t"
  "v_accvgpr_write_b32 a46, 0\n\t"
  "v_accvgpr_write_b32 a47, 0\n\t"
  "v_accvgpr_write_b32 a48, 0\n\t"
  "v_accvgpr_write_b32 a49, 0\n\t"
  "v_accvgpr_write_b32 a50, 0\n\t"
  "v_accvgpr_write_b32 a51, 0\n\t"
  "v_accvgpr_write_b32 a52, 0\n\t"
  "v_accvgpr_write_b32 a53, 0\n\t"
  "v_accvgpr_write_b32 a54, 0\n\t"
  "v_accvgpr_write_b32 a55, 0\n\t"
  "v_accvgpr_write_b32 a56, 0\n\t"
  "v_accvgpr_write_b32 a57, 0\n\t"
  "v_accvgpr_write_b32 a58, 0\n\t"
  "v_accvgpr_write_b32 a59, 0\n\t"
  "v_accvgpr_write_b32 a60, 0\n\t"
  "v_accvgpr_write_b32 a61, 0\n\t"
  "v_accvgpr_write_b32 a62, 0\n\t"
  "v_accvgpr_write_b32 a63, 0\n\t"
  "v_accvgpr_write_b32 a64, 0\n\t"
  "v_accvgpr_write_b32 a65, 0\n\t"
  "v_accvgpr_write_b32 a66, 0\n\t"
  "v_accvgpr_write_b32 a67, 0\n\t"
  "v_accvgpr_write_b32 a68, 0\n\t"
  "v_accvgpr_write_b32 a69, 0\n\t"
  "v_accvgpr_write_b32 a70, 0\n\t"
  "v_accvgpr_write_b32 a71, 0\n\t"
  "v_accvgpr_write_b32 a72, 0\n\t"
  "v_accvgpr_write_b32 a73, 0\n\t"
  "v_accvgpr_write_b32 a74, 0\n\t"
  "v_accvgpr_write_b32 a75, 0\n\t"
  "v_accvgpr_write_b32 a76, 0\n\t"
  "v_accvgpr_write_b32 a77, 0\n\t"
  "v_accvgpr_write_b32 a78, 0\n\t"
  "v_accvgpr_write_b32 a79, 0\n\t"
  "v_accvgpr_write_b32 a80, 0\n\t"
  "v_accvgpr_write_b32 a81, 0\n\t"
  "v_accvgpr_write_b32 a82, 0\n\t"
  "v_accvgpr_write_b32 a83, 0\n\t"
  "v_accvgpr_write_b32 a84, 0\n\t"
  "v_accvgpr_write_b32 a85, 0\n\t"
  "v_accvgpr_write_b32 a86, 0\n\t"
  "v_accvgpr_write_b32 a87, 0\n\t"
  "v_accvgpr_write_b32 a88, 0\n\t"
  "v_accvgpr_write_b32 a89, 0\n\t"
  "v_accvgpr_write_b32 a90, 0\n\t"
  "v_accvgpr_write_b32 a91, 0\n\t"
  "v_accvgpr_write_b32 a92, 0\n\t"
  "v_accvgpr_write_b32 a93, 0\n\t"
  "v_accvgpr_write_b32 a94, 0\n\t"
  "v_accvgpr_write_b32 a95, 0\n\t"
  "v_accvgpr_write_b32 a96, 0\n\t"
  "v_accvgpr_write_b32 a97, 0\n\t"
  "v_accvgpr_write_b32 a98, 0\n\t"
  "v_accvgpr_write_b32 a99, 0\n\t"
  "v_accvgpr_write_b32 a100, 0\n\t"
  "v_accvgpr_write_b32 a101, 0\n\t"
  "v_accvgpr_write_b32 a102, 0\n\t"
  "v_accvgpr_write_b32 a103, 0\n\t"
  "v_accvgpr_write_b32 a104, 0\n\t"
  "v_accvgpr_write_b32 a105, 0\n\t"
  "v_accvgpr_write_b32 a106, 0\n\t"
  "v_accvgpr_write_b32 a107, 0\n\t"
  "v_accvgpr_write_b32 a108, 0\n\t"
  "v_accvgpr_write_b32 a109, 0\n\t"
  "v_accvgpr_write_b32 a110, 0\n\t"
  "v_accvgpr_write_b32 a111, 0\n\t"
  "v_accvgpr_write_b32 a112, 0\n\t"
  "v_accvgpr_write_b32 a113, 0\n\t"
  "v_accvgpr_write_b32 a114, 0\n\t"
  "v_accvgpr_write_b32 a115, 0\n\t"
  "v_accvgpr_write_b32 a116, 0\n\t"
  "v_accvgpr_write_b32 a117, 0\n\t"
  "v_accvgpr_write_b32 a118, 0\n\t"
  "v_accvgpr_write_b32 a119, 0\n\t"
  "v_accvgpr_write_b32 a120, 0\n\t"
  "v_accvgpr_write_b32 a121, 0\n\t"
  "v_accvgpr_write_b32 a122, 0\n\t"
  "v_accvgpr_write_b32 a123, 0\n\t"
  "v_accvgpr_write_b32 a124, 0\n\t"
  "v_accvgpr_write_b32 a125, 0\n\t"
  "v_accvgpr_write_b32 a126, 0\n\t"
  "v_accvgpr_write_b32 a127, 0\n\t"
  "v_accvgpr_write_b32 a192, 0\n\t"
  "v_accvgpr_write_b32 a193, 0\n\t"
  "v_accvgpr_write_b32 a194, 0\n\t"
  "v_accvgpr_write_b32 a195, 0\n\t"
  "v_accvgpr_write_b32 a196, 0\n\t"
  "v_accvgpr_write_b32 a197, 0\n\t"
  "v_accvgpr_write_b32 a198, 0\n\t"
  "v_accvgpr_write_b32 a199, 0\n\t"
  "v_accvgpr_write_b32 a200, 0\n\t"
  "v_accvgpr_write_b32 a201, 0\n\t"
  "v_accvgpr_write_b32 a202, 0\n\t"
  "v_accvgpr_write_b32 a203, 0\n\t"
  "v_accvgpr_write_b32 a204, 0\n\t"
  "v_accvgpr_write_b32 a205, 0\n\t"
  "v_accvgpr_write_b32 a206, 0\n\t"
  "v_accvgpr_write_b32 a207, 0\n\t"
  "v_mov_b32_e32 v70, 0xff7fffff\n\t"
  "v_mov_b32_e32 v71, 0xff7fffff\n\t"
  "v_mov_b32_e32 v72, 0xff7fffff\n\t"
  "v_mov_b32_e32 v73, 0xff7fffff\n\t"
  "s_mov_b32 s60, 0\n\t"
  "s_mov_b32 s61, 64\n\t"
  "L_fbloop_%=:\n\t"
  "buffer_load_dwordx4 a[128:131], v64, s[52:55], s60 offen offset:0\n\t"
  "buffer_load_dwordx4 a[132:135], v64, s[52:55], s60 offen offset:64\n\t"
  "buffer_load_dwordx4 a[136:139], v64, s[52:55], s60 offen offset:128\n\t"
  "buffer_load_dwordx4 a[140:143], v64, s[52:55], s60 offen offset:192\n\t"
  "buffer_load_dwordx4 a[144:147], v65, s[52:55], s60 offen offset:0\n\t"
  "buffer_load_dwordx4 a[148:151], v65, s[52:55], s60 offen offset:64\n\t"
  "buffer_load_dwordx4 a[152:155], v65, s[52:55], s60 offen offset:128\n\t"
  "buffer_load_dwordx4 a[156:159], v65, s[52:55], s60 offen offset:192\n\t"
  "s_add_u32 m0, s62, 0\n\t"
  "s_add_u32 s63, s60, 0\n\t"
  "buffer_load_dwordx4 v66, s[56:59], s63 offen offset:0 lds\n\t"
  "buffer_load_dwordx4 v67, s[56:59], s63 offen offset:1024 lds\n\t"
  "buffer_load_dwordx4 v66, s[56:59], s63 offen offset:2048 lds\n\t"
  "buffer_load_dwordx4 v67, s[56:59], s63 offen offset:3072 lds\n\t"
  "s_add_u32 m0, s62, 4096\n\t"
  "s_add_u32 s63, s60, 4096\n\t"
  "buffer_load_dwordx4 v66, s[56:59], s63 offen offset:0 lds\n\t"
  "buffer_load_dwordx4 v67, s[56:59], s63 offen offset:1024 lds\n\t"
  "buffer_load_dwordx4 v66, s[56:59], s63 offen offset:2048 lds\n\t"
  "buffer_load_dwordx4 v67, s[56:59], s63 offen offset:3072 lds\n\t"
  "s_waitcnt vmcnt(0)\n\t"
  "ds_read_b64_tr_b16 a[160:161], v68 offset:0\n\t"
  "ds_read_b64_tr_b16 a[162:163], v68 offset:4096\n\t"
  "ds_read_b64_tr_b16 a[164:165], v69 offset:0\n\t"
  "ds_read_b64_tr_b16 a[166:167], v69 offset:4096\n\t"
  "ds_read_b64_tr_b16 a[168:169], v68 offset:512\n\t"
  "ds_read_b64_tr_b16 a[170:171], v68 offset:4608\n\t"
  "ds_read_b64_tr_b16 a[172:173], v69 offset:512\n\t"
  "ds_read_b64_tr_b16 a[174:175], v69 offset:4608\n\t"
  "ds_read_b64_tr_b16 a[176:177], v68 offset:1024\n\t"
  "ds_read_b64_tr_b16 a[178:179], v68 offset:5120\n\t"
  "ds_read_b64_tr_b16 a[180:181], v69 offset:1024\n\t"
  "ds_read_b64_tr_b16 a[182:183], v69 offset:5120\n\t"
  "ds_read_b64_tr_b16 a[184:185], v68 offset:1536\n\t"
  "ds_read_b64_tr_b16 a[186:187], v68 offset:5632\n\t"
  "ds_read_b64_tr_b16 a[188:189], v69 offset:1536\n\t"
  "ds_read_b64_tr_b16 a[190:191], v69 offset:5632\n\t"
  "v_mfma_f32_16x16x32_bf16 v[32:35], a[128:131], v[160:163], 0\n\t"
  "v_mfma_f32_16x16x32_bf16 v[32:35], a[132:135], v[164:167], v[32:35]\n\t"
  "v_mfma_f32_16x16x32_bf16 v[32:35], a[136:139], v[168:171], v[32:35]\n\t"
  "v_mfma_f32_16x16x32_bf16 v[32:35], a[140:143], v[172:175], v[32:35]\n\t"
  "v_mfma_f32_16x16x32_bf16 v[36:39], a[144:147], v[160:163], 0\n\t"
  "v_mfma_f32_16x16x32_bf16 v[36:39], a[148:151], v[164:167], v[36:39]\n\t"
  "v_mfma_f32_16x16x32_bf16 v[36:39], a[152:155], v[168:171], v[36:39]\n\t"
  "v_mfma_f32_16x16x32_bf16 v[36:39], a[156:159], v[172:175], v[36:39]\n\t"
  "v_mfma_f32_16x16x32_bf16 v[40:43], a[128:131], v[176:179], 0\n\t"
  "v_mfma_f32_16x16x32_bf16 v[40:43], a[132:135], v[180:183], v[40:43]\n\t"
  "v_mfma_f32_16x16x32_bf16 v[40:43], a[136:139], v[184:187], v[40:43]\n\t"
  "v_mfma_f32_16x16x32_bf16 v[40:43], a[140:143], v[188:191], v[40:43]\n\t"
  "v_mfma_f32_16x16x32_bf16 v[44:47], a[144:147], v[176:179], 0\n\t"
  "v_mfma_f32_16x16x32_bf16 v[44:47], a[148:151], v[180:183], v[44:47]\n\t"
  "v_mfma_f32_16x16x32_bf16 v[44:47], a[152:155], v[184:187], v[44:47]\n\t"
  "v_mfma_f32_16x16x32_bf16 v[44:47], a[156:159], v[188:191], v[44:47]\n\t"
  "v_mfma_f32_16x16x32_bf16 v[48:51], a[128:131], v[192:195], 0\n\t"
  "v_mfma_f32_16x16x32_bf16 v[48:51], a[132:135], v[196:199], v[48:51]\n\t"
  "v_mfma_f32_16x16x32_bf16 v[48:51], a[136:139], v[200:203], v[48:51]\n\t"
  "v_mfma_f32_16x16x32_bf16 v[48:51], a[140:143], v[204:207], v[48:51]\n\t"
  "v_mfma_f32_16x16x32_bf16 v[52:55], a[144:147], v[192:195], 0\n\t"
  "v_mfma_f32_16x16x32_bf16 v[52:55], a[148:151], v[196:199], v[52:55]\n\t"
  "v_mfma_f32_16x16x32_bf16 v[52:55], a[152:155], v[200:203], v[52:55]\n\t"
  "v_mfma_f32_16x16x32_bf16 v[52:55], a[156:159], v[204:207], v[52:55]\n\t"
  "v_mfma_f32_16x16x32_bf16 v[56:59], a[128:131], v[208:211], 0\n\t"
  "v_mfma_f32_16x16x32_bf16 v[56:59], a[132:135], v[212:215], v[56:59]\n\t"
  "v_mfma_f32_16x16x32_bf16 v[56:59], a[136:139], v[216:219], v[56:59]\n\t"
  "v_mfma_f32_16x16x32_bf16 v[56:59], a[140:143], v[220:223], v[56:59]\n\t"
  "v_mfma_f32_16x16x32_bf16 v[60:63], a[144:147], v[208:211], 0\n\t"
  "v_mfma_f32_16x16x32_bf16 v[60:63], a[148:151], v[212:215], v[60:63]\n\t"
  "v_mfma_f32_16x16x32_bf16 v[60:63], a[152:155], v[216:219], v[60:63]\n\t"
  "v_mfma_f32_16x16x32_bf16 v[60:63], a[156:159], v[220:223], v[60:63]\n\t"
  "v_max3_f32 v74, v32, v33, v34\n\t"
  "v_max3_f32 v74, v74, v35, v36\n\t"
  "v_max3_f32 v74, v74, v37, v38\n\t"
  "v_max_f32_e32 v74, v74, v39\n\t"
  "v_mov_b32_e32 v75, v74\n\t"
  "s_nop 1\n\t"
  "v_permlane16_swap_b32_e32 v74, v75\n\t"
  "v_max_f32_e32 v74, v74, v75\n\t"
  "v_mov_b32_e32 v75, v74\n\t"
  "s_nop 1\n\t"
  "v_permlane32_swap_b32_e32 v74, v75\n\t"
  "v_max_f32_e32 v74, v74, v75\n\t"
  "v_max_f32_e32 v74, v74, v70\n\t"
  "v_sub_f32_e32 v77, v70, v74\n\t"
  "v_exp_f32_e32 v77, v77\n\t"
  "v_mov_b32_e32 v70, v74\n\t"
  "v_sub_f32_e32 v32, v32, v74\n\t"
  "v_sub_f32_e32 v33, v33, v74\n\t"
  "v_sub_f32_e32 v34, v34, v74\n\t"
  "v_sub_f32_e32 v35, v35, v74\n\t"
  "v_sub_f32_e32 v36, v36, v74\n\t"
  "v_sub_f32_e32 v37, v37, v74\n\t"
  "v_sub_f32_e32 v38, v38, v74\n\t"
  "v_sub_f32_e32 v39, v39, v74\n\t"
  "v_exp_f32_e32 v32, v32\n\t"
  "v_exp_f32_e32 v33, v33\n\t"
  "v_exp_f32_e32 v34, v34\n\t"
  "v_exp_f32_e32 v35, v35\n\t"
  "v_exp_f32_e32 v36, v36\n\t"
  "v_exp_f32_e32 v37, v37\n\t"
  "v_exp_f32_e32 v38, v38\n\t"
  "v_exp_f32_e32 v39, v39\n\t"
  "v_cvt_pk_bf16_f32 v96, v32, v33\n\t"
  "v_cvt_pk_bf16_f32 v97, v34, v35\n\t"
  "v_cvt_pk_bf16_f32 v98, v36, v37\n\t"
  "v_cvt_pk_bf16_f32 v99, v38, v39\n\t"
  "v_accvgpr_read_b32 v75, a0\n\t"
  "v_mul_f32_e32 v75, v75, v77\n\t"
  "v_accvgpr_write_b32 a0, v75\n\t"
  "v_accvgpr_read_b32 v75, a1\n\t"
  "v_mul_f32_e32 v75, v75, v77\n\t"
  "v_accvgpr_write_b32 a1, v75\n\t"
  "v_accvgpr_read_b32 v75, a2\n\t"
  "v_mul_f32_e32 v75, v75, v77\n\t"
  "v_accvgpr_write_b32 a2, v75\n\t"
  "v_accvgpr_read_b32 v75, a3\n\t"
  "v_mul_f32_e32 v75, v75, v77\n\t"
  "v_accvgpr_write_b32 a3, v75\n\t"
  "v_accvgpr_read_b32 v75, a4\n\t"
  "v_mul_f32_e32 v75, v75, v77\n\t"
  "v_accvgpr_write_b32 a4, v75\n\t"
  "v_accvgpr_read_b32 v75, a5\n\t"
  "v_mul_f32_e32 v75, v75, v77\n\t"
  "v_accvgpr_write_b32 a5, v75\n\t"
  "v_accvgpr_read_b32 v75, a6\n\t"
  "v_mul_f32_e32 v75, v75, v77\n\t"
  "v_accvgpr_write_b32 a6, v75\n\t"
  "v_accvgpr_read_b32 v75, a7\n\t"
  "v_mul_f32_e32 v75, v75, v77\n\t"
  "v_accvgpr_write_b32 a7, v75\n\t"
  "v_accvgpr_read_b32 v75, a8\n\t"
  "v_mul_f32_e32 v75, v75, v77\n\t"
  "v_accvgpr_write_b32 a8, v75\n\t"
  "v_accvgpr_read_b32 v75, a9\n\t"
  "v_mul_f32_e32 v75, v75, v77\n\t"
  "v_accvgpr_write_b32 a9, v75\n\t"
  "v_accvgpr_read_b32 v75, a10\n\t"
  "v_mul_f32_e32 v75, v75, v77\n\t"
  "v_accvgpr_write_b32 a10, v75\n\t"
  "v_accvgpr_read_b32 v75, a11\n\t"
  "v_mul_f32_e32 v75, v75, v77\n\t"
  "v_accvgpr_write_b32 a11, v75\n\t"
  "v_accvgpr_read_b32 v75, a12\n\t"
  "v_mul_f32_e32 v75, v75, v77\n\t"
  "v_accvgpr_write_b32 a12, v75\n\t"
  "v_accvgpr_read_b32 v75, a13\n\t"
  "v_mul_f32_e32 v75, v75, v77\n\t"
  "v_accvgpr_write_b32 a13, v75\n\t"
  "v_accvgpr_read_b32 v75, a14\n\t"
  "v_mul_f32_e32 v75, v75, v77\n\t"
  "v_accvgpr_write_b32 a14, v75\n\t"
  "v_accvgpr_read_b32 v75, a15\n\t"
  "v_mul_f32_e32 v75, v75, v77\n\t"
  "v_accvgpr_write_b32 a15, v75\n\t"
  "v_accvgpr_read_b32 v75, a16\n\t"
  "v_mul_f32_e32 v75, v75, v77\n\t"
  "v_accvgpr_write_b32 a16, v75\n\t"
  "v_accvgpr_read_b32 v75, a17\n\t"
  "v_mul_f32_e32 v75, v75, v77\n\t"
  "v_accvgpr_write_b32 a17, v75\n\t"
  "v_accvgpr_read_b32 v75, a18\n\t"
  "v_mul_f32_e32 v75, v75, v77\n\t"
  "v_accvgpr_write_b32 a18, v75\n\t"
  "v_accvgpr_read_b32 v75, a19\n\t"
  "v_mul_f32_e32 v75, v75, v77\n\t"
  "v_accvgpr_write_b32 a19, v75\n\t"
  "v_accvgpr_read_b32 v75, a20\n\t"
  "v_mul_f32_e32 v75, v75, v77\n\t"
  "v_accvgpr_write_b32 a20, v75\n\t"
  "v_accvgpr_read_b32 v75, a21\n\t"
  "v_mul_f32_e32 v75, v75, v77\n\t"
  "v_accvgpr_write_b32 a21, v75\n\t"
  "v_accvgpr_read_b32 v75, a22\n\t"
  "v_mul_f32_e32 v75, v75, v77\n\t"
  "v_accvgpr_write_b32 a22, v75\n\t"
  "v_accvgpr_read_b32 v75, a23\n\t"
  "v_mul_f32_e32 v75, v75, v77\n\t"
  "v_accvgpr_write_b32 a23, v75\n\t"
  "v_accvgpr_read_b32 v75, a24\n\t"
  "v_mul_f32_e32 v75, v75, v77\n\t"
  "v_accvgpr_write_b32 a24, v75\n\t"
  "v_accvgpr_read_b32 v75, a25\n\t"
  "v_mul_f32_e32 v75, v75, v77\n\t"
  "v_accvgpr_write_b32 a25, v75\n\t"
  "v_accvgpr_read_b32 v75, a26\n\t"
  "v_mul_f32_e32 v75, v75, v77\n\t"
  "v_accvgpr_write_b32 a26, v75\n\t"
  "v_accvgpr_read_b32 v75, a27\n\t"
  "v_mul_f32_e32 v75, v75, v77\n\t"
  "v_accvgpr_write_b32 a27, v75\n\t"
  "v_accvgpr_read_b32 v75, a28\n\t"
  "v_mul_f32_e32 v75, v75, v77\n\t"
  "v_accvgpr_write_b32 a28, v75\n\t"
  "v_accvgpr_read_b32 v75, a29\n\t"
  "v_mul_f32_e32 v75, v75, v77\n\t"
  "v_accvgpr_write_b32 a29, v75\n\t"
  "v_accvgpr_read_b32 v75, a30\n\t"
  "v_mul_f32_e32 v75, v75, v77\n\t"
  "v_accvgpr_write_b32 a30, v75\n\t"
  "v_accvgpr_read_b32 v75, a31\n\t"
  "v_mul_f32_e32 v75, v75, v77\n\t"
  "v_accvgpr_write_b32 a31, v75\n\t"
  "v_accvgpr_read_b32 v75, a192\n\t"
  "v_mul_f32_e32 v75, v75, v77\n\t"
  "v_accvgpr_write_b32 a192, v75\n\t"
  "v_accvgpr_read_b32 v75, a193\n\t"
  "v_mul_f32_e32 v75, v75, v77\n\t"
  "v_accvgpr_write_b32 a193, v75\n\t"
  "v_accvgpr_read_b32 v75, a194\n\t"
  "v_mul_f32_e32 v75, v75, v77\n\t"
  "v_accvgpr_write_b32 a194, v75\n\t"
  "v_accvgpr_read_b32 v75, a195\n\t"
  "v_mul_f32_e32 v75, v75, v77\n\t"
  "v_accvgpr_write_b32 a195, v75\n\t"
  "v_max3_f32 v74, v40, v41, v42\n\t"
  "v_max3_f32 v74, v74, v43, v44\n\t"
  "v_max3_f32 v74, v74, v45, v46\n\t"
  "v_max_f32_e32 v74, v74, v47\n\t"
  "v_mov_b32_e32 v75, v74\n\t"
  "s_nop 1\n\t"
  "v_permlane16_swap_b32_e32 v74, v75\n\t"
  "v_max_f32_e32 v74, v74, v75\n\t"
  "v_mov_b32_e32 v75, v74\n\t"
  "s_nop 1\n\t"
  "v_permlane32_swap_b32_e32 v74, v75\n\t"
  "v_max_f32_e32 v74, v74, v75\n\t"
  "v_max_f32_e32 v74, v74, v71\n\t"
  "v_sub_f32_e32 v77, v71, v74\n\t"
  "v_exp_f32_e32 v77, v77\n\t"
  "v_mov_b32_e32 v71, v74\n\t"
  "v_sub_f32_e32 v40, v40, v74\n\t"
  "v_sub_f32_e32 v41, v41, v74\n\t"
  "v_sub_f32_e32 v42, v42, v74\n\t"
  "v_sub_f32_e32 v43, v43, v74\n\t"
  "v_sub_f32_e32 v44, v44, v74\n\t"
  "v_sub_f32_e32 v45, v45, v74\n\t"
  "v_sub_f32_e32 v46, v46, v74\n\t"
  "v_sub_f32_e32 v47, v47, v74\n\t"
  "v_exp_f32_e32 v40, v40\n\t"
  "v_exp_f32_e32 v41, v41\n\t"
  "v_exp_f32_e32 v42, v42\n\t"
  "v_exp_f32_e32 v43, v43\n\t"
  "v_exp_f32_e32 v44, v44\n\t"
  "v_exp_f32_e32 v45, v45\n\t"
  "v_exp_f32_e32 v46, v46\n\t"
  "v_exp_f32_e32 v47, v47\n\t"
  "v_cvt_pk_bf16_f32 v100, v40, v41\n\t"
  "v_cvt_pk_bf16_f32 v101, v42, v43\n\t"
  "v_cvt_pk_bf16_f32 v102, v44, v45\n\t"
  "v_cvt_pk_bf16_f32 v103, v46, v47\n\t"
  "v_accvgpr_read_b32 v75, a32\n\t"
  "v_mul_f32_e32 v75, v75, v77\n\t"
  "v_accvgpr_write_b32 a32, v75\n\t"
  "v_accvgpr_read_b32 v75, a33\n\t"
  "v_mul_f32_e32 v75, v75, v77\n\t"
  "v_accvgpr_write_b32 a33, v75\n\t"
  "v_accvgpr_read_b32 v75, a34\n\t"
  "v_mul_f32_e32 v75, v75, v77\n\t"
  "v_accvgpr_write_b32 a34, v75\n\t"
  "v_accvgpr_read_b32 v75, a35\n\t"
  "v_mul_f32_e32 v75, v75, v77\n\t"
  "v_accvgpr_write_b32 a35, v75\n\t"
  "v_accvgpr_read_b32 v75, a36\n\t"
  "v_mul_f32_e32 v75, v75, v77\n\t"
  "v_accvgpr_write_b32 a36, v75\n\t"
  "v_accvgpr_read_b32 v75, a37\n\t"
  "v_mul_f32_e32 v75, v75, v77\n\t"
  "v_accvgpr_write_b32 a37, v75\n\t"
  "v_accvgpr_read_b32 v75, a38\n\t"
  "v_mul_f32_e32 v75, v75, v77\n\t"
  "v_accvgpr_write_b32 a38, v75\n\t"
  "v_accvgpr_read_b32 v75, a39\n\t"
  "v_mul_f32_e32 v75, v75, v77\n\t"
  "v_accvgpr_write_b32 a39, v75\n\t"
  "v_accvgpr_read_b32 v75, a40\n\t"
  "v_mul_f32_e32 v75, v75, v77\n\t"
  "v_accvgpr_write_b32 a40, v75\n\t"
  "v_accvgpr_read_b32 v75, a41\n\t"
  "v_mul_f32_e32 v75, v75, v77\n\t"
  "v_accvgpr_write_b32 a41, v75\n\t"
  "v_accvgpr_read_b32 v75, a42\n\t"
  "v_mul_f32_e32 v75, v75, v77\n\t"
  "v_accvgpr_write_b32 a42, v75\n\t"
  "v_accvgpr_read_b32 v75, a43\n\t"
  "v_mul_f32_e32 v75, v75, v77\n\t"
  "v_accvgpr_write_b32 a43, v75\n\t"
  "v_accvgpr_read_b32 v75, a44\n\t"
  "v_mul_f32_e32 v75, v75, v77\n\t"
  "v_accvgpr_write_b32 a44, v75\n\t"
  "v_accvgpr_read_b32 v75, a45\n\t"
  "v_mul_f32_e32 v75, v75, v77\n\t"
  "v_accvgpr_write_b32 a45, v75\n\t"
  "v_accvgpr_read_b32 v75, a46\n\t"
  "v_mul_f32_e32 v75, v75, v77\n\t"
  "v_accvgpr_write_b32 a46, v75\n\t"
  "v_accvgpr_read_b32 v75, a47\n\t"
  "v_mul_f32_e32 v75, v75, v77\n\t"
  "v_accvgpr_write_b32 a47, v75\n\t"
  "v_accvgpr_read_b32 v75, a48\n\t"
  "v_mul_f32_e32 v75, v75, v77\n\t"
  "v_accvgpr_write_b32 a48, v75\n\t"
  "v_accvgpr_read_b32 v75, a49\n\t"
  "v_mul_f32_e32 v75, v75, v77\n\t"
  "v_accvgpr_write_b32 a49, v75\n\t"
  "v_accvgpr_read_b32 v75, a50\n\t"
  "v_mul_f32_e32 v75, v75, v77\n\t"
  "v_accvgpr_write_b32 a50, v75\n\t"
  "v_accvgpr_read_b32 v75, a51\n\t"
  "v_mul_f32_e32 v75, v75, v77\n\t"
  "v_accvgpr_write_b32 a51, v75\n\t"
  "v_accvgpr_read_b32 v75, a52\n\t"
  "v_mul_f32_e32 v75, v75, v77\n\t"
  "v_accvgpr_write_b32 a52, v75\n\t"
  "v_accvgpr_read_b32 v75, a53\n\t"
  "v_mul_f32_e32 v75, v75, v77\n\t"
  "v_accvgpr_write_b32 a53, v75\n\t"
  "v_accvgpr_read_b32 v75, a54\n\t"
  "v_mul_f32_e32 v75, v75, v77\n\t"
  "v_accvgpr_write_b32 a54, v75\n\t"
  "v_accvgpr_read_b32 v75, a55\n\t"
  "v_mul_f32_e32 v75, v75, v77\n\t"
  "v_accvgpr_write_b32 a55, v75\n\t"
  "v_accvgpr_read_b32 v75, a56\n\t"
  "v_mul_f32_e32 v75, v75, v77\n\t"
  "v_accvgpr_write_b32 a56, v75\n\t"
  "v_accvgpr_read_b32 v75, a57\n\t"
  "v_mul_f32_e32 v75, v75, v77\n\t"
  "v_accvgpr_write_b32 a57, v75\n\t"
  "v_accvgpr_read_b32 v75, a58\n\t"
  "v_mul_f32_e32 v75, v75, v77\n\t"
  "v_accvgpr_write_b32 a58, v75\n\t"
  "v_accvgpr_read_b32 v75, a59\n\t"
  "v_mul_f32_e32 v75, v75, v77\n\t"
  "v_accvgpr_write_b32 a59, v75\n\t"
  "v_accvgpr_read_b32 v75, a60\n\t"
  "v_mul_f32_e32 v75, v75, v77\n\t"
  "v_accvgpr_write_b32 a60, v75\n\t"
  "v_accvgpr_read_b32 v75, a61\n\t"
  "v_mul_f32_e32 v75, v75, v77\n\t"
  "v_accvgpr_write_b32 a61, v75\n\t"
  "v_accvgpr_read_b32 v75, a62\n\t"
  "v_mul_f32_e32 v75, v75, v77\n\t"
  "v_accvgpr_write_b32 a62, v75\n\t"
  "v_accvgpr_read_b32 v75, a63\n\t"
  "v_mul_f32_e32 v75, v75, v77\n\t"
  "v_accvgpr_write_b32 a63, v75\n\t"
  "v_accvgpr_read_b32 v75, a196\n\t"
  "v_mul_f32_e32 v75, v75, v77\n\t"
  "v_accvgpr_write_b32 a196, v75\n\t"
  "v_accvgpr_read_b32 v75, a197\n\t"
  "v_mul_f32_e32 v75, v75, v77\n\t"
  "v_accvgpr_write_b32 a197, v75\n\t"
  "v_accvgpr_read_b32 v75, a198\n\t"
  "v_mul_f32_e32 v75, v75, v77\n\t"
  "v_accvgpr_write_b32 a198, v75\n\t"
  "v_accvgpr_read_b32 v75, a199\n\t"
  "v_mul_f32_e32 v75, v75, v77\n\t"
  "v_accvgpr_write_b32 a199, v75\n\t"
  "v_max3_f32 v74, v48, v49, v50\n\t"
  "v_max3_f32 v74, v74, v51, v52\n\t"
  "v_max3_f32 v74, v74, v53, v54\n\t"
  "v_max_f32_e32 v74, v74, v55\n\t"
  "v_mov_b32_e32 v75, v74\n\t"
  "s_nop 1\n\t"
  "v_permlane16_swap_b32_e32 v74, v75\n\t"
  "v_max_f32_e32 v74, v74, v75\n\t"
  "v_mov_b32_e32 v75, v74\n\t"
  "s_nop 1\n\t"
  "v_permlane32_swap_b32_e32 v74, v75\n\t"
  "v_max_f32_e32 v74, v74, v75\n\t"
  "v_max_f32_e32 v74, v74, v72\n\t"
  "v_sub_f32_e32 v77, v72, v74\n\t"
  "v_exp_f32_e32 v77, v77\n\t"
  "v_mov_b32_e32 v72, v74\n\t"
  "v_sub_f32_e32 v48, v48, v74\n\t"
  "v_sub_f32_e32 v49, v49, v74\n\t"
  "v_sub_f32_e32 v50, v50, v74\n\t"
  "v_sub_f32_e32 v51, v51, v74\n\t"
  "v_sub_f32_e32 v52, v52, v74\n\t"
  "v_sub_f32_e32 v53, v53, v74\n\t"
  "v_sub_f32_e32 v54, v54, v74\n\t"
  "v_sub_f32_e32 v55, v55, v74\n\t"
  "v_exp_f32_e32 v48, v48\n\t"
  "v_exp_f32_e32 v49, v49\n\t"
  "v_exp_f32_e32 v50, v50\n\t"
  "v_exp_f32_e32 v51, v51\n\t"
  "v_exp_f32_e32 v52, v52\n\t"
  "v_exp_f32_e32 v53, v53\n\t"
  "v_exp_f32_e32 v54, v54\n\t"
  "v_exp_f32_e32 v55, v55\n\t"
  "v_cvt_pk_bf16_f32 v104, v48, v49\n\t"
  "v_cvt_pk_bf16_f32 v105, v50, v51\n\t"
  "v_cvt_pk_bf16_f32 v106, v52, v53\n\t"
  "v_cvt_pk_bf16_f32 v107, v54, v55\n\t"
  "v_accvgpr_read_b32 v75, a64\n\t"
  "v_mul_f32_e32 v75, v75, v77\n\t"
  "v_accvgpr_write_b32 a64, v75\n\t"
  "v_accvgpr_read_b32 v75, a65\n\t"
  "v_mul_f32_e32 v75, v75, v77\n\t"
  "v_accvgpr_write_b32 a65, v75\n\t"
  "v_accvgpr_read_b32 v75, a66\n\t"
  "v_mul_f32_e32 v75, v75, v77\n\t"
  "v_accvgpr_write_b32 a66, v75\n\t"
  "v_accvgpr_read_b32 v75, a67\n\t"
  "v_mul_f32_e32 v75, v75, v77\n\t"
  "v_accvgpr_write_b32 a67, v75\n\t"
  "v_accvgpr_read_b32 v75, a68\n\t"
  "v_mul_f32_e32 v75, v75, v77\n\t"
  "v_accvgpr_write_b32 a68, v75\n\t"
  "v_accvgpr_read_b32 v75, a69\n\t"
  "v_mul_f32_e32 v75, v75, v77\n\t"
  "v_accvgpr_write_b32 a69, v75\n\t"
  "v_accvgpr_read_b32 v75, a70\n\t"
  "v_mul_f32_e32 v75, v75, v77\n\t"
  "v_accvgpr_write_b32 a70, v75\n\t"
  "v_accvgpr_read_b32 v75, a71\n\t"
  "v_mul_f32_e32 v75, v75, v77\n\t"
  "v_accvgpr_write_b32 a71, v75\n\t"
  "v_accvgpr_read_b32 v75, a72\n\t"
  "v_mul_f32_e32 v75, v75, v77\n\t"
  "v_accvgpr_write_b32 a72, v75\n\t"
  "v_accvgpr_read_b32 v75, a73\n\t"
  "v_mul_f32_e32 v75, v75, v77\n\t"
  "v_accvgpr_write_b32 a73, v75\n\t"
  "v_accvgpr_read_b32 v75, a74\n\t"
  "v_mul_f32_e32 v75, v75, v77\n\t"
  "v_accvgpr_write_b32 a74, v75\n\t"
  "v_accvgpr_read_b32 v75, a75\n\t"
  "v_mul_f32_e32 v75, v75, v77\n\t"
  "v_accvgpr_write_b32 a75, v75\n\t"
  "v_accvgpr_read_b32 v75, a76\n\t"
  "v_mul_f32_e32 v75, v75, v77\n\t"
  "v_accvgpr_write_b32 a76, v75\n\t"
  "v_accvgpr_read_b32 v75, a77\n\t"
  "v_mul_f32_e32 v75, v75, v77\n\t"
  "v_accvgpr_write_b32 a77, v75\n\t"
  "v_accvgpr_read_b32 v75, a78\n\t"
  "v_mul_f32_e32 v75, v75, v77\n\t"
  "v_accvgpr_write_b32 a78, v75\n\t"
  "v_accvgpr_read_b32 v75, a79\n\t"
  "v_mul_f32_e32 v75, v75, v77\n\t"
  "v_accvgpr_write_b32 a79, v75\n\t"
  "v_accvgpr_read_b32 v75, a80\n\t"
  "v_mul_f32_e32 v75, v75, v77\n\t"
  "v_accvgpr_write_b32 a80, v75\n\t"
  "v_accvgpr_read_b32 v75, a81\n\t"
  "v_mul_f32_e32 v75, v75, v77\n\t"
  "v_accvgpr_write_b32 a81, v75\n\t"
  "v_accvgpr_read_b32 v75, a82\n\t"
  "v_mul_f32_e32 v75, v75, v77\n\t"
  "v_accvgpr_write_b32 a82, v75\n\t"
  "v_accvgpr_read_b32 v75, a83\n\t"
  "v_mul_f32_e32 v75, v75, v77\n\t"
  "v_accvgpr_write_b32 a83, v75\n\t"
  "v_accvgpr_read_b32 v75, a84\n\t"
  "v_mul_f32_e32 v75, v75, v77\n\t"
  "v_accvgpr_write_b32 a84, v75\n\t"
  "v_accvgpr_read_b32 v75, a85\n\t"
  "v_mul_f32_e32 v75, v75, v77\n\t"
  "v_accvgpr_write_b32 a85, v75\n\t"
  "v_accvgpr_read_b32 v75, a86\n\t"
  "v_mul_f32_e32 v75, v75, v77\n\t"
  "v_accvgpr_write_b32 a86, v75\n\t"
  "v_accvgpr_read_b32 v75, a87\n\t"
  "v_mul_f32_e32 v75, v75, v77\n\t"
  "v_accvgpr_write_b32 a87, v75\n\t"
  "v_accvgpr_read_b32 v75, a88\n\t"
  "v_mul_f32_e32 v75, v75, v77\n\t"
  "v_accvgpr_write_b32 a88, v75\n\t"
  "v_accvgpr_read_b32 v75, a89\n\t"
  "v_mul_f32_e32 v75, v75, v77\n\t"
  "v_accvgpr_write_b32 a89, v75\n\t"
  "v_accvgpr_read_b32 v75, a90\n\t"
  "v_mul_f32_e32 v75, v75, v77\n\t"
  "v_accvgpr_write_b32 a90, v75\n\t"
  "v_accvgpr_read_b32 v75, a91\n\t"
  "v_mul_f32_e32 v75, v75, v77\n\t"
  "v_accvgpr_write_b32 a91, v75\n\t"
  "v_accvgpr_read_b32 v75, a92\n\t"
  "v_mul_f32_e32 v75, v75, v77\n\t"
  "v_accvgpr_write_b32 a92, v75\n\t"
  "v_accvgpr_read_b32 v75, a93\n\t"
  "v_mul_f32_e32 v75, v75, v77\n\t"
  "v_accvgpr_write_b32 a93, v75\n\t"
  "v_accvgpr_read_b32 v75, a94\n\t"
  "v_mul_f32_e32 v75, v75, v77\n\t"
  "v_accvgpr_write_b32 a94, v75\n\t"
  "v_accvgpr_read_b32 v75, a95\n\t"
  "v_mul_f32_e32 v75, v75, v77\n\t"
  "v_accvgpr_write_b32 a95, v75\n\t"
  "v_accvgpr_read_b32 v75, a200\n\t"
  "v_mul_f32_e32 v75, v75, v77\n\t"
  "v_accvgpr_write_b32 a200, v75\n\t"
  "v_accvgpr_read_b32 v75, a201\n\t"
  "v_mul_f32_e32 v75, v75, v77\n\t"
  "v_accvgpr_write_b32 a201, v75\n\t"
  "v_accvgpr_read_b32 v75, a202\n\t"
  "v_mul_f32_e32 v75, v75, v77\n\t"
  "v_accvgpr_write_b32 a202, v75\n\t"
  "v_accvgpr_read_b32 v75, a203\n\t"
  "v_mul_f32_e32 v75, v75, v77\n\t"
  "v_accvgpr_write_b32 a203, v75\n\t"
  "v_max3_f32 v74, v56, v57, v58\n\t"
  "v_max3_f32 v74, v74, v59, v60\n\t"
  "v_max3_f32 v74, v74, v61, v62\n\t"
  "v_max_f32_e32 v74, v74, v63\n\t"
  "v_mov_b32_e32 v75, v74\n\t"
  "s_nop 1\n\t"
  "v_permlane16_swap_b32_e32 v74, v75\n\t"
  "v_max_f32_e32 v74, v74, v75\n\t"
  "v_mov_b32_e32 v75, v74\n\t"
  "s_nop 1\n\t"
  "v_permlane32_swap_b32_e32 v74, v75\n\t"
  "v_max_f32_e32 v74, v74, v75\n\t"
  "v_max_f32_e32 v74, v74, v73\n\t"
  "v_sub_f32_e32 v77, v73, v74\n\t"
  "v_exp_f32_e32 v77, v77\n\t"
  "v_mov_b32_e32 v73, v74\n\t"
  "v_sub_f32_e32 v56, v56, v74\n\t"
  "v_sub_f32_e32 v57, v57, v74\n\t"
  "v_sub_f32_e32 v58, v58, v74\n\t"
  "v_sub_f32_e32 v59, v59, v74\n\t"
  "v_sub_f32_e32 v60, v60, v74\n\t"
  "v_sub_f32_e32 v61, v61, v74\n\t"
  "v_sub_f32_e32 v62, v62, v74\n\t"
  "v_sub_f32_e32 v63, v63, v74\n\t"
  "v_exp_f32_e32 v56, v56\n\t"
  "v_exp_f32_e32 v57, v57\n\t"
  "v_exp_f32_e32 v58, v58\n\t"
  "v_exp_f32_e32 v59, v59\n\t"
  "v_exp_f32_e32 v60, v60\n\t"
  "v_exp_f32_e32 v61, v61\n\t"
  "v_exp_f32_e32 v62, v62\n\t"
  "v_exp_f32_e32 v63, v63\n\t"
  "v_cvt_pk_bf16_f32 v108, v56, v57\n\t"
  "v_cvt_pk_bf16_f32 v109, v58, v59\n\t"
  "v_cvt_pk_bf16_f32 v110, v60, v61\n\t"
  "v_cvt_pk_bf16_f32 v111, v62, v63\n\t"
  "v_accvgpr_read_b32 v75, a96\n\t"
  "v_mul_f32_e32 v75, v75, v77\n\t"
  "v_accvgpr_write_b32 a96, v75\n\t"
  "v_accvgpr_read_b32 v75, a97\n\t"
  "v_mul_f32_e32 v75, v75, v77\n\t"
  "v_accvgpr_write_b32 a97, v75\n\t"
  "v_accvgpr_read_b32 v75, a98\n\t"
  "v_mul_f32_e32 v75, v75, v77\n\t"
  "v_accvgpr_write_b32 a98, v75\n\t"
  "v_accvgpr_read_b32 v75, a99\n\t"
  "v_mul_f32_e32 v75, v75, v77\n\t"
  "v_accvgpr_write_b32 a99, v75\n\t"
  "v_accvgpr_read_b32 v75, a100\n\t"
  "v_mul_f32_e32 v75, v75, v77\n\t"
  "v_accvgpr_write_b32 a100, v75\n\t"
  "v_accvgpr_read_b32 v75, a101\n\t"
  "v_mul_f32_e32 v75, v75, v77\n\t"
  "v_accvgpr_write_b32 a101, v75\n\t"
  "v_accvgpr_read_b32 v75, a102\n\t"
  "v_mul_f32_e32 v75, v75, v77\n\t"
  "v_accvgpr_write_b32 a102, v75\n\t"
  "v_accvgpr_read_b32 v75, a103\n\t"
  "v_mul_f32_e32 v75, v75, v77\n\t"
  "v_accvgpr_write_b32 a103, v75\n\t"
  "v_accvgpr_read_b32 v75, a104\n\t"
  "v_mul_f32_e32 v75, v75, v77\n\t"
  "v_accvgpr_write_b32 a104, v75\n\t"
  "v_accvgpr_read_b32 v75, a105\n\t"
  "v_mul_f32_e32 v75, v75, v77\n\t"
  "v_accvgpr_write_b32 a105, v75\n\t"
  "v_accvgpr_read_b32 v75, a106\n\t"
  "v_mul_f32_e32 v75, v75, v77\n\t"
  "v_accvgpr_write_b32 a106, v75\n\t"
  "v_accvgpr_read_b32 v75, a107\n\t"
  "v_mul_f32_e32 v75, v75, v77\n\t"
  "v_accvgpr_write_b32 a107, v75\n\t"
  "v_accvgpr_read_b32 v75, a108\n\t"
  "v_mul_f32_e32 v75, v75, v77\n\t"
  "v_accvgpr_write_b32 a108, v75\n\t"
  "v_accvgpr_read_b32 v75, a109\n\t"
  "v_mul_f32_e32 v75, v75, v77\n\t"
  "v_accvgpr_write_b32 a109, v75\n\t"
  "v_accvgpr_read_b32 v75, a110\n\t"
  "v_mul_f32_e32 v75, v75, v77\n\t"
  "v_accvgpr_write_b32 a110, v75\n\t"
  "v_accvgpr_read_b32 v75, a111\n\t"
  "v_mul_f32_e32 v75, v75, v77\n\t"
  "v_accvgpr_write_b32 a111, v75\n\t"
  "v_accvgpr_read_b32 v75, a112\n\t"
  "v_mul_f32_e32 v75, v75, v77\n\t"
  "v_accvgpr_write_b32 a112, v75\n\t"
  "v_accvgpr_read_b32 v75, a113\n\t"
  "v_mul_f32_e32 v75, v75, v77\n\t"
  "v_accvgpr_write_b32 a113, v75\n\t"
  "v_accvgpr_read_b32 v75, a114\n\t"
  "v_mul_f32_e32 v75, v75, v77\n\t"
  "v_accvgpr_write_b32 a114, v75\n\t"
  "v_accvgpr_read_b32 v75, a115\n\t"
  "v_mul_f32_e32 v75, v75, v77\n\t"
  "v_accvgpr_write_b32 a115, v75\n\t"
  "v_accvgpr_read_b32 v75, a116\n\t"
  "v_mul_f32_e32 v75, v75, v77\n\t"
  "v_accvgpr_write_b32 a116, v75\n\t"
  "v_accvgpr_read_b32 v75, a117\n\t"
  "v_mul_f32_e32 v75, v75, v77\n\t"
  "v_accvgpr_write_b32 a117, v75\n\t"
  "v_accvgpr_read_b32 v75, a118\n\t"
  "v_mul_f32_e32 v75, v75, v77\n\t"
  "v_accvgpr_write_b32 a118, v75\n\t"
  "v_accvgpr_read_b32 v75, a119\n\t"
  "v_mul_f32_e32 v75, v75, v77\n\t"
  "v_accvgpr_write_b32 a119, v75\n\t"
  "v_accvgpr_read_b32 v75, a120\n\t"
  "v_mul_f32_e32 v75, v75, v77\n\t"
  "v_accvgpr_write_b32 a120, v75\n\t"
  "v_accvgpr_read_b32 v75, a121\n\t"
  "v_mul_f32_e32 v75, v75, v77\n\t"
  "v_accvgpr_write_b32 a121, v75\n\t"
  "v_accvgpr_read_b32 v75, a122\n\t"
  "v_mul_f32_e32 v75, v75, v77\n\t"
  "v_accvgpr_write_b32 a122, v75\n\t"
  "v_accvgpr_read_b32 v75, a123\n\t"
  "v_mul_f32_e32 v75, v75, v77\n\t"
  "v_accvgpr_write_b32 a123, v75\n\t"
  "v_accvgpr_read_b32 v75, a124\n\t"
  "v_mul_f32_e32 v75, v75, v77\n\t"
  "v_accvgpr_write_b32 a124, v75\n\t"
  "v_accvgpr_read_b32 v75, a125\n\t"
  "v_mul_f32_e32 v75, v75, v77\n\t"
  "v_accvgpr_write_b32 a125, v75\n\t"
  "v_accvgpr_read_b32 v75, a126\n\t"
  "v_mul_f32_e32 v75, v75, v77\n\t"
  "v_accvgpr_write_b32 a126, v75\n\t"
  "v_accvgpr_read_b32 v75, a127\n\t"
  "v_mul_f32_e32 v75, v75, v77\n\t"
  "v_accvgpr_write_b32 a127, v75\n\t"
  "v_accvgpr_read_b32 v75, a204\n\t"
  "v_mul_f32_e32 v75, v75, v77\n\t"
  "v_accvgpr_write_b32 a204, v75\n\t"
  "v_accvgpr_read_b32 v75, a205\n\t"
  "v_mul_f32_e32 v75, v75, v77\n\t"
  "v_accvgpr_write_b32 a205, v75\n\t"
  "v_accvgpr_read_b32 v75, a206\n\t"
  "v_mul_f32_e32 v75, v75, v77\n\t"
  "v_accvgpr_write_b32 a206, v75\n\t"
  "v_accvgpr_read_b32 v75, a207\n\t"
  "v_mul_f32_e32 v75, v75, v77\n\t"
  "v_accvgpr_write_b32 a207, v75\n\t"
  "s_waitcnt lgkmcnt(0)\n\t"
  "v_mfma_f32_16x16x32_bf16 a[0:3], a[160:163], v[96:99], a[0:3]\n\t"
  "v_mfma_f32_16x16x32_bf16 a[4:7], a[164:167], v[96:99], a[4:7]\n\t"
  "v_mfma_f32_16x16x32_bf16 a[8:11], a[168:171], v[96:99], a[8:11]\n\t"
  "v_mfma_f32_16x16x32_bf16 a[12:15], a[172:175], v[96:99], a[12:15]\n\t"
  "v_mfma_f32_16x16x32_bf16 a[16:19], a[176:179], v[96:99], a[16:19]\n\t"
  "v_mfma_f32_16x16x32_bf16 a[20:23], a[180:183], v[96:99], a[20:23]\n\t"
  "v_mfma_f32_16x16x32_bf16 a[24:27], a[184:187], v[96:99], a[24:27]\n\t"
  "v_mfma_f32_16x16x32_bf16 a[28:31], a[188:191], v[96:99], a[28:31]\n\t"
  "v_mfma_f32_16x16x32_bf16 a[192:195], a[208:211], v[96:99], a[192:195]\n\t"
  "v_mfma_f32_16x16x32_bf16 a[32:35], a[160:163], v[100:103], a[32:35]\n\t"
  "v_mfma_f32_16x16x32_bf16 a[36:39], a[164:167], v[100:103], a[36:39]\n\t"
  "v_mfma_f32_16x16x32_bf16 a[40:43], a[168:171], v[100:103], a[40:43]\n\t"
  "v_mfma_f32_16x16x32_bf16 a[44:47], a[172:175], v[100:103], a[44:47]\n\t"
  "v_mfma_f32_16x16x32_bf16 a[48:51], a[176:179], v[100:103], a[48:51]\n\t"
  "v_mfma_f32_16x16x32_bf16 a[52:55], a[180:183], v[100:103], a[52:55]\n\t"
  "v_mfma_f32_16x16x32_bf16 a[56:59], a[184:187], v[100:103], a[56:59]\n\t"
  "v_mfma_f32_16x16x32_bf16 a[60:63], a[188:191], v[100:103], a[60:63]\n\t"
  "v_mfma_f32_16x16x32_bf16 a[196:199], a[208:211], v[100:103], a[196:199]\n\t"
  "v_mfma_f32_16x16x32_bf16 a[64:67], a[160:163], v[104:107], a[64:67]\n\t"
  "v_mfma_f32_16x16x32_bf16 a[68:71], a[164:167], v[104:107], a[68:71]\n\t"
  "v_mfma_f32_16x16x32_bf16 a[72:75], a[168:171], v[104:107], a[72:75]\n\t"
  "v_mfma_f32_16x16x32_bf16 a[76:79], a[172:175], v[104:107], a[76:79]\n\t"
  "v_mfma_f32_16x16x32_bf16 a[80:83], a[176:179], v[104:107], a[80:83]\n\t"
  "v_mfma_f32_16x16x32_bf16 a[84:87], a[180:183], v[104:107], a[84:87]\n\t"
  "v_mfma_f32_16x16x32_bf16 a[88:91], a[184:187], v[104:107], a[88:91]\n\t"
  "v_mfma_f32_16x16x32_bf16 a[92:95], a[188:191], v[104:107], a[92:95]\n\t"
  "v_mfma_f32_16x16x32_bf16 a[200:203], a[208:211], v[104:107], a[200:203]\n\t"
  "v_mfma_f32_16x16x32_bf16 a[96:99], a[160:163], v[108:111], a[96:99]\n\t"
  "v_mfma_f32_16x16x32_bf16 a[100:103], a[164:167], v[108:111], a[100:103]\n\t"
  "v_mfma_f32_16x16x32_bf16 a[104:107], a[168:171], v[108:111], a[104:107]\n\t"
  "v_mfma_f32_16x16x32_bf16 a[108:111], a[172:175], v[108:111], a[108:111]\n\t"
  "v_mfma_f32_16x16x32_bf16 a[112:115], a[176:179], v[108:111], a[112:115]\n\t"
  "v_mfma_f32_16x16x32_bf16 a[116:119], a[180:183], v[108:111], a[116:119]\n\t"
  "v_mfma_f32_16x16x32_bf16 a[120:123], a[184:187], v[108:111], a[120:123]\n\t"
  "v_mfma_f32_16x16x32_bf16 a[124:127], a[188:191], v[108:111], a[124:127]\n\t"
  "v_mfma_f32_16x16x32_bf16 a[204:207], a[208:211], v[108:111], a[204:207]\n\t"
  "s_nop 7\n\t"
  "s_nop 7\n\t"
  "s_add_u32 s60, s60, 0x2000\n\t"
  "s_sub_u32 s61, s61, 1\n\t"
  "s_cmp_lg_u32 s61, 0\n\t"
  "s_cbranch_scc1 L_fbloop_%=\n\t"
  "s_branch L_fbret_%=\n\t"
  "L_done_%=:\n\t"
  :: "{s[64:65]}"(ws), "{s[66:67]}"(w1), "{s[68:69]}"(wq), "{s[70:71]}"(kc), "{s[72:73]}"(vc), "{s[74:75]}"(w2), "{s[76:77]}"(wo),
     "s"(w), "s"(h), "s"(qb), "s"(pfo), "s"(j), "s"(qb128), "s"(nb128)
  : "memory", "vcc", "scc", "m0", "exec", "v0","v1","v2","v3","v4","v5","v6","v7","v8","v9","v10","v11","v12","v13","v14","v15","v16","v17","v18","v19","v20","v21","v22","v23","v24","v25","v26","v27","v28","v29","v30","v31","v32","v33","v34","v35","v36","v37","v38","v39","v40","v41","v42","v43","v44","v45","v46","v47","v48","v49","v50","v51","v52","v53","v54","v55","v56","v57","v58","v59","v60","v61","v62","v63","v64","v65","v66","v67","v68","v69","v70","v71","v72","v73","v74","v75","v76","v77","v78","v79","v80","v81","v82","v83","v84","v85","v86","v87","v88","v89","v90","v91","v92","v93","v94","v95","v96","v97","v98","v99","v100","v101","v102","v103","v104","v105","v106","v107","v108","v109","v110","v111","v112","v113","v114","v115","v116","v117","v118","v119","v120","v121","v122","v123","v124","v125","v126","v127","v128","v129","v130","v131","v132","v133","v134","v135","v136","v137","v138","v139","v140","v141","v142","v143","v144","v145","v146","v147","v148","v149","v150","v151","v152","v153","v154","v155","v156","v157","v158","v159","v160","v161","v162","v163","v164","v165","v166","v167","v168","v169","v170","v171","v172","v173","v174","v175","v176","v177","v178","v179","v180","v181","v182","v183","v184","v185","v186","v187","v188","v189","v190","v191","v192","v193","v194","v195","v196","v197","v198","v199","v200","v201","v202","v203","v204","v205","v206","v207","v208","v209","v210","v211","v212","v213","v214","v215","v216","v217","v218","v219","v220","v221","v222","v223","v224","v225","v226","v227","v228","v229","v230","v231","v232","v233","v234","v235","v236","v237","v238","v239","v240","v241","v242","v243","v244","v245","v246","v247","v248","v249","v250","v251","v252","v253","v254","v255","a0","a1","a2","a3","a4","a5","a6","a7","a8","a9","a10","a11","a12","a13","a14","a15","a16","a17","a18","a19","a20","a21","a22","a23","a24","a25","a26","a27","a28","a29","a30","a31","a32","a33","a34","a35","a36","a37","a38","a39","a40","a41","a42","a43","a44","a45","a46","a47","a48","a49","a50","a51","a52","a53","a54","a55","a56","a57","a58","a59","a60","a61","a62","a63","a64","a65","a66","a67","a68","a69","a70","a71","a72","a73","a74","a75","a76","a77","a78","a79","a80","a81","a82","a83","a84","a85","a86","a87","a88","a89","a90","a91","a92","a93","a94","a95","a96","a97","a98","a99","a100","a101","a102","a103","a104","a105","a106","a107","a108","a109","a110","a111","a112","a113","a114","a115","a116","a117","a118","a119","a120","a121","a122","a123","a124","a125","a126","a127","a128","a129","a130","a131","a132","a133","a134","a135","a136","a137","a138","a139","a140","a141","a142","a143","a144","a145","a146","a147","a148","a149","a150","a151","a152","a153","a154","a155","a156","a157","a158","a159","a160","a161","a162","a163","a164","a165","a166","a167","a168","a169","a170","a171","a172","a173","a174","a175","a176","a177","a178","a179","a180","a181","a182","a183","a184","a185","a186","a187","a188","a189","a190","a191","a192","a193","a194","a195","a196","a197","a198","a199","a200","a201","a202","a203","a204","a205","a206","a207","a208","a209","a210","a211","a212","a213","a214","a215","a216","a217","a218","a219","a220","a221","a222","a223","a224","a225","a226","a227","a228","a229","a230","a231","a232","a233","a234","a235","a236","a237","a238","a239","a240","a241","a242","a243","a244","a245","a246","a247","a248","a249","a250","a251","a252","a253","a254","a255","s8","s9","s10","s11","s12","s13","s14","s15","s16","s17","s18","s19","s20","s21","s22","s23","s24","s25","s26","s27","s28","s29","s30","s31","s32","s33","s34","s35","s36","s37","s38","s39","s40","s41","s42","s43","s44","s45","s46","s47","s48","s49","s50","s51","s52","s53","s54","s55","s56","s57","s58","s59","s60","s61","s62","s63");

  typedef __attribute__((address_space(1))) unsigned gu32;
  int lane;
  asm volatile("v_mbcnt_lo_u32_b32 %0, -1, 0 \n v_mbcnt_hi_u32_b32 %0, -1, %0" : "=v"(lane));
  const int q2 = w & 1, wn = w >> 1;
  const int row = qb128 * 128 + j * 32 + q2 * 16 + (lane & 15), col = nb128 * 128 + wn * 64 + 4 * (lane >> 4);
  u32x2 xv[4], bv[4];
#pragma unroll
  for (int nb = 0; nb < 4; ++nb) { xv[nb] = *reinterpret_cast<const u32x2*>(x + (size_t)row * 512 + col + 16 * nb); bv[nb] = *reinterpret_cast<const u32x2*>(b2 + col + 16 * nb); }
  __builtin_amdgcn_s_barrier();
  gu32* flag = (gu32*)(ws + WS_FLAG) + tile * 64;
  if (w == 0 && lane == 0) __hip_atomic_fetch_add(flag, 1u, __ATOMIC_RELAXED, __HIP_MEMORY_SCOPE_AGENT);
  unsigned sl_ = 0x00003f80u, sh_ = 0x3f800000u;
  asm volatile("" : "+s"(sl_), "+s"(sh_));
  const bf16x2 sel_lo = __builtin_bit_cast(bf16x2, sl_), sel_hi = __builtin_bit_cast(bf16x2, sh_);
#define PICK(word, hi, acc) __builtin_amdgcn_fdot2_f32_bf16(__builtin_bit_cast(bf16x2, (unsigned)(word)), (hi) ? sel_hi : sel_lo, (acc), false)
  float gs[16], ss[16];
  {
    u32x4 own[2][2];
#pragma unroll
    for (int m = 0; m < 2; ++m)
#pragma unroll
      for (int h = 0; h < 2; ++h) own[m][h] = *reinterpret_cast<const u32x4*>(lds + 131072 + m * 8192 + q2 * 4096 + wn * 2048 + h * 1024 + lane * 16);
#pragma unroll
    for (int i = 0; i < 16; ++i) {
      const int nb = i >> 2, e = i & 3;
      gs[i] = PICK(own[0][nb >> 1][2 * (nb & 1) + (e >> 1)], e & 1, PICK(bv[nb][e >> 1], e & 1, 0.f));
      ss[i] = PICK(own[1][nb >> 1][2 * (nb & 1) + (e >> 1)], e & 1, 0.f);
    }
  }
  if (w == 0) {
    for (unsigned spins = 0; spins < (1u << 18); ++spins) {
      if (__builtin_amdgcn_readfirstlane(__hip_atomic_load(flag, __ATOMIC_RELAXED, __HIP_MEMORY_SCOPE_AGENT)) >= 4u) break;
      __builtin_amdgcn_s_sleep(1);
    }
  }
  asm volatile("" ::: "memory");
  __builtin_amdgcn_s_barrier();
  asm volatile("" ::: "memory");

  {
    u32x4 pv[3][2][2];
    const unsigned voff = lane * 16 + q2 * 4096 + wn * 2048 + j * 65536;
    const __amdgpu_buffer_rsrc_t rs = __builtin_amdgcn_make_buffer_rsrc(ws + WS_PART + (size_t)tile * 262144, (short)0, 262144, 0x00020000);
#pragma unroll
    for (int d = 0; d < 3; ++d) {
      const int s = (j + d + 1) & 3;
#pragma unroll
      for (int m = 0; m < 2; ++m)
#pragma unroll
        for (int h = 0; h < 2; ++h) pv[d][m][h] = __builtin_amdgcn_raw_buffer_load_b128(rs, voff + h * 1024 + m * 8192, s * 16384, 16);
    }
    __builtin_amdgcn_sched_barrier(0);
#pragma unroll
    for (int nb = 0; nb < 4; ++nb) {
      float r[4];
#pragma unroll
      for (int e = 0; e < 4; ++e) {
        float z = gs[4 * nb + e], sv = ss[4 * nb + e];
#pragma unroll
        for (int d = 0; d < 3; ++d) {
          z = PICK(pv[d][0][nb >> 1][2 * (nb & 1) + (e >> 1)], e & 1, z);
          sv = PICK(pv[d][1][nb >> 1][2 * (nb & 1) + (e >> 1)], e & 1, sv);
        }
        const float xx = __uint_as_float((e & 1) ? (xv[nb][e >> 1] & 0xffff0000u) : (xv[nb][e >> 1] << 16));
        r[e] = xx + sv * (z * __builtin_amdgcn_rcpf(1.f + __builtin_amdgcn_exp2f(z * -1.4426950408889634f)));
      }
      u32x2 o = {pk2(r[0], r[1]), pk2(r[2], r[3])};
      *reinterpret_cast<u32x2*>(out + (size_t)row * 512 + col + 16 * nb) = o;
    }
  }
#undef PICK

  asm volatile("mega_code_end:");
}

extern "C" void kernel_launch(void* const* d_in, const int* in_sizes, int n_in, void* d_out, int out_size, void* d_ws, size_t ws_size, hipStream_t stream) {
  const u16* x = (const u16*)d_in[0]; const u16* kc = (const u16*)d_in[1]; const u16* vc = (const u16*)d_in[2];
  const u16* wq = (const u16*)d_in[3]; const u16* wo = (const u16*)d_in[4];
  const u16* w1 = (const u16*)d_in[6]; const u16* b1 = (const u16*)d_in[7]; const u16* w2 = (const u16*)d_in[8]; const u16* b2 = (const u16*)d_in[9];
  char* ws = (char*)d_ws;
  (void)hipFuncSetAttribute((const void*)mega_kernel, hipFuncAttributeMaxDynamicSharedMemorySize, 163840);
  hipLaunchKernelGGL(prep_kernel, dim3(A_LN_BLOCKS + A_CS_BLOCKS + A_PF_BLOCKS), dim3(256), 0, stream, x, w1, b1, wq, (u16*)(ws + WS_XN), (float*)(ws + WS_ST), (float*)(ws + WS_CB), (unsigned*)(ws + WS_FLAG));
  hipLaunchKernelGGL(mega_kernel, dim3(256), dim3(256), 163840, stream, wq, kc, vc, w1, w2, wo, ws, x, b2, (u16*)d_out);
}
```
